```python
import torch

def _softmax(x, axis=-1):
    return torch.softmax(x, dim=axis)
def _gelu(x):
    return torch.nn.functional.gelu(x, approximate="tanh")
def _silu(x):
    return torch.nn.functional.silu(x)
def _sigmoid(x):
    return torch.sigmoid(x)
def _layernorm(x, axis=-1, eps=1e-5):
    m = x.mean(dim=axis, keepdim=True)
    v = x.var(dim=axis, keepdim=True, unbiased=False)
    return (x - m) / torch.sqrt(v + eps)
def _scatter_add(tgt, idx, src, axis=0):
    return tgt.clone().index_add_(axis, idx, src)
def _segment_sum(src, seg, n):
    out = torch.zeros((n,) + src.shape[1:], dtype=src.dtype)
    return out.index_add_(0, seg, src)
def _pad(x, pw):
    flat = tuple(v for pair in reversed(pw) for v in pair)
    return torch.nn.functional.pad(x, flat)

kernel_name = 'concat_heads__mha_block_cached__mha_block_cached__near_ridge'

def setup_inputs():

    _w = (lambda a: (lambda t: t.to(torch.bfloat16) if t.is_floating_point() else t)(torch.as_tensor(a)))
    def _A_setup():
        B, Sq, D = 16, 160, 512
        H, Sk, Dh = 32, 512, 128
        return {
            "x": _w((lambda *s: torch.randn(*s, dtype=torch.bfloat16) * 0.8)(B, Sq, D)),
            "k_cache": _w((lambda *s: torch.randn(*s, dtype=torch.bfloat16) * 0.8)(B, H, Sk, Dh)),
            "v_cache": _w((lambda *s: torch.randn(*s, dtype=torch.bfloat16) * 0.05)(B, H, Sk, Dh)),
            "wq": _w((lambda *s: torch.randn(*s, dtype=torch.bfloat16) * 0.8)(D, H * Dh) * (D ** -0.5)),
            "wo": _w((lambda *s: torch.randn(*s, dtype=torch.bfloat16) * 0.05)(H * Dh, D) * ((H * Dh) ** -0.5)),
        }
    _A_in = _A_setup()

    def _B_setup():
        B, Sq, D = 16, 160, 512
        H, Sk, Dh = 32, 512, 128
        return {
            "x": _w((lambda *s: torch.randn(*s, dtype=torch.bfloat16) * 0.8)(B, Sq, D)),
            "k_cache": _w((lambda *s: torch.randn(*s, dtype=torch.bfloat16) * 0.8)(B, H, Sk, Dh)),
            "v_cache": _w((lambda *s: torch.randn(*s, dtype=torch.bfloat16) * 0.05)(B, H, Sk, Dh)),
            "wq": _w((lambda *s: torch.randn(*s, dtype=torch.bfloat16) * 0.8)(D, H * Dh) * (D ** -0.5)),
            "wo": _w((lambda *s: torch.randn(*s, dtype=torch.bfloat16) * 0.05)(H * Dh, D) * ((H * Dh) ** -0.5)),
        }
    _B_in = _B_setup()

    return {
        "A_x": _A_in["x"],
        "A_k_cache": _A_in["k_cache"],
        "A_v_cache": _A_in["v_cache"],
        "A_wq": _A_in["wq"],
        "A_wo": _A_in["wo"],
        "B_x": _B_in["x"],
        "B_k_cache": _B_in["k_cache"],
        "B_v_cache": _B_in["v_cache"],
        "B_wq": _B_in["wq"],
        "B_wo": _B_in["wo"],
    }

def reference(A_x, A_k_cache, A_v_cache, A_wq, A_wo, B_x, B_k_cache, B_v_cache, B_wq, B_wo):
    def _A_ref(x, k_cache, v_cache, wq, wo):
        B, Sq, D = x.shape
        H, Dh = 32, 128
        q = (lambda x, *d: x.permute(*d))((x @ wq).reshape(B, Sq, H, Dh), 0, 2, 1, 3)
        scores = torch.einsum("bhqd,bhkd->bhqk", q, k_cache) * (Dh ** -0.5)
        p = _softmax(scores, axis=-1)
        o = torch.einsum("bhqk,bhkd->bhqd", p, v_cache)
        return (lambda x, *d: x.permute(*d))(o, 0, 2, 1, 3).reshape(B, Sq, H * Dh) @ wo

    def _B_ref(x, k_cache, v_cache, wq, wo):
        B, Sq, D = x.shape
        H, Dh = 32, 128
        q = (lambda x, *d: x.permute(*d))((x @ wq).reshape(B, Sq, H, Dh), 0, 2, 1, 3)
        scores = torch.einsum("bhqd,bhkd->bhqk", q, k_cache) * (Dh ** -0.5)
        p = _softmax(scores, axis=-1)
        o = torch.einsum("bhqk,bhkd->bhqd", p, v_cache)
        return (lambda x, *d: x.permute(*d))(o, 0, 2, 1, 3).reshape(B, Sq, H * Dh) @ wo

    a = _A_ref(A_x, A_k_cache, A_v_cache, A_wq, A_wo)
    b = _B_ref(B_x, B_k_cache, B_v_cache, B_wq, B_wo)
    return torch.concatenate([a, b], axis=-1)
```

```cpp
#include <hip/hip_runtime.h>
#include <hip/hip_bf16.h>
typedef __attribute__((ext_vector_type(8))) short bf16x8;
typedef __attribute__((ext_vector_type(16))) float f32x16;
typedef unsigned short u16;
#define DI __device__ __forceinline__
#define MFMA(a,b,c) __builtin_amdgcn_mfma_f32_32x32x16_bf16((a),(b),(c),0,0,0)
DI u16 f2bf(float x){ __hip_bfloat16 b = __float2bfloat16(x); return *(u16*)&b; }
DI int crow(int reg,int h){ return (reg&3)+8*(reg>>2)+4*h; }

__global__ __launch_bounds__(64) void gemm_simple(const u16* A, int lda, const u16* B, int ldb, u16* C, int ldc, int K){
  int lane=threadIdx.x, r=lane&31, h=lane>>5;
  int n0=blockIdx.x*32, m0=blockIdx.y*32;
  f32x16 acc; for(int i=0;i<16;i++) acc[i]=0.f;
  for(int k=0;k<K;k+=16){
    bf16x8 a=*(const bf16x8*)(A+(size_t)(m0+r)*lda+k+8*h);
    bf16x8 b; for(int j=0;j<8;j++) b[j]=B[(size_t)(k+8*h+j)*ldb+n0+r];
    acc=MFMA(a,b,acc);
  }
  for(int i=0;i<16;i++) C[(size_t)(m0+crow(i,h))*ldc+n0+r]=f2bf(acc[i]);
}
__global__ __launch_bounds__(64) void attn_simple(const u16* Q, const u16* Kc, const u16* Vc, u16* O){
  int lane=threadIdx.x, r=lane&31, hh=lane>>5;
  int rb=blockIdx.x, h=blockIdx.y, b=blockIdx.z;
  const u16* q=Q+(size_t)(b*160+rb*32+r)*4096+h*128;
  const u16* kp=Kc+(size_t)(b*32+h)*512*128;
  const u16* vp=Vc+(size_t)(b*32+h)*512*128;
  bf16x8 qf[8];
  #pragma unroll
  for(int s=0;s<8;s++) qf[s]=*(const bf16x8*)(q+16*s+8*hh);
  f32x16 o[4];
  #pragma unroll
  for(int d=0;d<4;d++) for(int i=0;i<16;i++) o[d][i]=0.f;
  float m=-1e30f,l=0.f;
  const float c=0.08838834764831845f*1.4426950408889634f;
  for(int t=0;t<16;t++){
    f32x16 s; for(int i=0;i<16;i++) s[i]=0.f;
    #pragma unroll
    for(int ks=0;ks<8;ks++){
      bf16x8 kf=*(const bf16x8*)(kp+(size_t)(t*32+r)*128+16*ks+8*hh);
      s=MFMA(kf,qf[ks],s);
    }
    float mx=s[0];
    #pragma unroll
    for(int i=1;i<16;i++) mx=fmaxf(mx,s[i]);
    mx=fmaxf(mx,__shfl_xor(mx,32));
    float mn=fmaxf(m,mx*c); float al=exp2f(m-mn); m=mn;
    float rs=0.f;
    #pragma unroll
    for(int i=0;i<16;i++){ s[i]=exp2f(s[i]*c-mn); rs+=s[i]; }
    rs+=__shfl_xor(rs,32);
    l=l*al+rs;
    #pragma unroll
    for(int d=0;d<4;d++) for(int i=0;i<16;i++) o[d][i]*=al;
    #pragma unroll
    for(int ss=0;ss<2;ss++){
      bf16x8 p;
      #pragma unroll
      for(int j=0;j<8;j++) p[j]=f2bf(s[8*ss+j]);
      #pragma unroll
      for(int d=0;d<4;d++){
        bf16x8 vf;
        #pragma unroll
        for(int j=0;j<8;j++) vf[j]=vp[(size_t)(t*32+16*ss+8*(j>>2)+4*hh+(j&3))*128+32*d+r];
        o[d]=MFMA(vf,p,o[d]);
      }
    }
  }
  float inv=1.f/l;
  u16* op=O+(size_t)(b*160+rb*32+r)*4096+h*128;
  #pragma unroll
  for(int d=0;d<4;d++) for(int i=0;i<16;i++) op[32*d+crow(i,hh)]=f2bf(o[d][i]*inv);
}

extern "C" void kernel_launch(void* const* d_in, const int* in_sizes, int n_in, void* d_out, int out_size, void* d_ws, size_t ws_size, hipStream_t stream){
  u16* Q=(u16*)d_ws; u16* O=Q+(size_t)2560*4096;
  for(int br=0;br<2;br++){
    const u16* x=(const u16*)d_in[5*br+0]; const u16* k=(const u16*)d_in[5*br+1]; const u16* v=(const u16*)d_in[5*br+2];
    const u16* wq=(const u16*)d_in[5*br+3]; const u16* wo=(const u16*)d_in[5*br+4];
    gemm_simple<<<dim3(128,80),64,0,stream>>>(x,512,wq,4096,Q,4096,512);
    attn_simple<<<dim3(5,32,16),64,0,stream>>>(Q,k,v,O);
    gemm_simple<<<dim3(16,80),64,0,stream>>>(O,4096,wo,512,(u16*)d_out+512*br,1024,4096);
  }
}
```

```cpp
#include <hip/hip_runtime.h>

__global__ __launch_bounds__(256, 1) void fused_mha_pair_kernel(const void* xa, const void* ka, const void* va, const void* wqa, const void* woa,
                                                                const void* xb, const void* kb, const void* vb, const void* wqb, const void* wob,
                                                                void* out, void* ws) {
  __shared__ __attribute__((aligned(16))) char lds[163840];
  const unsigned wave = __builtin_amdgcn_readfirstlane(threadIdx.x >> 6);
  const unsigned wg = blockIdx.x;
  asm volatile(
R"ASM(.macro MZ dc,d,ac,a,bc,b
v_mfma_f32_16x16x32_bf16 \dc[\d:\d+3], \ac[\a:\a+3], \bc[\b:\b+3], 0
.endm
.macro MA dc,d,ac,a,bc,b
v_mfma_f32_16x16x32_bf16 \dc[\d:\d+3], \ac[\a:\a+3], \bc[\b:\b+3], \dc[\d:\d+3]
.endm
.macro MC dc,d,ac,a,bc,b,c
v_mfma_f32_16x16x32_bf16 \dc[\d:\d+3], \ac[\a:\a+3], \bc[\b:\b+3], \dc[\c:\c+3]
.endm
.macro R4 c,d,a,o
ds_read_b128 \c[\d:\d+3], v\a offset:\o
.endm
.macro TR c,d,a,o
ds_read_b64_tr_b16 \c[\d:\d+1], v\a offset:\o
.endm
.macro CV d,a,b
v_cvt_pk_bf16_f32 v\d, v\a, v\b
.endm
.macro AR d,a
v_accvgpr_read_b32 v\d, a\a
.endm
.macro AW d,a
v_accvgpr_write_b32 a\d, v\a
.endm
s_mov_b32 s24, %1
s_mov_b32 s25, %2
s_bfe_u32 s30, s24, 0x10000
s_mul_i32 s31, s30, 40
s_load_dwordx8 s[8:15], %0, s31
s_add_u32 s31, s31, 32
s_load_dwordx2 s[16:17], %0, s31
s_load_dwordx4 s[20:23], %0, 80
s_lshr_b32 s31, s24, 3
s_and_b32 s28, s31, 3
s_lshr_b32 s29, s31, 2
s_and_b32 s31, s24, 2
s_lshl_b32 s31, s31, 1
s_add_u32 s28, s28, s31
s_bfe_u32 s31, s24, 0x10002
s_lshl_b32 s31, s31, 3
s_add_u32 s29, s29, s31
s_and_b32 s26, s25, 1
s_lshr_b32 s27, s25, 1
s_lshl_b32 s64, s26, 13
s_lshl_b32 s31, s27, 12
s_add_u32 s64, s64, s31
s_mul_i32 s31, s26, 5120
s_mul_i32 s65, s27, 10240
s_add_u32 s65, s65, 131072
s_mul_i32 s66, s27, -102400
s_add_i32 s66, s66, 151552
v_mbcnt_lo_u32_b32 v255, -1, 0
v_mbcnt_hi_u32_b32 v255, -1, v255
v_lshrrev_b32 v0, 4, v255
v_and_b32 v1, 1, v0
v_mul_u32_u24 v2, 3, v1
v_and_b32 v3, 3, v255
v_xor_b32 v4, v3, v2
v_lshlrev_b32 v4, 4, v4
v_lshrrev_b32 v5, 5, v255
v_lshl_add_u32 v4, v5, 6, v4
v_bfe_u32 v6, v255, 2, 3
v_lshl_add_u32 v243, v6, 10, v4
v_add_u32 v244, 7168, v243
v_add_u32 v245, 14336, v243
v_add_u32 v246, 21504, v243
s_lshl_b32 s81, s27, 1
v_xor_b32 v7, s81, v3
v_lshlrev_b32 v7, 4, v7
v_lshl_add_u32 v7, v5, 6, v7
v_lshl_add_u32 v247, v6, 13, v7
v_add_u32 v248, 130048, v247
v_add_u32 v249, 260096, v247
v_add_u32 v250, 390144, v247
s_add_u32 s66, s66, s31
s_add_u32 s65, s65, s31
s_mul_i32 s72, s29, 160
s_mul_i32 s81, s27, 80
s_add_u32 s72, s72, s81
s_mul_i32 s81, s26, 40
s_add_u32 s72, s72, s81
s_lshl_b32 s72, s72, 10
s_lshl2_add_u32 s73, s28, s26
s_lshl_b32 s73, s73, 8
s_lshl_b32 s81, s27, 16
s_add_u32 s73, s73, s81
s_mov_b32 s34, 40960
s_mov_b32 s35, 0x20000
s_sub_u32 s38, 4194304, s73
s_mov_b32 s39, 0x20000
s_waitcnt lgkmcnt(0)
s_add_u32 s32, s8, s72
s_addc_u32 s33, s9, 0
s_and_b32 s33, s33, 0xffff
s_add_u32 s36, s14, s73
s_addc_u32 s37, s15, 0
s_and_b32 s37, s37, 0xffff
s_mov_b32 m0, s65
s_mov_b32 s67, 0
s_nop 0
buffer_load_dwordx4 v243, s[32:35], s67 offen offset:0 lds
buffer_load_dwordx4 v244, s[32:35], s67 offen offset:1024 lds
buffer_load_dwordx4 v245, s[32:35], s67 offen offset:2048 lds
buffer_load_dwordx4 v246, s[32:35], s67 offen offset:3072 lds
s_add_u32 m0, s65, 4096
s_mov_b32 s68, 32768
s_nop 0
buffer_load_dwordx4 v243, s[32:35], s68 offen offset:0 lds
s_add_u32 m0, s64, 98304
s_mov_b32 s69, 0
s_nop 0
buffer_load_dwordx4 v247, s[36:39], s69 offen offset:0 lds
buffer_load_dwordx4 v248, s[36:39], s69 offen offset:1024 lds
buffer_load_dwordx4 v249, s[36:39], s69 offen offset:2048 lds
buffer_load_dwordx4 v250, s[36:39], s69 offen offset:3072 lds
s_add_u32 m0, s64, 114688
s_mov_b32 s70, 128
s_nop 0
buffer_load_dwordx4 v247, s[36:39], s70 offen offset:0 lds
buffer_load_dwordx4 v248, s[36:39], s70 offen offset:1024 lds
buffer_load_dwordx4 v249, s[36:39], s70 offen offset:2048 lds
buffer_load_dwordx4 v250, s[36:39], s70 offen offset:3072 lds
s_lshl_b32 s31, s29, 3
s_add_u32 s31, s31, s28
s_lshl_b32 s31, s31, 19
s_add_u32 s40, s10, s31
s_addc_u32 s41, s11, 0
s_sub_u32 s40, s40, 1024
s_subb_u32 s41, s41, 0
s_and_b32 s41, s41, 0xffff
s_mov_b32 s42, 525312
s_mov_b32 s43, 0x20000
s_add_u32 s44, s12, s31
s_addc_u32 s45, s13, 0
s_sub_u32 s44, s44, 1024
s_subb_u32 s45, s45, 0
s_and_b32 s45, s45, 0xffff
s_mov_b32 s46, 525312
s_mov_b32 s47, 0x20000
s_lshl_b32 s31, s28, 19
s_lshl_b32 s81, s64, 3
s_add_u32 s31, s31, s81
s_add_u32 s48, s16, s31
s_addc_u32 s49, s17, 0
s_and_b32 s49, s49, 0xffff
s_mov_b32 s50, 425984
s_mov_b32 s51, 0x20000
s_lshl_b32 s31, s30, 4
s_add_u32 s31, s31, s29
s_lshl_b32 s81, s31, 12
s_add_u32 s60, s22, s81
s_addc_u32 s61, s23, 0
s_and_b32 s61, s61, 0xffff
s_mov_b32 s62, 4096
s_mov_b32 s63, 0x20000
s_mul_i32 s81, s31, 1310720
s_add_u32 s81, s81, 131072
s_add_u32 s52, s22, s81
s_addc_u32 s53, s23, 0
s_and_b32 s53, s53, 0xffff
s_mov_b32 s54, 1310720
s_mov_b32 s55, 0x20000
s_mul_i32 s31, s29, 160
s_mul_i32 s81, s27, 80
s_add_u32 s31, s31, s81
s_lshl_b32 s31, s31, 11
s_lshl_b32 s81, s30, 10
s_add_u32 s31, s31, s81
s_lshl_b32 s81, s28, 7
s_add_u32 s31, s31, s81
s_lshl_b32 s81, s26, 6
s_add_u32 s31, s31, s81
s_add_u32 s56, s20, s31
s_addc_u32 s57, s21, 0
s_and_b32 s57, s57, 0xffff
s_mov_b32 s58, 161856
s_mov_b32 s59, 0x20000
s_and_b32 s31, s28, 3
s_lshl_b32 s31, s31, 2
s_add_u32 s31, s31, s25
s_mul_i32 s31, s31, 10
s_mul_i32 s81, s29, 160
s_add_u32 s31, s31, s81
s_lshl_b32 s31, s31, 10
s_add_u32 s8, s8, s31
s_addc_u32 s9, s9, 0
s_and_b32 s9, s9, 0xffff
s_mov_b32 s10, 10240
s_mov_b32 s11, 0x20000
s_and_b32 s31, s29, 7
s_lshl_b32 s31, s31, 2
s_add_u32 s31, s31, s25
s_lshl_b32 s81, s31, 14
s_lshl_b32 s77, s28, 19
s_add_u32 s81, s81, s77
s_lshl_b32 s31, s31, 14
s_lshl_b32 s77, s28, 10
s_add_u32 s31, s31, s77
s_add_u32 s12, s14, s31
s_addc_u32 s13, s15, 0
s_and_b32 s13, s13, 0xffff
s_mov_b32 s14, 3679232
s_mov_b32 s15, 0x20000
v_and_b32 v20, 3, v255
v_lshlrev_b32 v20, 7, v20
v_bfe_u32 v252, v255, 2, 1
v_lshl_add_u32 v20, v252, 13, v20
v_lshrrev_b32 v252, 3, v255
v_lshl_add_u32 v252, v252, 19, v20
v_mul_u32_u24 v20, 26, v255
v_lshrrev_b32 v20, 8, v20
v_mul_u32_u24 v21, 10, v20
v_sub_u32 v21, v255, v21
v_lshlrev_b32 v21, 10, v21
v_lshl_add_u32 v21, v20, 7, v21
v_cmp_gt_u32 vcc, 20, v255
v_mov_b32 v20, 0x1000000
s_nop 0
v_cndmask_b32 v224, v20, v21, vcc
v_cmp_gt_u32 vcc, 20, v255
v_mov_b32 v20, 0x1000000
s_nop 0
v_cndmask_b32 v225, v20, v21, vcc
v_cmp_gt_u32 vcc, 20, v255
v_mov_b32 v20, 0x1000000
s_nop 0
v_cndmask_b32 v226, v20, v21, vcc
v_cmp_gt_u32 vcc, 10, v255
v_mov_b32 v20, 0x1000000
s_nop 0
v_cndmask_b32 v227, v20, v21, vcc
s_add_u32 s16, s16, s81
s_addc_u32 s17, s17, 0
s_and_b32 s17, s17, 0xffff
s_mov_b32 s18, 16
s_mov_b32 s19, 0x20000
s_or_b32 s17, s17, 67108864
s_getpc_b64 s[92:93]
s_and_b32 s92, s92, 0xffffff80
s_and_b32 s93, s93, 0xffff
s_or_b32 s93, s93, 8388608
s_mov_b32 s94, 64
s_mov_b32 s95, 0x20000
CODEBEG%=:
s_lshr_b32 s31, s24, 3
s_lshl2_add_u32 s31, s31, s25
s_and_b32 s31, s31, 63
s_lshl_b32 s31, s31, 7
s_add_u32 s92, s92, s31
s_addc_u32 s93, s93, 528482304
s_sub_u32 s30, SLOW%=-CODEBEG%=+8192, s31
s_lshr_b32 s30, s30, 13
s_mov_b32 m0, 161792
s_add_u32 s12, s12, 0x80000
s_addc_u32 s13, s13, 0
s_mov_b32 s14, 532992
s_mov_b32 s67, 0
s_nop 0
buffer_load_dword v252, s[12:15], s67 offen lds
s_mov_b32 m0, 161792
s_mov_b32 s68, 128
s_nop 0
buffer_load_dword v224, s[8:11], s68 offen lds
s_mov_b32 s29, 106
s_getreg_b32 s31, hwreg(HW_REG_XCC_ID, 0, 1)
s_cmp_eq_u32 s31, 1
s_cselect_b32 s29, 101, s29
s_mov_b32 s82, 0x3e0293ee
s_mov_b32 s83, 0x53800000
s_lshl_b32 s31, s26, 17
s_lshl_b32 s81, s27, 12
s_add_u32 s31, s31, s81
s_add_u32 s31, s31, 1024
v_lshl_add_u32 v241, v6, 8, v4
v_add_u32 v241, s31, v241
v_subrev_u32 v242, 896, v241
v_bfe_u32 v7, v255, 2, 1
v_mul_u32_u24 v7, 3, v7
v_and_b32 v8, 7, v255
v_lshlrev_b32 v8, 6, v8
v_bfe_u32 v9, v255, 3, 1
v_xor_b32 v10, v0, v7
v_lshl_add_u32 v10, v10, 4, v8
v_lshl_add_u32 v10, v9, 10, v10
s_mul_i32 s31, s26, 5120
s_sub_u32 s81, s65, s31
v_add_u32 v251, s81, v10
s_sub_u32 s71, s66, s65
v_mov_b32 v11, v0
v_xor_b32 v11, v11, v7
v_lshl_add_u32 v11, v11, 4, v8
v_lshl_add_u32 v11, v9, 11, v11
s_lshl_b32 s31, s26, 13
v_add_u32 v238, s31, v11
s_add_u32 s31, s31, 65536
v_bfe_u32 v12, v255, 2, 2
v_lshrrev_b32 v13, 1, v3
v_and_b32 v14, 1, v3
v_lshlrev_b32 v14, 3, v14
v_xor_b32 v15, v13, v2
v_lshl_add_u32 v15, v15, 4, v14
v_lshl_add_u32 v15, v12, 6, v15
v_lshl_add_u32 v15, v1, 8, v15
v_lshl_add_u32 v16, v5, 11, v15
v_add_u32 v239, s31, v16
v_xor_b32 v240, 32, v239
v_lshlrev_b32 v17, 1, v1
v_xor_b32 v17, v3, v17
v_lshlrev_b32 v17, 4, v17
v_lshl_add_u32 v17, v12, 6, v17
v_lshl_add_u32 v17, v5, 10, v17
v_lshl_add_u32 v17, v1, 12, v17
v_add_u32 v253, s31, v17
v_xor_b32 v254, 8, v253
s_waitcnt vmcnt(6)
s_barrier
TR v,208,253,32768
TR v,210,253,33024
TR v,212,253,34816
TR v,214,253,35072
R4 a,240,251,0
R4 a,244,251,512
R4 a,248,251,2048
R4 a,252,251,2560
R4 v,160,251,4096
R4 v,164,251,4608
R4 v,168,251,6144
R4 v,172,251,6656
R4 v,176,251,8192
R4 v,180,251,8704
TR v,216,254,32768
s_waitcnt lgkmcnt(14)
TR v,218,254,33024
s_waitcnt lgkmcnt(14)
TR v,220,254,34816
s_waitcnt lgkmcnt(14)
TR v,222,254,35072
s_waitcnt lgkmcnt(13)
MZ v,0,v,208,a,240
TR v,200,253,33280
s_waitcnt lgkmcnt(13)
MA v,0,v,212,a,244
TR v,202,253,33536
s_waitcnt lgkmcnt(12)
MZ v,4,v,212,a,252
s_mov_b32 m0, s66
s_mov_b32 s69, 128
MA v,4,v,208,a,248
TR v,204,253,35328
s_waitcnt lgkmcnt(3)
MZ v,8,v,208,v,160
TR v,206,253,35584
v_add_u32 v251, s71, v251
MA v,8,v,212,v,164
buffer_load_dwordx4 v243, s[32:35], s69 offen offset:0 lds
MZ v,12,v,212,v,172
buffer_load_dwordx4 v244, s[32:35], s69 offen offset:1024 lds
MA v,12,v,208,v,168
buffer_load_dwordx4 v245, s[32:35], s69 offen offset:2048 lds
MZ v,16,v,208,v,176
buffer_load_dwordx4 v246, s[32:35], s69 offen offset:3072 lds
MA v,16,v,212,v,180
s_add_u32 m0, s66, 4096
s_mov_b32 s70, 32896
MZ v,80,v,216,a,240
TR v,208,254,33280
MA v,80,v,220,a,244
TR v,210,254,33536
MZ v,88,v,220,a,252
buffer_load_dwordx4 v243, s[32:35], s70 offen offset:0 lds
MA v,88,v,216,a,248
TR v,212,254,35328
s_add_u32 m0, s64, 65536
MZ v,96,v,216,v,160
TR v,214,254,35584
s_mov_b32 s67, 524288
MA v,96,v,220,v,164
buffer_load_dwordx4 v247, s[36:39], s67 offen offset:0 lds
MZ v,104,v,220,v,172
buffer_load_dwordx4 v248, s[36:39], s67 offen offset:1024 lds
MA v,104,v,216,v,168
buffer_load_dwordx4 v249, s[36:39], s67 offen offset:2048 lds
MZ v,112,v,216,v,176
buffer_load_dwordx4 v250, s[36:39], s67 offen offset:3072 lds
MA v,112,v,220,v,180
s_waitcnt vmcnt(11) lgkmcnt(0)
s_barrier
MZ v,20,v,200,a,240
TR v,216,253,49152
s_add_u32 m0, s64, 81920
MA v,20,v,204,a,244
TR v,218,253,49408
s_mov_b32 s68, 524416
MZ v,24,v,204,a,252
buffer_load_dwordx4 v247, s[36:39], s68 offen offset:0 lds
MA v,24,v,200,a,248
TR v,220,253,51200
MZ v,28,v,200,v,160
TR v,222,253,51456
MA v,28,v,204,v,164
MZ v,32,v,204,v,172
buffer_load_dwordx4 v248, s[36:39], s68 offen offset:1024 lds
MA v,32,v,200,v,168
MZ v,36,v,200,v,176
MA v,36,v,204,v,180
MZ v,84,v,208,a,240
TR v,200,254,49152
MA v,84,v,212,a,244
TR v,202,254,49408
MZ v,92,v,212,a,252
buffer_load_dwordx4 v249, s[36:39], s68 offen offset:2048 lds
MA v,92,v,208,a,248
TR v,204,254,51200
MZ v,100,v,208,v,160
TR v,206,254,51456
MA v,100,v,212,v,164
MZ v,108,v,212,v,172
buffer_load_dwordx4 v250, s[36:39], s68 offen offset:3072 lds
MA v,108,v,208,v,168
s_add_u32 m0, s64, 98304
s_mov_b32 s69, 1048576
MZ v,116,v,208,v,176
MA v,116,v,212,v,180
s_waitcnt lgkmcnt(0)
MZ v,40,v,216,a,240
TR v,208,253,49664
MA v,40,v,220,a,244
TR v,210,253,49920
MZ v,44,v,220,a,252
buffer_load_dwordx4 v247, s[36:39], s69 offen offset:0 lds
MA v,44,v,216,a,248
TR v,212,253,51712
MZ v,48,v,216,v,160
TR v,214,253,51968
MA v,48,v,220,v,164
MZ v,52,v,220,v,172
buffer_load_dwordx4 v248, s[36:39], s69 offen offset:1024 lds
MA v,52,v,216,v,168
MZ v,56,v,216,v,176
MA v,56,v,220,v,180
MZ v,120,v,200,a,240
TR v,216,254,49664
MA v,120,v,204,a,244
TR v,218,254,49920
MZ v,124,v,204,a,252
buffer_load_dwordx4 v249, s[36:39], s69 offen offset:2048 lds
MA v,124,v,200,a,248
TR v,220,254,51712
MZ v,128,v,200,v,160
TR v,222,254,51968
MA v,128,v,204,v,164
MZ v,132,v,204,v,172
buffer_load_dwordx4 v250, s[36:39], s69 offen offset:3072 lds
MA v,132,v,200,v,168
s_mov_b32 m0, s65
s_mov_b32 s70, 256
MZ v,136,v,200,v,176
buffer_load_dwordx4 v243, s[32:35], s70 offen offset:0 lds
MA v,136,v,204,v,180
s_waitcnt vmcnt(9) lgkmcnt(0)
s_barrier
MZ v,60,v,208,a,240
TR v,200,253,0
MA v,60,v,212,a,244
TR v,202,253,256
MZ v,64,v,212,a,252
buffer_load_dwordx4 v244, s[32:35], s70 offen offset:1024 lds
)ASM"
R"ASM(MA v,64,v,208,a,248
TR v,204,253,2048
MZ v,68,v,208,v,160
TR v,206,253,2304
MA v,68,v,212,v,164
R4 v,184,251,0
MZ v,72,v,212,v,172
R4 v,188,251,512
MA v,72,v,208,v,168
buffer_load_dwordx4 v245, s[32:35], s70 offen offset:2048 lds
MZ v,76,v,208,v,176
R4 v,192,251,2048
MA v,76,v,212,v,180
R4 v,196,251,2560
MZ v,140,v,216,a,240
TR v,208,254,0
MA v,140,v,220,a,244
TR v,210,254,256
MZ v,144,v,220,a,252
buffer_load_dwordx4 v246, s[32:35], s70 offen offset:3072 lds
MA v,144,v,216,a,248
TR v,212,254,2048
s_add_u32 m0, s65, 4096
MZ v,148,v,216,v,160
TR v,214,254,2304
s_mov_b32 s67, 33024
MA v,148,v,220,v,164
R4 a,240,251,4096
MZ v,152,v,220,v,172
R4 a,244,251,4608
MA v,152,v,216,v,168
buffer_load_dwordx4 v243, s[32:35], s67 offen offset:0 lds
MZ v,156,v,216,v,176
R4 a,248,251,6144
s_mov_b32 m0, 161792
MA v,156,v,220,v,180
s_waitcnt lgkmcnt(2)
R4 a,252,251,6656
MA v,0,v,200,v,184
R4 v,160,251,8192
s_add_u32 s12, s12, 0x100000
s_addc_u32 s13, s13, 0
MA v,0,v,204,v,188
R4 v,164,251,8704
s_mov_b32 s14, 532992
MA v,4,v,204,v,196
s_mov_b32 s68, 0
v_subrev_u32 v251, s71, v251
MA v,4,v,200,v,192
TR v,216,253,512
MA v,8,v,200,a,240
TR v,218,253,768
s_waitcnt lgkmcnt(3)
MA v,8,v,204,a,244
TR v,220,253,2560
MA v,12,v,204,a,252
TR v,222,253,2816
MA v,12,v,200,a,248
buffer_load_dword v252, s[12:15], s68 offen lds
MA v,16,v,200,v,160
s_waitcnt lgkmcnt(2)
s_mov_b32 m0, 161792
MA v,16,v,204,v,164
s_mov_b32 s69, 384
MA v,80,v,208,v,184
TR v,200,254,512
MA v,80,v,212,v,188
TR v,202,254,768
MA v,88,v,212,v,196
buffer_load_dword v225, s[8:11], s69 offen lds
s_mov_b32 m0, 161792
s_min_u32 s94, s30, 3
s_sub_u32 s30, s30, s94
s_mov_b32 s70, 0
MA v,88,v,208,v,192
TR v,204,254,2560
buffer_load_dword v255, s[92:95], s70 idxen lds
MA v,96,v,208,a,240
TR v,206,254,2816
s_add_u32 m0, s64, 114688
s_mov_b32 s70, 1048704
MA v,96,v,212,a,244
buffer_load_dwordx4 v247, s[36:39], s70 offen offset:0 lds
MA v,104,v,212,a,252
buffer_load_dwordx4 v248, s[36:39], s70 offen offset:1024 lds
MA v,104,v,208,a,248
buffer_load_dwordx4 v249, s[36:39], s70 offen offset:2048 lds
MA v,112,v,208,v,160
buffer_load_dwordx4 v250, s[36:39], s70 offen offset:3072 lds
MA v,112,v,212,v,164
s_waitcnt vmcnt(16) lgkmcnt(0)
s_barrier
MA v,20,v,216,v,184
TR v,208,253,16384
s_add_u32 m0, s64, 0
MA v,20,v,220,v,188
TR v,210,253,16640
s_mov_b32 s67, 0
MA v,24,v,220,v,196
buffer_load_dwordx4 v241, s[40:43], s67 offen offset:0 nt lds
MA v,24,v,216,v,192
TR v,212,253,18432
MA v,28,v,216,a,240
TR v,214,253,18688
MA v,28,v,220,a,244
MA v,32,v,220,a,252
buffer_load_dwordx4 v242, s[40:43], s67 offen offset:1024 nt lds
MA v,32,v,216,a,248
MA v,36,v,216,v,160
MA v,36,v,220,v,164
MA v,84,v,200,v,184
TR v,216,254,16384
MA v,84,v,204,v,188
TR v,218,254,16640
MA v,92,v,204,v,196
buffer_load_dwordx4 v241, s[40:43], s67 offen offset:2048 nt lds
MA v,92,v,200,v,192
TR v,220,254,18432
MA v,100,v,200,a,240
TR v,222,254,18688
MA v,100,v,204,a,244
MA v,108,v,204,a,252
buffer_load_dwordx4 v242, s[40:43], s67 offen offset:3072 nt lds
MA v,108,v,200,a,248
s_add_u32 m0, s64, 65536
s_mov_b32 s68, 1572864
MA v,116,v,200,v,160
MA v,116,v,204,v,164
s_waitcnt lgkmcnt(0)
MA v,40,v,208,v,184
TR v,200,253,16896
MA v,40,v,212,v,188
TR v,202,253,17152
MA v,44,v,212,v,196
buffer_load_dwordx4 v247, s[36:39], s68 offen offset:0 lds
MA v,44,v,208,v,192
TR v,204,253,18944
MA v,48,v,208,a,240
TR v,206,253,19200
MA v,48,v,212,a,244
MA v,52,v,212,a,252
buffer_load_dwordx4 v248, s[36:39], s68 offen offset:1024 lds
MA v,52,v,208,a,248
MA v,56,v,208,v,160
MA v,56,v,212,v,164
MA v,120,v,216,v,184
TR v,208,254,16896
MA v,120,v,220,v,188
TR v,210,254,17152
MA v,124,v,220,v,196
buffer_load_dwordx4 v249, s[36:39], s68 offen offset:2048 lds
MA v,124,v,216,v,192
TR v,212,254,18944
MA v,128,v,216,a,240
TR v,214,254,19200
MA v,128,v,220,a,244
MA v,132,v,220,a,252
buffer_load_dwordx4 v250, s[36:39], s68 offen offset:3072 lds
MA v,132,v,216,a,248
s_mov_b32 m0, s66
s_mov_b32 s69, 384
MA v,136,v,216,v,160
MA v,136,v,220,v,164
s_waitcnt vmcnt(15) lgkmcnt(0)
s_barrier
MA v,60,v,200,v,184
TR v,216,253,32768
MA v,60,v,204,v,188
TR v,218,253,33024
MA v,64,v,204,v,196
buffer_load_dwordx4 v243, s[32:35], s69 offen offset:0 lds
MA v,64,v,200,v,192
TR v,220,253,34816
MA v,68,v,200,a,240
TR v,222,253,35072
MA v,68,v,204,a,244
R4 v,168,251,0
MA v,72,v,204,a,252
R4 v,172,251,512
MA v,72,v,200,a,248
buffer_load_dwordx4 v244, s[32:35], s69 offen offset:1024 lds
MA v,76,v,200,v,160
R4 v,176,251,2048
MA v,76,v,204,v,164
R4 v,180,251,2560
MA v,140,v,208,v,184
TR v,200,254,32768
MA v,140,v,212,v,188
TR v,202,254,33024
MA v,144,v,212,v,196
buffer_load_dwordx4 v245, s[32:35], s69 offen offset:2048 lds
MA v,144,v,208,v,192
TR v,204,254,34816
MA v,148,v,208,a,240
TR v,206,254,35072
MA v,148,v,212,a,244
R4 v,184,251,4096
MA v,152,v,212,a,252
R4 v,188,251,4608
MA v,152,v,208,a,248
buffer_load_dwordx4 v246, s[32:35], s69 offen offset:3072 lds
MA v,156,v,208,v,160
R4 v,192,251,6144
s_add_u32 m0, s66, 4096
MA v,156,v,212,v,164
s_waitcnt lgkmcnt(2)
R4 v,196,251,6656
MA v,0,v,216,v,168
R4 a,240,251,8192
s_mov_b32 s70, 33152
MA v,0,v,220,v,172
R4 a,244,251,8704
MA v,4,v,220,v,180
buffer_load_dwordx4 v243, s[32:35], s70 offen offset:0 lds
MA v,4,v,216,v,176
TR v,208,253,33280
s_add_u32 m0, s64, 81920
MA v,8,v,216,v,184
TR v,210,253,33536
s_waitcnt lgkmcnt(3)
MA v,8,v,220,v,188
TR v,212,253,35328
s_mov_b32 s67, 1572992
MA v,12,v,220,v,196
TR v,214,253,35584
v_add_u32 v251, s71, v251
MA v,12,v,216,v,192
buffer_load_dwordx4 v247, s[36:39], s67 offen offset:0 lds
MA v,16,v,216,a,240
s_waitcnt lgkmcnt(2)
MA v,16,v,220,a,244
MA v,80,v,200,v,168
TR v,216,254,33280
MA v,80,v,204,v,172
TR v,218,254,33536
MA v,88,v,204,v,180
buffer_load_dwordx4 v248, s[36:39], s67 offen offset:1024 lds
MA v,88,v,200,v,176
TR v,220,254,35328
MA v,96,v,200,v,184
TR v,222,254,35584
MA v,96,v,204,v,188
MA v,104,v,204,v,196
buffer_load_dwordx4 v249, s[36:39], s67 offen offset:2048 lds
MA v,104,v,200,v,192
buffer_load_dwordx4 v250, s[36:39], s67 offen offset:3072 lds
MA v,112,v,200,a,240
s_mov_b32 m0, 161792
s_add_u32 s12, s12, 0x100000
s_addc_u32 s13, s13, 0
MA v,112,v,204,a,244
s_waitcnt vmcnt(17) lgkmcnt(0)
s_barrier
MA v,20,v,208,v,168
TR v,200,253,49152
s_mov_b32 s14, 532992
MA v,20,v,212,v,172
TR v,202,253,49408
s_mov_b32 s68, 0
MA v,24,v,212,v,180
buffer_load_dword v252, s[12:15], s68 offen lds
MA v,24,v,208,v,176
TR v,204,253,51200
s_mov_b32 m0, 161792
MA v,28,v,208,v,184
TR v,206,253,51456
s_mov_b32 s69, 640
MA v,28,v,212,v,188
MA v,32,v,212,v,196
buffer_load_dword v226, s[8:11], s69 offen lds
MA v,32,v,208,v,192
s_add_u32 m0, s64, 98304
s_mov_b32 s70, 2097152
MA v,36,v,208,a,240
MA v,36,v,212,a,244
MA v,84,v,216,v,168
TR v,208,254,49152
MA v,84,v,220,v,172
TR v,210,254,49408
MA v,92,v,220,v,180
buffer_load_dwordx4 v247, s[36:39], s70 offen offset:0 lds
MA v,92,v,216,v,176
TR v,212,254,51200
MA v,100,v,216,v,184
TR v,214,254,51456
MA v,100,v,220,v,188
MA v,108,v,220,v,196
buffer_load_dwordx4 v248, s[36:39], s70 offen offset:1024 lds
MA v,108,v,216,v,192
MA v,116,v,216,a,240
MA v,116,v,220,a,244
s_waitcnt lgkmcnt(0)
MA v,40,v,200,v,168
TR v,216,253,49664
MA v,40,v,204,v,172
TR v,218,253,49920
MA v,44,v,204,v,180
buffer_load_dwordx4 v249, s[36:39], s70 offen offset:2048 lds
MA v,44,v,200,v,176
TR v,220,253,51712
MA v,48,v,200,v,184
TR v,222,253,51968
MA v,48,v,204,v,188
MA v,52,v,204,v,196
buffer_load_dwordx4 v250, s[36:39], s70 offen offset:3072 lds
MA v,52,v,200,v,192
s_mov_b32 m0, s65
s_mov_b32 s67, 512
MA v,56,v,200,a,240
MA v,56,v,204,a,244
MA v,120,v,208,v,168
TR v,200,254,49664
MA v,120,v,212,v,172
TR v,202,254,49920
MA v,124,v,212,v,180
buffer_load_dwordx4 v243, s[32:35], s67 offen offset:0 lds
MA v,124,v,208,v,176
TR v,204,254,51712
MA v,128,v,208,v,184
TR v,206,254,51968
MA v,128,v,212,v,188
MA v,132,v,212,v,196
buffer_load_dwordx4 v244, s[32:35], s67 offen offset:1024 lds
MA v,132,v,208,v,192
MA v,136,v,208,a,240
MA v,136,v,212,a,244
s_waitcnt vmcnt(12) lgkmcnt(0)
s_barrier
MA v,60,v,216,v,168
TR v,208,253,0
MA v,60,v,220,v,172
TR v,210,253,256
MA v,64,v,220,v,180
buffer_load_dwordx4 v245, s[32:35], s67 offen offset:2048 lds
MA v,64,v,216,v,176
TR v,212,253,2048
MA v,68,v,216,v,184
TR v,214,253,2304
MA v,68,v,220,v,188
R4 a,248,251,0
MA v,72,v,220,v,196
R4 a,252,251,512
MA v,72,v,216,v,192
buffer_load_dwordx4 v246, s[32:35], s67 offen offset:3072 lds
MA v,76,v,216,a,240
R4 v,160,251,2048
s_add_u32 m0, s65, 4096
MA v,76,v,220,a,244
R4 v,164,251,2560
s_mov_b32 s68, 33280
MA v,140,v,200,v,168
TR v,216,254,0
MA v,140,v,204,v,172
TR v,218,254,256
MA v,144,v,204,v,180
buffer_load_dwordx4 v243, s[32:35], s68 offen offset:0 lds
MA v,144,v,200,v,176
TR v,220,254,2048
s_add_u32 m0, s64, 114688
MA v,148,v,200,v,184
TR v,222,254,2304
s_mov_b32 s69, 2097280
MA v,148,v,204,v,188
R4 v,168,251,4096
MA v,152,v,204,v,196
R4 v,172,251,4608
MA v,152,v,200,v,192
buffer_load_dwordx4 v247, s[36:39], s69 offen offset:0 lds
MA v,156,v,200,a,240
R4 v,176,251,6144
MA v,156,v,204,a,244
s_waitcnt lgkmcnt(2)
R4 v,180,251,6656
MA v,0,v,208,a,248
R4 v,184,251,8192
MA v,0,v,212,a,252
R4 v,188,251,8704
MA v,4,v,212,v,164
buffer_load_dwordx4 v248, s[36:39], s69 offen offset:1024 lds
MA v,4,v,208,v,160
TR v,200,253,512
v_subrev_u32 v251, s71, v251
MA v,8,v,208,v,168
TR v,202,253,768
s_waitcnt lgkmcnt(3)
MA v,8,v,212,v,172
TR v,204,253,2560
MA v,12,v,212,v,180
TR v,206,253,2816
MA v,12,v,208,v,176
buffer_load_dwordx4 v249, s[36:39], s69 offen offset:2048 lds
MA v,16,v,208,v,184
s_waitcnt lgkmcnt(2)
MA v,16,v,212,v,188
MA v,80,v,216,a,248
TR v,208,254,512
MA v,80,v,220,a,252
TR v,210,254,768
MA v,88,v,220,v,164
buffer_load_dwordx4 v250, s[36:39], s69 offen offset:3072 lds
MA v,88,v,216,v,160
TR v,212,254,2560
s_add_u32 m0, s64, 16384
MA v,96,v,216,v,168
TR v,214,254,2816
s_mov_b32 s70, 8192
MA v,96,v,220,v,172
MA v,104,v,220,v,180
buffer_load_dwordx4 v241, s[40:43], s70 offen offset:0 nt lds
MA v,104,v,216,v,176
MA v,112,v,216,v,184
MA v,112,v,220,v,188
s_waitcnt vmcnt(16) lgkmcnt(0)
s_barrier
MA v,20,v,200,a,248
TR v,216,253,16384
MA v,20,v,204,a,252
TR v,218,253,16640
MA v,24,v,204,v,164
buffer_load_dwordx4 v242, s[40:43], s70 offen offset:1024 nt lds
MA v,24,v,200,v,160
TR v,220,253,18432
MA v,28,v,200,v,168
TR v,222,253,18688
MA v,28,v,204,v,172
MA v,32,v,204,v,180
buffer_load_dwordx4 v241, s[40:43], s70 offen offset:2048 nt lds
MA v,32,v,200,v,176
MA v,36,v,200,v,184
MA v,36,v,204,v,188
MA v,84,v,208,a,248
TR v,200,254,16384
MA v,84,v,212,a,252
TR v,202,254,16640
MA v,92,v,212,v,164
buffer_load_dwordx4 v242, s[40:43], s70 offen offset:3072 nt lds
MA v,92,v,208,v,160
TR v,204,254,18432
s_add_u32 m0, s64, 65536
MA v,100,v,208,v,168
TR v,206,254,18688
s_mov_b32 s67, 2621440
MA v,100,v,212,v,172
MA v,108,v,212,v,180
buffer_load_dwordx4 v247, s[36:39], s67 offen offset:0 lds
MA v,108,v,208,v,176
MA v,116,v,208,v,184
MA v,116,v,212,v,188
s_waitcnt lgkmcnt(0)
MA v,40,v,216,a,248
TR v,208,253,16896
MA v,40,v,220,a,252
TR v,210,253,17152
MA v,44,v,220,v,164
buffer_load_dwordx4 v248, s[36:39], s67 offen offset:1024 lds
MA v,44,v,216,v,160
TR v,212,253,18944
MA v,48,v,216,v,168
TR v,214,253,19200
MA v,48,v,220,v,172
MA v,52,v,220,v,180
buffer_load_dwordx4 v249, s[36:39], s67 offen offset:2048 lds
MA v,52,v,216,v,176
MA v,56,v,216,v,184
MA v,56,v,220,v,188
MA v,120,v,200,a,248
TR v,216,254,16896
MA v,120,v,204,a,252
TR v,218,254,17152
MA v,124,v,204,v,164
buffer_load_dwordx4 v250, s[36:39], s67 offen offset:3072 lds
MA v,124,v,200,v,160
TR v,220,254,18944
s_mov_b32 m0, s66
MA v,128,v,200,v,168
TR v,222,254,19200
s_mov_b32 s68, 640
MA v,128,v,204,v,172
MA v,132,v,204,v,180
buffer_load_dwordx4 v243, s[32:35], s68 offen offset:0 lds
MA v,132,v,200,v,176
buffer_load_dwordx4 v244, s[32:35], s68 offen offset:1024 lds
MA v,136,v,200,v,184
buffer_load_dwordx4 v245, s[32:35], s68 offen offset:2048 lds
MA v,136,v,204,v,188
s_waitcnt vmcnt(15) lgkmcnt(0)
s_barrier
MA v,60,v,208,a,248
TR v,200,253,32768
MA v,60,v,212,a,252
TR v,202,253,33024
MA v,64,v,212,v,164
buffer_load_dwordx4 v246, s[32:35], s68 offen offset:3072 lds
)ASM"
R"ASM(MA v,64,v,208,v,160
TR v,204,253,34816
s_add_u32 m0, s66, 4096
MA v,68,v,208,v,168
TR v,206,253,35072
s_mov_b32 s69, 33408
MA v,68,v,212,v,172
R4 v,192,251,0
MA v,72,v,212,v,180
R4 v,196,251,512
MA v,72,v,208,v,176
buffer_load_dwordx4 v243, s[32:35], s69 offen offset:0 lds
MA v,76,v,208,v,184
R4 a,240,251,2048
s_mov_b32 m0, 161792
MA v,76,v,212,v,188
R4 a,244,251,2560
s_add_u32 s12, s12, 0x100000
s_addc_u32 s13, s13, 0
MA v,140,v,216,a,248
TR v,208,254,32768
s_mov_b32 s14, 8704
MA v,140,v,220,a,252
TR v,210,254,33024
s_mov_b32 s70, 0
MA v,144,v,220,v,164
buffer_load_dword v252, s[12:15], s70 offen lds
MA v,144,v,216,v,160
TR v,212,254,34816
s_mov_b32 m0, 161792
MA v,148,v,216,v,168
TR v,214,254,35072
s_mov_b32 s67, 896
MA v,148,v,220,v,172
R4 a,248,251,4096
MA v,152,v,220,v,180
R4 a,252,251,4608
MA v,152,v,216,v,176
buffer_load_dword v227, s[8:11], s67 offen lds
MA v,156,v,216,v,184
R4 v,160,251,6144
s_add_u32 m0, s64, 81920
MA v,156,v,220,v,188
s_waitcnt lgkmcnt(2)
R4 v,164,251,6656
MA v,0,v,200,v,192
R4 v,168,251,8192
s_mov_b32 s68, 2621568
MA v,0,v,204,v,196
R4 v,172,251,8704
MA v,4,v,204,a,244
buffer_load_dwordx4 v247, s[36:39], s68 offen offset:0 lds
MA v,4,v,200,a,240
TR v,216,253,33280
v_add_u32 v251, s71, v251
MA v,8,v,200,a,248
TR v,218,253,33536
s_waitcnt lgkmcnt(3)
MA v,8,v,204,a,252
TR v,220,253,35328
MA v,12,v,204,v,164
TR v,222,253,35584
MA v,12,v,200,v,160
buffer_load_dwordx4 v248, s[36:39], s68 offen offset:1024 lds
MA v,16,v,200,v,168
s_waitcnt lgkmcnt(2)
MA v,16,v,204,v,172
MA v,80,v,208,v,192
TR v,200,254,33280
MA v,80,v,212,v,196
TR v,202,254,33536
MA v,88,v,212,a,244
buffer_load_dwordx4 v249, s[36:39], s68 offen offset:2048 lds
MA v,88,v,208,a,240
TR v,204,254,35328
MA v,96,v,208,a,248
TR v,206,254,35584
MA v,96,v,212,a,252
MA v,104,v,212,v,164
buffer_load_dwordx4 v250, s[36:39], s68 offen offset:3072 lds
MA v,104,v,208,v,160
s_add_u32 m0, s64, 98304
s_mov_b32 s69, 3145728
MA v,112,v,208,v,168
MA v,112,v,212,v,172
s_waitcnt vmcnt(19) lgkmcnt(0)
s_barrier
MA v,20,v,216,v,192
TR v,208,253,49152
MA v,20,v,220,v,196
TR v,210,253,49408
MA v,24,v,220,a,244
buffer_load_dwordx4 v247, s[36:39], s69 offen offset:0 lds
MA v,24,v,216,a,240
TR v,212,253,51200
MA v,28,v,216,a,248
TR v,214,253,51456
MA v,28,v,220,a,252
MA v,32,v,220,v,164
buffer_load_dwordx4 v248, s[36:39], s69 offen offset:1024 lds
MA v,32,v,216,v,160
MA v,36,v,216,v,168
MA v,36,v,220,v,172
MA v,84,v,200,v,192
TR v,216,254,49152
MA v,84,v,204,v,196
TR v,218,254,49408
MA v,92,v,204,a,244
buffer_load_dwordx4 v249, s[36:39], s69 offen offset:2048 lds
MA v,92,v,200,a,240
TR v,220,254,51200
MA v,100,v,200,a,248
TR v,222,254,51456
MA v,100,v,204,a,252
MA v,108,v,204,v,164
buffer_load_dwordx4 v250, s[36:39], s69 offen offset:3072 lds
MA v,108,v,200,v,160
s_mov_b32 m0, s65
s_mov_b32 s70, 768
MA v,116,v,200,v,168
MA v,116,v,204,v,172
s_waitcnt lgkmcnt(0)
MA v,40,v,208,v,192
TR v,200,253,49664
MA v,40,v,212,v,196
TR v,202,253,49920
MA v,44,v,212,a,244
buffer_load_dwordx4 v243, s[32:35], s70 offen offset:0 lds
MA v,44,v,208,a,240
TR v,204,253,51712
MA v,48,v,208,a,248
TR v,206,253,51968
MA v,48,v,212,a,252
MA v,52,v,212,v,164
buffer_load_dwordx4 v244, s[32:35], s70 offen offset:1024 lds
MA v,52,v,208,v,160
MA v,56,v,208,v,168
MA v,56,v,212,v,172
MA v,120,v,216,v,192
TR v,208,254,49664
MA v,120,v,220,v,196
TR v,210,254,49920
MA v,124,v,220,a,244
buffer_load_dwordx4 v245, s[32:35], s70 offen offset:2048 lds
MA v,124,v,216,a,240
TR v,212,254,51712
MA v,128,v,216,a,248
TR v,214,254,51968
MA v,128,v,220,a,252
MA v,132,v,220,v,164
buffer_load_dwordx4 v246, s[32:35], s70 offen offset:3072 lds
MA v,132,v,216,v,160
s_add_u32 m0, s65, 4096
s_mov_b32 s67, 33536
MA v,136,v,216,v,168
MA v,136,v,220,v,172
s_waitcnt vmcnt(14) lgkmcnt(0)
s_barrier
MA v,60,v,200,v,192
TR v,216,253,0
MA v,60,v,204,v,196
TR v,218,253,256
MA v,64,v,204,a,244
buffer_load_dwordx4 v243, s[32:35], s67 offen offset:0 lds
MA v,64,v,200,a,240
TR v,220,253,2048
s_add_u32 m0, s64, 114688
MA v,68,v,200,a,248
TR v,222,253,2304
s_mov_b32 s68, 0
MA v,68,v,204,a,252
R4 v,176,251,0
MA v,72,v,204,v,164
R4 v,180,251,512
MA v,72,v,200,v,160
buffer_load_dwordx4 v241, s[44:47], s68 offen offset:0 nt lds
MA v,76,v,200,v,168
R4 v,184,251,2048
MA v,76,v,204,v,172
R4 v,188,251,2560
MA v,140,v,208,v,192
TR v,200,254,0
MA v,140,v,212,v,196
TR v,202,254,256
MA v,144,v,212,a,244
buffer_load_dwordx4 v242, s[44:47], s68 offen offset:1024 nt lds
MA v,144,v,208,a,240
TR v,204,254,2048
MA v,148,v,208,a,248
TR v,206,254,2304
MA v,148,v,212,a,252
R4 v,192,251,4096
MA v,152,v,212,v,164
R4 v,196,251,4608
MA v,152,v,208,v,160
buffer_load_dwordx4 v241, s[44:47], s68 offen offset:2048 nt lds
MA v,156,v,208,v,168
R4 a,240,251,6144
MA v,156,v,212,v,172
s_waitcnt lgkmcnt(2)
R4 a,244,251,6656
MA v,0,v,216,v,176
R4 a,248,251,8192
MA v,0,v,220,v,180
R4 a,252,251,8704
MA v,4,v,220,v,188
buffer_load_dwordx4 v242, s[44:47], s68 offen offset:3072 nt lds
MA v,4,v,216,v,184
TR v,208,253,512
s_add_u32 m0, s64, 32768
MA v,8,v,216,v,192
TR v,210,253,768
s_waitcnt lgkmcnt(3)
MA v,8,v,220,v,196
TR v,212,253,2560
s_mov_b32 s69, 16384
MA v,12,v,220,a,244
TR v,214,253,2816
v_subrev_u32 v251, s71, v251
MA v,12,v,216,a,240
buffer_load_dwordx4 v241, s[40:43], s69 offen offset:0 nt lds
MA v,16,v,216,a,248
s_waitcnt lgkmcnt(2)
MA v,16,v,220,a,252
MA v,80,v,200,v,176
TR v,216,254,512
MA v,80,v,204,v,180
TR v,218,254,768
MA v,88,v,204,v,188
buffer_load_dwordx4 v242, s[40:43], s69 offen offset:1024 nt lds
MA v,88,v,200,v,184
TR v,220,254,2560
MA v,96,v,200,v,192
TR v,222,254,2816
MA v,96,v,204,v,196
MA v,104,v,204,a,244
buffer_load_dwordx4 v241, s[40:43], s69 offen offset:2048 nt lds
MA v,104,v,200,a,240
MA v,112,v,200,a,248
MA v,112,v,204,a,252
s_waitcnt vmcnt(16) lgkmcnt(0)
s_barrier
MA v,20,v,208,v,176
TR v,200,253,16384
MA v,20,v,212,v,180
TR v,202,253,16640
MA v,24,v,212,v,188
buffer_load_dwordx4 v242, s[40:43], s69 offen offset:3072 nt lds
MA v,24,v,208,v,184
TR v,204,253,18432
s_add_u32 m0, s64, 65536
MA v,28,v,208,v,192
TR v,206,253,18688
s_mov_b32 s70, 3145856
MA v,28,v,212,v,196
MA v,32,v,212,a,244
buffer_load_dwordx4 v247, s[36:39], s70 offen offset:0 lds
MA v,32,v,208,a,240
MA v,36,v,208,a,248
MA v,36,v,212,a,252
MA v,84,v,216,v,176
TR v,208,254,16384
MA v,84,v,220,v,180
TR v,210,254,16640
MA v,92,v,220,v,188
buffer_load_dwordx4 v248, s[36:39], s70 offen offset:1024 lds
MA v,92,v,216,v,184
TR v,212,254,18432
MA v,100,v,216,v,192
TR v,214,254,18688
MA v,100,v,220,v,196
MA v,108,v,220,a,244
buffer_load_dwordx4 v249, s[36:39], s70 offen offset:2048 lds
MA v,108,v,216,a,240
MA v,116,v,216,a,248
MA v,116,v,220,a,252
s_waitcnt lgkmcnt(0)
MA v,40,v,200,v,176
TR v,216,253,16896
MA v,40,v,204,v,180
TR v,218,253,17152
MA v,44,v,204,v,188
buffer_load_dwordx4 v250, s[36:39], s70 offen offset:3072 lds
MA v,44,v,200,v,184
TR v,220,253,18944
s_mov_b32 m0, s66
MA v,48,v,200,v,192
TR v,222,253,19200
s_mov_b32 s67, 896
MA v,48,v,204,v,196
MA v,52,v,204,a,244
buffer_load_dwordx4 v243, s[32:35], s67 offen offset:0 lds
MA v,52,v,200,a,240
MA v,56,v,200,a,248
MA v,56,v,204,a,252
MA v,120,v,208,v,176
TR v,200,254,16896
MA v,120,v,212,v,180
TR v,202,254,17152
MA v,124,v,212,v,188
buffer_load_dwordx4 v244, s[32:35], s67 offen offset:1024 lds
MA v,124,v,208,v,184
TR v,204,254,18944
MA v,128,v,208,v,192
TR v,206,254,19200
MA v,128,v,212,v,196
MA v,132,v,212,a,244
buffer_load_dwordx4 v245, s[32:35], s67 offen offset:2048 lds
MA v,132,v,208,a,240
MA v,136,v,208,a,248
MA v,136,v,212,a,252
s_waitcnt vmcnt(15) lgkmcnt(0)
s_barrier
MA v,60,v,216,v,176
TR v,208,253,32768
MA v,60,v,220,v,180
TR v,210,253,33024
MA v,64,v,220,v,188
buffer_load_dwordx4 v246, s[32:35], s67 offen offset:3072 lds
MA v,64,v,216,v,184
TR v,212,253,34816
s_add_u32 m0, s66, 4096
MA v,68,v,216,v,192
TR v,214,253,35072
s_mov_b32 s68, 33664
MA v,68,v,220,v,196
R4 v,160,251,0
MA v,72,v,220,a,244
R4 v,164,251,512
MA v,72,v,216,a,240
buffer_load_dwordx4 v243, s[32:35], s68 offen offset:0 lds
MA v,76,v,216,a,248
R4 v,168,251,2048
s_add_u32 m0, s64, 81920
MA v,76,v,220,a,252
R4 v,172,251,2560
s_mov_b32 s69, 3670016
MA v,140,v,200,v,176
TR v,216,254,32768
MA v,140,v,204,v,180
TR v,218,254,33024
MA v,144,v,204,v,188
buffer_load_dwordx4 v247, s[36:39], s69 offen offset:0 lds
MA v,144,v,200,v,184
TR v,220,254,34816
MA v,148,v,200,v,192
TR v,222,254,35072
MA v,148,v,204,v,196
R4 v,176,251,4096
MA v,152,v,204,a,244
R4 v,180,251,4608
MA v,152,v,200,a,240
buffer_load_dwordx4 v248, s[36:39], s69 offen offset:1024 lds
MA v,156,v,200,a,248
R4 v,184,251,6144
MA v,156,v,204,a,252
s_waitcnt lgkmcnt(2)
R4 v,188,251,6656
MA v,0,v,208,v,160
R4 v,192,251,8192
MA v,0,v,212,v,164
R4 v,196,251,8704
MA v,4,v,212,v,172
buffer_load_dwordx4 v249, s[36:39], s69 offen offset:2048 lds
MA v,4,v,208,v,168
TR v,200,253,33280
v_add_u32 v251, s71, v251
MA v,8,v,208,v,176
TR v,202,253,33536
s_waitcnt lgkmcnt(3)
MA v,8,v,212,v,180
TR v,204,253,35328
MA v,12,v,212,v,188
TR v,206,253,35584
MA v,12,v,208,v,184
buffer_load_dwordx4 v250, s[36:39], s69 offen offset:3072 lds
MA v,16,v,208,v,192
s_waitcnt lgkmcnt(2)
MA v,16,v,212,v,196
MA v,80,v,216,v,160
TR v,208,254,33280
MA v,80,v,220,v,164
TR v,210,254,33536
MA v,88,v,220,v,172
MA v,88,v,216,v,168
TR v,212,254,35328
MA v,96,v,216,v,176
TR v,214,254,35584
MA v,96,v,220,v,180
MA v,104,v,220,v,188
s_add_u32 m0, s64, 98304
s_mov_b32 s70, 3670144
MA v,104,v,216,v,184
MA v,112,v,216,v,192
MA v,112,v,220,v,196
s_waitcnt vmcnt(9) lgkmcnt(0)
s_barrier
MA v,20,v,200,v,160
TR v,216,253,0
MA v,20,v,204,v,164
TR v,218,253,256
MA v,24,v,204,v,172
buffer_load_dwordx4 v247, s[36:39], s70 offen offset:0 lds
MA v,24,v,200,v,168
TR v,220,253,2048
MA v,28,v,200,v,176
TR v,222,253,2304
MA v,28,v,204,v,180
MA v,32,v,204,v,188
buffer_load_dwordx4 v248, s[36:39], s70 offen offset:1024 lds
MA v,32,v,200,v,184
MA v,36,v,200,v,192
MA v,36,v,204,v,196
MA v,84,v,208,v,160
TR v,200,254,0
MA v,84,v,212,v,164
TR v,202,254,256
MA v,92,v,212,v,172
buffer_load_dwordx4 v249, s[36:39], s70 offen offset:2048 lds
MA v,92,v,208,v,168
TR v,204,254,2048
MA v,100,v,208,v,176
TR v,206,254,2304
MA v,100,v,212,v,180
MA v,108,v,212,v,188
buffer_load_dwordx4 v250, s[36:39], s70 offen offset:3072 lds
MA v,108,v,208,v,184
MA v,116,v,208,v,192
MA v,116,v,212,v,196
s_waitcnt lgkmcnt(0)
MA v,40,v,216,v,160
TR v,208,253,512
MA v,40,v,220,v,164
TR v,210,253,768
MA v,44,v,220,v,172
MA v,44,v,216,v,168
TR v,212,253,2560
MA v,48,v,216,v,176
TR v,214,253,2816
MA v,48,v,220,v,180
MA v,52,v,220,v,188
MA v,52,v,216,v,184
MA v,56,v,216,v,192
MA v,56,v,220,v,196
MA v,120,v,200,v,160
TR v,216,254,512
MA v,120,v,204,v,164
TR v,218,254,768
MA v,124,v,204,v,172
MA v,124,v,200,v,168
TR v,220,254,2560
MA v,128,v,200,v,176
TR v,222,254,2816
MA v,128,v,204,v,180
MA v,132,v,204,v,188
s_add_u32 m0, s64, 65536
s_mov_b32 s67, 8192
MA v,132,v,200,v,184
MA v,136,v,200,v,192
MA v,136,v,204,v,196
s_waitcnt vmcnt(4) lgkmcnt(0)
s_barrier
MA v,60,v,208,v,160
TR v,200,253,16384
MA v,60,v,212,v,164
TR v,202,253,16640
MA v,64,v,212,v,172
buffer_load_dwordx4 v241, s[44:47], s67 offen offset:0 nt lds
MA v,64,v,208,v,168
TR v,204,253,18432
MA v,68,v,208,v,176
TR v,206,253,18688
MA v,68,v,212,v,180
R4 a,240,251,0
MA v,72,v,212,v,188
R4 a,244,251,512
MA v,72,v,208,v,184
buffer_load_dwordx4 v242, s[44:47], s67 offen offset:1024 nt lds
MA v,76,v,208,v,192
R4 a,248,251,2048
MA v,76,v,212,v,196
R4 a,252,251,2560
MA v,140,v,216,v,160
TR v,208,254,16384
MA v,140,v,220,v,164
TR v,210,254,16640
MA v,144,v,220,v,172
buffer_load_dwordx4 v241, s[44:47], s67 offen offset:2048 nt lds
MA v,144,v,216,v,168
TR v,212,254,18432
MA v,148,v,216,v,176
TR v,214,254,18688
MA v,148,v,220,v,180
R4 v,160,251,4096
MA v,152,v,220,v,188
R4 v,164,251,4608
MA v,152,v,216,v,184
buffer_load_dwordx4 v242, s[44:47], s67 offen offset:3072 nt lds
MA v,156,v,216,v,192
R4 v,168,251,6144
MA v,156,v,220,v,196
s_waitcnt lgkmcnt(2)
R4 v,172,251,6656
MA v,0,v,200,a,240
R4 v,176,251,8192
MA v,0,v,204,a,244
R4 v,180,251,8704
MA v,4,v,204,a,252
v_subrev_u32 v251, s71, v251
MA v,4,v,200,a,248
)ASM"
R"ASM(TR v,216,253,16896
MA v,8,v,200,v,160
TR v,218,253,17152
s_waitcnt lgkmcnt(3)
MA v,8,v,204,v,164
TR v,220,253,18944
v_mul_f32 v0, s82, v0
MA v,12,v,204,v,172
TR v,222,253,19200
v_mul_f32 v1, s82, v1
MA v,12,v,200,v,168
v_mul_f32 v2, s82, v2
v_mul_f32 v3, s82, v3
MA v,16,v,200,v,176
s_waitcnt lgkmcnt(2)
CV 0,0,1
MA v,16,v,204,v,180
CV 1,2,3
v_mul_f32 v4, s82, v4
MA v,80,v,208,a,240
TR v,200,254,16896
v_mul_f32 v5, s82, v5
MA v,80,v,212,a,244
TR v,202,254,17152
v_mul_f32 v6, s82, v6
MA v,88,v,212,a,252
v_mul_f32 v7, s82, v7
CV 4,4,5
MA v,88,v,208,a,248
TR v,204,254,18944
CV 5,6,7
MA v,96,v,208,v,160
TR v,206,254,19200
v_mul_f32 v80, s82, v80
MA v,96,v,212,v,164
v_mul_f32 v81, s82, v81
v_mul_f32 v82, s82, v82
MA v,104,v,212,v,172
s_add_u32 m0, s64, 81920
s_mov_b32 s68, 16384
MA v,104,v,208,v,168
v_mul_f32 v83, s82, v83
CV 2,80,81
MA v,112,v,208,v,176
CV 3,82,83
v_mul_f32 v88, s82, v88
MA v,112,v,212,v,180
s_waitcnt vmcnt(4) lgkmcnt(0)
s_barrier
MA v,20,v,216,a,240
TR v,208,253,32768
v_mul_f32 v89, s82, v89
MA v,20,v,220,a,244
TR v,210,253,33024
v_mul_f32 v90, s82, v90
MA v,24,v,220,a,252
buffer_load_dwordx4 v241, s[44:47], s68 offen offset:0 nt lds
MA v,24,v,216,a,248
TR v,212,253,34816
v_mul_f32 v91, s82, v91
MA v,28,v,216,v,160
TR v,214,253,35072
v_mul_f32 v20, s82, v20
MA v,28,v,220,v,164
v_mul_f32 v21, s82, v21
v_mul_f32 v22, s82, v22
MA v,32,v,220,v,172
buffer_load_dwordx4 v242, s[44:47], s68 offen offset:1024 nt lds
MA v,32,v,216,v,168
v_mul_f32 v23, s82, v23
CV 20,20,21
MA v,36,v,216,v,176
CV 21,22,23
CV 6,88,89
MA v,36,v,220,v,180
CV 7,90,91
v_mul_f32 v24, s82, v24
MA v,84,v,200,a,240
TR v,216,254,32768
v_mul_f32 v25, s82, v25
MA v,84,v,204,a,244
TR v,218,254,33024
v_mul_f32 v26, s82, v26
MA v,92,v,204,a,252
buffer_load_dwordx4 v241, s[44:47], s68 offen offset:2048 nt lds
MA v,92,v,200,a,248
TR v,220,254,34816
v_mul_f32 v27, s82, v27
MA v,100,v,200,v,160
TR v,222,254,35072
v_mul_f32 v84, s82, v84
MA v,100,v,204,v,164
v_mul_f32 v85, s82, v85
v_mul_f32 v86, s82, v86
MA v,108,v,204,v,172
buffer_load_dwordx4 v242, s[44:47], s68 offen offset:3072 nt lds
MA v,108,v,200,v,168
s_add_u32 m0, s64, 49152
s_mov_b32 s69, 24576
MA v,116,v,200,v,176
v_mul_f32 v87, s82, v87
CV 22,84,85
MA v,116,v,204,v,180
s_waitcnt lgkmcnt(0)
CV 23,86,87
MA v,40,v,208,a,240
TR v,200,253,33280
CV 24,24,25
MA v,40,v,212,a,244
TR v,202,253,33536
CV 25,26,27
MA v,44,v,212,a,252
buffer_load_dwordx4 v241, s[40:43], s69 offen offset:0 nt lds
MA v,44,v,208,a,248
TR v,204,253,35328
v_mul_f32 v92, s82, v92
MA v,48,v,208,v,160
TR v,206,253,35584
v_mul_f32 v40, s82, v40
MA v,48,v,212,v,164
v_mul_f32 v41, s82, v41
v_mul_f32 v42, s82, v42
MA v,52,v,212,v,172
buffer_load_dwordx4 v242, s[40:43], s69 offen offset:1024 nt lds
MA v,52,v,208,v,168
v_mul_f32 v43, s82, v43
CV 40,40,41
MA v,56,v,208,v,176
CV 41,42,43
v_mul_f32 v93, s82, v93
MA v,56,v,212,v,180
v_mul_f32 v94, s82, v94
v_mul_f32 v95, s82, v95
MA v,120,v,216,a,240
TR v,208,254,33280
CV 26,92,93
MA v,120,v,220,a,244
TR v,210,254,33536
CV 27,94,95
MA v,124,v,220,a,252
buffer_load_dwordx4 v241, s[40:43], s69 offen offset:2048 nt lds
MA v,124,v,216,a,248
TR v,212,254,35328
v_mul_f32 v44, s82, v44
MA v,128,v,216,v,160
TR v,214,254,35584
v_mul_f32 v120, s82, v120
MA v,128,v,220,v,164
v_mul_f32 v121, s82, v121
v_mul_f32 v122, s82, v122
MA v,132,v,220,v,172
buffer_load_dwordx4 v242, s[40:43], s69 offen offset:3072 nt lds
MA v,132,v,216,v,168
s_add_u32 m0, s64, 98304
s_mov_b32 s70, 24576
MA v,136,v,216,v,176
v_mul_f32 v123, s82, v123
CV 42,120,121
MA v,136,v,220,v,180
s_waitcnt lgkmcnt(0)
s_barrier
MA v,60,v,200,a,240
buffer_load_dwordx4 v241, s[44:47], s70 offen offset:0 nt lds
MA v,60,v,204,a,244
CV 43,122,123
v_mul_f32 v45, s82, v45
MA v,64,v,204,a,252
v_mul_f32 v46, s82, v46
v_mul_f32 v47, s82, v47
MA v,64,v,200,a,248
CV 44,44,45
CV 45,46,47
MA v,68,v,200,v,160
buffer_load_dwordx4 v242, s[44:47], s70 offen offset:1024 nt lds
MA v,68,v,204,v,164
v_mul_f32 v60, s82, v60
v_mul_f32 v61, s82, v61
MA v,72,v,204,v,172
v_mul_f32 v62, s82, v62
v_mul_f32 v63, s82, v63
MA v,72,v,200,v,168
CV 60,60,61
CV 61,62,63
MA v,76,v,200,v,176
buffer_load_dwordx4 v241, s[44:47], s70 offen offset:2048 nt lds
MA v,76,v,204,v,180
v_mul_f32 v124, s82, v124
v_mul_f32 v125, s82, v125
MA v,140,v,208,a,240
R4 v,200,238,0
v_mul_f32 v126, s82, v126
MA v,140,v,212,a,244
R4 v,204,238,512
v_mul_f32 v127, s82, v127
MA v,144,v,212,a,252
buffer_load_dwordx4 v242, s[44:47], s70 offen offset:3072 nt lds
MA v,144,v,208,a,248
CV 46,124,125
CV 47,126,127
MA v,148,v,208,v,160
R4 a,240,238,4096
v_mul_f32 v140, s82, v140
MA v,148,v,212,v,164
R4 a,244,238,4608
v_mul_f32 v141, s82, v141
MA v,152,v,212,v,172
R4 a,248,238,5120
v_mul_f32 v142, s82, v142
MA v,152,v,208,v,168
R4 a,252,238,5632
v_mul_f32 v143, s82, v143
MA v,156,v,208,v,176
R4 v,208,238,1024
CV 62,140,141
MA v,156,v,212,v,180
R4 v,212,238,1536
s_waitcnt lgkmcnt(4)
MZ v,80,v,200,v,0
CV 63,142,143
v_mul_f32 v64, s82, v64
MA v,80,v,204,v,20
s_waitcnt lgkmcnt(1)
v_mul_f32 v65, s82, v65
MA v,80,v,208,v,40
s_waitcnt lgkmcnt(0)
v_mul_f32 v66, s82, v66
MA v,80,v,212,v,60
v_mul_f32 v67, s82, v67
CV 64,64,65
MZ v,84,a,252,v,60
s_add_u32 m0, s64, 0
s_mov_b32 s67, 32768
MA v,84,a,248,v,40
CV 65,66,67
v_mul_f32 v144, s82, v144
MA v,84,a,244,v,20
s_waitcnt lgkmcnt(0)
s_barrier
v_max3_f32 v120, v80, v81, v82
MA v,84,a,240,v,0
v_max_f32 v120, v120, v83
v_mov_b32 v121, v120
MZ v,88,v,200,v,4
buffer_load_dwordx4 v241, s[40:43], s67 offen offset:0 nt lds
v_mul_f32 v145, s82, v145
v_mul_f32 v146, s82, v146
v_mul_f32 v147, s82, v147
CV 66,144,145
CV 67,146,147
v_permlane32_swap_b32 v120, v121
MA v,88,v,204,v,24
v_max_f32 v120, v120, v121
v_mov_b32 v121, v120
MA v,88,v,208,v,44
s_nop 0
v_permlane16_swap_b32 v120, v121
v_max_f32 v120, v120, v121
v_sub_f32 v140, 0, v120
v_sub_f32 v141, 0, v120
v_sub_f32 v142, 0, v120
v_sub_f32 v143, 0, v120
v_add_f32 v80, v140, v80
v_exp_f32 v80, v80
MA v,88,v,212,v,64
v_add_f32 v81, v140, v81
v_exp_f32 v81, v81
MZ v,92,a,252,v,64
s_memrealtime s[98:99]
v_add_f32 v82, v140, v82
v_exp_f32 v82, v82
MA v,92,a,248,v,44
buffer_load_dwordx4 v242, s[40:43], s67 offen offset:1024 nt lds
v_mul_f32 v8, s82, v8
v_mul_f32 v9, s82, v9
v_mul_f32 v10, s82, v10
v_mul_f32 v11, s82, v11
CV 8,8,9
CV 9,10,11
v_mul_f32 v96, s82, v96
v_mul_f32 v97, s82, v97
v_mul_f32 v98, s82, v98
v_mul_f32 v99, s82, v99
CV 10,96,97
CV 11,98,99
v_add_f32 v83, v140, v83
v_exp_f32 v83, v83
MA v,92,a,244,v,24
buffer_load_dwordx4 v241, s[40:43], s67 offen offset:2048 nt lds
v_mul_f32 v28, s82, v28
v_mul_f32 v29, s82, v29
v_mul_f32 v30, s82, v30
v_mul_f32 v31, s82, v31
CV 28,28,29
CV 29,30,31
v_mul_f32 v100, s82, v100
v_mul_f32 v101, s82, v101
v_mul_f32 v102, s82, v102
v_mul_f32 v103, s82, v103
CV 30,100,101
CV 31,102,103
v_add_f32 v84, v140, v84
v_exp_f32 v84, v84
MA v,92,a,240,v,4
s_waitcnt lgkmcnt(0)
s_add_u32 s76, s98, s29
s_barrier
TR v,160,239,49152
TR v,162,239,53248
TR v,164,240,49152
TR v,166,240,53248
TR v,168,239,49664
TR v,170,239,53760
TR v,172,240,49664
TR v,174,240,53760
v_mul_f32 v48, s82, v48
v_mul_f32 v49, s82, v49
v_mul_f32 v50, s82, v50
v_mul_f32 v51, s82, v51
CV 48,48,49
CV 49,50,51
v_mul_f32 v128, s82, v128
v_mul_f32 v129, s82, v129
v_mul_f32 v130, s82, v130
v_mul_f32 v131, s82, v131
CV 50,128,129
CV 51,130,131
v_add_f32 v85, v140, v85
v_max3_f32 v124, v88, v89, v90
v_exp_f32 v85, v85
MZ v,96,v,200,v,8
TR v,176,239,50176
v_mul_f32 v68, s82, v68
v_mul_f32 v69, s82, v69
v_mul_f32 v70, s82, v70
v_mul_f32 v71, s82, v71
CV 68,68,69
CV 69,70,71
v_mul_f32 v148, s82, v148
v_mul_f32 v149, s82, v149
v_mul_f32 v150, s82, v150
v_mul_f32 v151, s82, v151
CV 70,148,149
CV 71,150,151
v_add_f32 v86, v140, v86
v_max_f32 v124, v124, v91
v_exp_f32 v86, v86
MA v,96,v,204,v,28
TR v,178,239,54272
v_add_f32 v87, v140, v87
v_mov_b32 v125, v124
v_exp_f32 v87, v87
MA v,96,v,208,v,48
TR v,180,240,50176
v_permlane32_swap_b32 v124, v125
MA v,96,v,212,v,68
TR v,182,240,54272
v_max_f32 v124, v124, v125
MZ v,100,a,252,v,68
TR v,184,239,50688
v_mov_b32 v125, v124
MA v,100,a,248,v,48
TR v,186,239,54784
v_permlane16_swap_b32 v124, v125
CV 120,80,81
CV 121,82,83
CV 122,84,85
CV 123,86,87
MA v,100,a,244,v,28
TR v,188,240,50688
v_and_b32 v216, 3, v255
v_max_f32 v124, v124, v125
MA v,100,a,240,v,8
s_waitcnt lgkmcnt(3)
TR v,190,240,54784
v_cmp_eq_u32 vcc, 0, v216
v_sub_f32 v144, 0, v124
v_max3_f32 v128, v96, v97, v98
MZ a,0,v,160,v,120
buffer_load_dwordx4 v242, s[40:43], s67 offen offset:3072 nt lds
v_mov_b32 v217, 0x3f803f80
MZ a,20,v,164,v,120
v_cndmask_b32 v216, 0, v217, vcc
v_sub_f32 v145, 0, v124
v_max_f32 v128, v128, v99
MZ a,40,v,168,v,120
v_mov_b32 v217, v216
v_sub_f32 v146, 0, v124
v_mov_b32 v129, v128
MZ a,60,v,172,v,120
v_mov_b32 v218, v216
v_sub_f32 v147, 0, v124
v_add_f32 v88, v144, v88
v_exp_f32 v88, v88
MZ a,80,v,176,v,120
v_mov_b32 v219, v216
v_add_f32 v89, v144, v89
v_permlane32_swap_b32 v128, v129
v_exp_f32 v89, v89
MZ a,100,v,180,v,120
s_waitcnt lgkmcnt(0)
v_add_f32 v90, v144, v90
v_max_f32 v128, v128, v129
v_exp_f32 v90, v90
MZ a,120,v,184,v,120
v_mul_f32 v12, s82, v12
v_mul_f32 v13, s82, v13
v_mul_f32 v14, s82, v14
v_mul_f32 v15, s82, v15
CV 12,12,13
CV 13,14,15
v_mul_f32 v104, s82, v104
v_mul_f32 v105, s82, v105
v_mul_f32 v106, s82, v106
v_mul_f32 v107, s82, v107
CV 14,104,105
CV 15,106,107
v_add_f32 v91, v144, v91
v_exp_f32 v91, v91
MZ a,140,v,188,v,120
v_mul_f32 v32, s82, v32
v_mul_f32 v33, s82, v33
v_mul_f32 v34, s82, v34
v_mul_f32 v35, s82, v35
CV 32,32,33
CV 33,34,35
v_mul_f32 v108, s82, v108
v_mul_f32 v109, s82, v109
v_mul_f32 v110, s82, v110
v_mul_f32 v111, s82, v111
CV 34,108,109
CV 35,110,111
v_add_f32 v92, v144, v92
v_mov_b32 v129, v128
v_exp_f32 v92, v92
MZ v,192,v,216,v,120
v_mul_f32 v52, s82, v52
v_mul_f32 v53, s82, v53
v_mul_f32 v54, s82, v54
v_mul_f32 v55, s82, v55
CV 52,52,53
CV 53,54,55
v_mul_f32 v132, s82, v132
v_mul_f32 v133, s82, v133
v_mul_f32 v134, s82, v134
v_mul_f32 v135, s82, v135
CV 54,132,133
CV 55,134,135
v_add_f32 v93, v144, v93
v_permlane16_swap_b32 v128, v129
v_exp_f32 v93, v93
MZ v,104,v,200,v,12
v_mul_f32 v72, s82, v72
v_mul_f32 v73, s82, v73
v_mul_f32 v74, s82, v74
v_mul_f32 v75, s82, v75
CV 72,72,73
CV 73,74,75
v_mul_f32 v152, s82, v152
v_mul_f32 v153, s82, v153
v_mul_f32 v154, s82, v154
v_mul_f32 v155, s82, v155
CV 74,152,153
CV 75,154,155
v_add_f32 v94, v144, v94
v_exp_f32 v94, v94
MA v,104,v,204,v,32
v_add_f32 v95, v144, v95
v_max_f32 v128, v128, v129
v_exp_f32 v95, v95
MA v,104,v,208,v,52
v_sub_f32 v148, 0, v128
v_sub_f32 v149, 0, v128
MA v,104,v,212,v,72
CV 124,88,89
CV 125,90,91
MZ v,108,a,252,v,72
CV 126,92,93
CV 127,94,95
MA v,108,a,248,v,52
v_sub_f32 v150, 0, v128
v_sub_f32 v151, 0, v128
MA v,108,a,244,v,32
v_max3_f32 v132, v104, v105, v106
v_add_f32 v96, v148, v96
MA v,108,a,240,v,12
v_exp_f32 v96, v96
MZ a,4,v,160,v,124
v_max_f32 v132, v132, v107
v_add_f32 v97, v148, v97
MZ a,24,v,164,v,124
v_mov_b32 v133, v132
v_add_f32 v98, v148, v98
MZ a,44,v,168,v,124
v_exp_f32 v97, v97
MZ a,64,v,172,v,124
v_permlane32_swap_b32 v132, v133
v_add_f32 v99, v148, v99
MZ a,84,v,176,v,124
v_exp_f32 v98, v98
MZ a,104,v,180,v,124
v_max_f32 v132, v132, v133
v_mul_f32 v16, s82, v16
MZ a,124,v,184,v,124
v_mul_f32 v17, s82, v17
v_mul_f32 v18, s82, v18
v_mul_f32 v19, s82, v19
CV 16,16,17
CV 17,18,19
v_mul_f32 v112, s82, v112
v_mul_f32 v113, s82, v113
v_mul_f32 v114, s82, v114
v_mul_f32 v115, s82, v115
CV 18,112,113
CV 19,114,115
v_exp_f32 v99, v99
MZ a,144,v,188,v,124
v_mul_f32 v36, s82, v36
v_mul_f32 v37, s82, v37
v_mul_f32 v38, s82, v38
v_mul_f32 v39, s82, v39
CV 36,36,37
CV 37,38,39
v_mul_f32 v116, s82, v116
v_mul_f32 v117, s82, v117
v_mul_f32 v118, s82, v118
v_mul_f32 v119, s82, v119
CV 38,116,117
CV 39,118,119
v_add_f32 v100, v148, v100
v_mov_b32 v133, v132
v_exp_f32 v100, v100
MZ v,196,v,216,v,124
v_mul_f32 v56, s82, v56
v_mul_f32 v57, s82, v57
v_mul_f32 v58, s82, v58
v_mul_f32 v59, s82, v59
CV 56,56,57
CV 57,58,59
v_mul_f32 v136, s82, v136
v_mul_f32 v137, s82, v137
v_mul_f32 v138, s82, v138
v_mul_f32 v139, s82, v139
CV 58,136,137
CV 59,138,139
v_add_f32 v101, v148, v101
)ASM"
R"ASM(v_permlane16_swap_b32 v132, v133
v_exp_f32 v101, v101
MZ v,112,v,200,v,16
R4 v,200,238,16384
v_mul_f32 v76, s82, v76
v_mul_f32 v77, s82, v77
v_mul_f32 v78, s82, v78
v_mul_f32 v79, s82, v79
CV 76,76,77
CV 77,78,79
v_mul_f32 v156, s82, v156
v_mul_f32 v157, s82, v157
v_mul_f32 v158, s82, v158
v_mul_f32 v159, s82, v159
CV 78,156,157
CV 79,158,159
v_add_f32 v102, v148, v102
v_exp_f32 v102, v102
MA v,112,v,204,v,36
R4 v,204,238,16896
v_add_f32 v103, v148, v103
v_max_f32 v132, v132, v133
v_exp_f32 v103, v103
MA v,112,v,208,v,56
R4 v,208,238,17408
v_sub_f32 v152, 0, v132
MA v,112,v,212,v,76
R4 v,212,238,17920
v_sub_f32 v153, 0, v132
MZ v,116,a,252,v,76
R4 a,252,238,22016
CV 128,96,97
MA v,116,a,248,v,56
R4 a,248,238,21504
CV 129,98,99
CV 130,100,101
CV 131,102,103
MA v,116,a,244,v,36
R4 a,244,238,20992
v_sub_f32 v154, 0, v132
MA v,116,a,240,v,16
R4 a,240,238,20480
v_sub_f32 v155, 0, v132
v_max3_f32 v136, v112, v113, v114
MZ a,8,v,160,v,128
v_add_f32 v104, v152, v104
v_max_f32 v136, v136, v115
MZ a,28,v,164,v,128
v_add_f32 v105, v152, v105
v_mov_b32 v137, v136
MZ a,48,v,168,v,128
v_exp_f32 v104, v104
MZ a,68,v,172,v,128
v_exp_f32 v105, v105
MZ a,88,v,176,v,128
v_add_f32 v106, v152, v106
v_permlane32_swap_b32 v136, v137
MZ a,108,v,180,v,128
v_add_f32 v107, v152, v107
v_max_f32 v136, v136, v137
v_exp_f32 v106, v106
MZ a,128,v,184,v,128
v_exp_f32 v107, v107
MZ a,148,v,188,v,128
v_add_f32 v108, v152, v108
v_mov_b32 v137, v136
v_exp_f32 v108, v108
MZ v,220,v,216,v,128
s_waitcnt lgkmcnt(0)
v_add_f32 v109, v152, v109
v_permlane16_swap_b32 v136, v137
v_exp_f32 v109, v109
MC v,80,v,200,v,0,140
v_add_f32 v110, v152, v110
v_exp_f32 v110, v110
MA v,80,v,204,v,20
v_add_f32 v111, v152, v111
v_max_f32 v136, v136, v137
v_exp_f32 v111, v111
MA v,80,v,208,v,40
v_sub_f32 v156, 0, v136
v_sub_f32 v157, 0, v136
MA v,80,v,212,v,60
CV 132,104,105
CV 133,106,107
MC v,84,a,252,v,60,140
CV 134,108,109
CV 135,110,111
MA v,84,a,248,v,40
v_sub_f32 v158, 0, v136
v_sub_f32 v159, 0, v136
MA v,84,a,244,v,20
v_exp_f32 v80, v80
MA v,84,a,240,v,0
v_exp_f32 v81, v81
MZ a,12,v,160,v,132
v_add_f32 v112, v156, v112
v_add_f32 v113, v156, v113
MZ a,32,v,164,v,132
v_exp_f32 v112, v112
MZ a,52,v,168,v,132
v_exp_f32 v113, v113
MZ a,72,v,172,v,132
v_add_f32 v114, v156, v114
v_add_f32 v115, v156, v115
MZ a,92,v,176,v,132
v_exp_f32 v114, v114
MZ a,112,v,180,v,132
v_exp_f32 v115, v115
MZ a,132,v,184,v,132
v_add_f32 v116, v156, v116
v_add_f32 v117, v156, v117
MZ a,152,v,188,v,132
v_exp_f32 v116, v116
MZ v,224,v,216,v,132
v_exp_f32 v117, v117
MC v,88,v,200,v,4,144
v_add_f32 v118, v156, v118
v_exp_f32 v118, v118
MA v,88,v,204,v,24
s_memrealtime s[98:99]
v_add_f32 v119, v156, v119
v_exp_f32 v119, v119
MA v,88,v,208,v,44
v_exp_f32 v82, v82
MA v,88,v,212,v,64
v_exp_f32 v83, v83
MC v,92,a,252,v,64,144
s_waitcnt lgkmcnt(0)
s_sub_i32 s98, s98, s76
MA v,92,a,248,v,44
s_cmp_lt_i32 s98, 0
s_cbranch_scc1 PACE0_1%=
PACED0_1%=:
CV 136,112,113
CV 137,114,115
CV 138,116,117
CV 139,118,119
MA v,92,a,244,v,24
v_exp_f32 v84, v84
MA v,92,a,240,v,4
v_exp_f32 v85, v85
MZ a,16,v,160,v,136
s_waitcnt vmcnt(16)
s_barrier
MZ a,36,v,164,v,136
TR v,160,239,0
CV 120,80,81
MZ a,56,v,168,v,136
TR v,162,239,4096
CV 121,82,83
MZ a,76,v,172,v,136
TR v,164,240,0
CV 122,84,85
MZ a,96,v,176,v,136
TR v,166,240,4096
s_add_u32 s76, s76, s29
MZ a,116,v,180,v,136
TR v,168,239,512
MZ a,136,v,184,v,136
TR v,170,239,4608
MZ a,156,v,188,v,136
TR v,172,240,512
MZ v,228,v,216,v,136
TR v,174,240,4608
MC v,96,v,200,v,8,148
TR v,176,239,1024
v_exp_f32 v86, v86
MA v,96,v,204,v,28
TR v,178,239,5120
v_exp_f32 v87, v87
MA v,96,v,208,v,48
TR v,180,240,1024
CV 123,86,87
MA v,96,v,212,v,68
TR v,182,240,5120
MC v,100,a,252,v,68,148
TR v,184,239,1536
MA v,100,a,248,v,48
TR v,186,239,5632
s_waitcnt lgkmcnt(6)
MA v,100,a,244,v,28
TR v,188,240,1536
MA v,100,a,240,v,8
TR v,190,240,5632
MA a,0,v,160,v,120
v_exp_f32 v88, v88
MA a,20,v,164,v,120
v_exp_f32 v89, v89
MA a,40,v,168,v,120
v_exp_f32 v90, v90
MA a,60,v,172,v,120
s_waitcnt lgkmcnt(0)
CV 124,88,89
MA a,80,v,176,v,120
v_exp_f32 v91, v91
MA a,100,v,180,v,120
s_mov_b32 m0, 161792
s_min_u32 s94, s30, 3
MA a,120,v,184,v,120
s_sub_u32 s30, s30, s94
s_mov_b32 s67, 24576
MA a,140,v,188,v,120
v_exp_f32 v92, v92
MA v,192,v,216,v,120
v_exp_f32 v93, v93
MC v,104,v,200,v,12,152
buffer_load_dword v255, s[92:95], s67 idxen lds
v_exp_f32 v94, v94
MA v,104,v,204,v,32
s_add_u32 m0, s64, 16384
s_mov_b32 s68, 40960
v_exp_f32 v95, v95
MA v,104,v,208,v,52
v_exp_f32 v96, v96
MA v,104,v,212,v,72
v_exp_f32 v97, v97
MC v,108,a,252,v,72,152
buffer_load_dwordx4 v241, s[40:43], s68 offen offset:0 nt lds
MA v,108,a,248,v,52
CV 125,90,91
CV 126,92,93
CV 127,94,95
MA v,108,a,244,v,32
v_exp_f32 v98, v98
MA v,108,a,240,v,12
v_exp_f32 v99, v99
MA a,4,v,160,v,124
buffer_load_dwordx4 v242, s[40:43], s68 offen offset:1024 nt lds
MA a,24,v,164,v,124
buffer_load_dwordx4 v241, s[40:43], s68 offen offset:2048 nt lds
MA a,44,v,168,v,124
buffer_load_dwordx4 v242, s[40:43], s68 offen offset:3072 nt lds
MA a,64,v,172,v,124
s_add_u32 m0, s64, 114688
s_mov_b32 s69, 32768
MA a,84,v,176,v,124
buffer_load_dwordx4 v241, s[44:47], s69 offen offset:0 nt lds
MA a,104,v,180,v,124
buffer_load_dwordx4 v242, s[44:47], s69 offen offset:1024 nt lds
MA a,124,v,184,v,124
buffer_load_dwordx4 v241, s[44:47], s69 offen offset:2048 nt lds
MA a,144,v,188,v,124
buffer_load_dwordx4 v242, s[44:47], s69 offen offset:3072 nt lds
v_exp_f32 v100, v100
MA v,196,v,216,v,124
v_exp_f32 v101, v101
MC v,112,v,200,v,16,156
R4 v,200,238,32768
v_exp_f32 v102, v102
MA v,112,v,204,v,36
R4 v,204,238,33280
v_exp_f32 v103, v103
MA v,112,v,208,v,56
R4 v,208,238,33792
CV 128,96,97
MA v,112,v,212,v,76
R4 v,212,238,34304
CV 129,98,99
MC v,116,a,252,v,76,156
R4 a,252,238,38400
CV 130,100,101
MA v,116,a,248,v,56
R4 a,248,238,37888
CV 131,102,103
MA v,116,a,244,v,36
R4 a,244,238,37376
MA v,116,a,240,v,16
R4 a,240,238,36864
MA a,8,v,160,v,128
v_exp_f32 v104, v104
MA a,28,v,164,v,128
v_exp_f32 v105, v105
MA a,48,v,168,v,128
v_exp_f32 v106, v106
MA a,68,v,172,v,128
v_exp_f32 v107, v107
MA a,88,v,176,v,128
v_exp_f32 v108, v108
MA a,108,v,180,v,128
v_exp_f32 v109, v109
MA a,128,v,184,v,128
v_exp_f32 v110, v110
MA a,148,v,188,v,128
v_exp_f32 v111, v111
MA v,220,v,216,v,128
s_waitcnt lgkmcnt(0)
CV 132,104,105
MC v,80,v,200,v,0,140
v_exp_f32 v112, v112
MA v,80,v,204,v,20
v_exp_f32 v113, v113
MA v,80,v,208,v,40
v_exp_f32 v114, v114
MA v,80,v,212,v,60
v_exp_f32 v115, v115
MC v,84,a,252,v,60,140
CV 133,106,107
CV 134,108,109
MA v,84,a,248,v,40
CV 135,110,111
CV 136,112,113
MA v,84,a,244,v,20
v_exp_f32 v116, v116
MA v,84,a,240,v,0
v_exp_f32 v117, v117
MA a,12,v,160,v,132
v_exp_f32 v118, v118
MA a,32,v,164,v,132
v_exp_f32 v119, v119
MA a,52,v,168,v,132
v_exp_f32 v80, v80
MA a,72,v,172,v,132
v_exp_f32 v81, v81
MA a,92,v,176,v,132
v_exp_f32 v82, v82
MA a,112,v,180,v,132
v_exp_f32 v83, v83
MA a,132,v,184,v,132
v_exp_f32 v84, v84
MA a,152,v,188,v,132
v_exp_f32 v85, v85
MA v,224,v,216,v,132
v_exp_f32 v86, v86
MC v,88,v,200,v,4,144
v_exp_f32 v87, v87
MA v,88,v,204,v,24
s_memrealtime s[98:99]
CV 137,114,115
MA v,88,v,208,v,44
CV 138,116,117
CV 139,118,119
MA v,88,v,212,v,64
CV 120,80,81
CV 121,82,83
MC v,92,a,252,v,64,144
s_waitcnt lgkmcnt(0)
s_sub_i32 s98, s98, s76
MA v,92,a,248,v,44
s_cmp_lt_i32 s98, 0
s_cbranch_scc1 PACE0_2%=
PACED0_2%=:
MA v,92,a,244,v,24
v_exp_f32 v88, v88
MA v,92,a,240,v,4
v_exp_f32 v89, v89
MA a,16,v,160,v,136
s_waitcnt vmcnt(17)
s_barrier
MA a,36,v,164,v,136
TR v,160,239,16384
CV 122,84,85
MA a,56,v,168,v,136
TR v,162,239,20480
CV 123,86,87
MA a,76,v,172,v,136
TR v,164,240,16384
s_add_u32 s76, s76, s29
MA a,96,v,176,v,136
TR v,166,240,20480
CV 124,88,89
MA a,116,v,180,v,136
TR v,168,239,16896
MA a,136,v,184,v,136
TR v,170,239,20992
MA a,156,v,188,v,136
TR v,172,240,16896
MA v,228,v,216,v,136
TR v,174,240,20992
MC v,96,v,200,v,8,148
TR v,176,239,17408
MA v,96,v,204,v,28
TR v,178,239,21504
MA v,96,v,208,v,48
TR v,180,240,17408
MA v,96,v,212,v,68
TR v,182,240,21504
MC v,100,a,252,v,68,148
TR v,184,239,17920
MA v,100,a,248,v,48
TR v,186,239,22016
s_waitcnt lgkmcnt(6)
MA v,100,a,244,v,28
TR v,188,240,17920
MA v,100,a,240,v,8
TR v,190,240,22016
MA a,0,v,160,v,120
v_exp_f32 v90, v90
MA a,20,v,164,v,120
v_exp_f32 v91, v91
MA a,40,v,168,v,120
v_exp_f32 v92, v92
MA a,60,v,172,v,120
s_waitcnt lgkmcnt(0)
CV 125,90,91
MA a,80,v,176,v,120
v_exp_f32 v93, v93
MA a,100,v,180,v,120
v_exp_f32 v94, v94
MA a,120,v,184,v,120
v_exp_f32 v95, v95
MA a,140,v,188,v,120
v_exp_f32 v96, v96
MA v,192,v,216,v,120
s_add_u32 m0, s64, 32768
s_mov_b32 s70, 49152
MC v,104,v,200,v,12,152
v_exp_f32 v97, v97
MA v,104,v,204,v,32
v_exp_f32 v98, v98
MA v,104,v,208,v,52
v_exp_f32 v99, v99
MA v,104,v,212,v,72
buffer_load_dwordx4 v241, s[40:43], s70 offen offset:0 nt lds
MC v,108,a,252,v,72,152
CV 126,92,93
CV 127,94,95
MA v,108,a,248,v,52
v_exp_f32 v100, v100
MA v,108,a,244,v,32
v_exp_f32 v101, v101
MA v,108,a,240,v,12
buffer_load_dwordx4 v242, s[40:43], s70 offen offset:1024 nt lds
MA a,4,v,160,v,124
buffer_load_dwordx4 v241, s[40:43], s70 offen offset:2048 nt lds
MA a,24,v,164,v,124
buffer_load_dwordx4 v242, s[40:43], s70 offen offset:3072 nt lds
MA a,44,v,168,v,124
s_add_u32 m0, s64, 65536
s_mov_b32 s67, 40960
MA a,64,v,172,v,124
v_exp_f32 v102, v102
MA a,84,v,176,v,124
v_exp_f32 v103, v103
MA a,104,v,180,v,124
buffer_load_dwordx4 v241, s[44:47], s67 offen offset:0 nt lds
MA a,124,v,184,v,124
buffer_load_dwordx4 v242, s[44:47], s67 offen offset:1024 nt lds
MA a,144,v,188,v,124
buffer_load_dwordx4 v241, s[44:47], s67 offen offset:2048 nt lds
MA v,196,v,216,v,124
buffer_load_dwordx4 v242, s[44:47], s67 offen offset:3072 nt lds
MC v,112,v,200,v,16,156
R4 v,200,238,49152
CV 128,96,97
MA v,112,v,204,v,36
R4 v,204,238,49664
CV 129,98,99
MA v,112,v,208,v,56
R4 v,208,238,50176
CV 130,100,101
MA v,112,v,212,v,76
R4 v,212,238,50688
CV 131,102,103
MC v,116,a,252,v,76,156
R4 a,252,238,54784
MA v,116,a,248,v,56
R4 a,248,238,54272
MA v,116,a,244,v,36
R4 a,244,238,53760
MA v,116,a,240,v,16
R4 a,240,238,53248
MA a,8,v,160,v,128
v_exp_f32 v104, v104
MA a,28,v,164,v,128
v_exp_f32 v105, v105
MA a,48,v,168,v,128
buffer_load_dwordx2 v[232:233], off, s[60:63], 0 offset:2048 sc1
MA a,68,v,172,v,128
v_exp_f32 v106, v106
MA a,88,v,176,v,128
v_exp_f32 v107, v107
MA a,108,v,180,v,128
v_exp_f32 v108, v108
MA a,128,v,184,v,128
v_exp_f32 v109, v109
MA a,148,v,188,v,128
v_exp_f32 v110, v110
MA v,220,v,216,v,128
s_waitcnt lgkmcnt(0)
CV 132,104,105
MC v,80,v,200,v,0,140
v_exp_f32 v111, v111
MA v,80,v,204,v,20
v_exp_f32 v112, v112
MA v,80,v,208,v,40
v_exp_f32 v113, v113
MA v,80,v,212,v,60
v_exp_f32 v114, v114
MC v,84,a,252,v,60,140
CV 133,106,107
CV 134,108,109
MA v,84,a,248,v,40
CV 135,110,111
CV 136,112,113
MA v,84,a,244,v,20
v_exp_f32 v115, v115
MA v,84,a,240,v,0
v_exp_f32 v116, v116
MA a,12,v,160,v,132
v_exp_f32 v117, v117
MA a,32,v,164,v,132
v_exp_f32 v118, v118
MA a,52,v,168,v,132
v_exp_f32 v119, v119
MA a,72,v,172,v,132
v_exp_f32 v80, v80
MA a,92,v,176,v,132
v_exp_f32 v81, v81
MA a,112,v,180,v,132
v_exp_f32 v82, v82
MA a,132,v,184,v,132
v_exp_f32 v83, v83
MA a,152,v,188,v,132
v_exp_f32 v84, v84
MA v,224,v,216,v,132
v_exp_f32 v85, v85
MC v,88,v,200,v,4,144
v_exp_f32 v86, v86
MA v,88,v,204,v,24
s_memrealtime s[98:99]
CV 137,114,115
MA v,88,v,208,v,44
v_exp_f32 v87, v87
MA v,88,v,212,v,64
CV 138,116,117
CV 139,118,119
MC v,92,a,252,v,64,144
s_waitcnt lgkmcnt(0)
s_sub_i32 s98, s98, s76
MA v,92,a,248,v,44
s_cmp_lt_i32 s98, 0
s_cbranch_scc1 PACE0_3%=
PACED0_3%=:
MA v,92,a,244,v,24
v_exp_f32 v88, v88
MA v,92,a,240,v,4
v_exp_f32 v89, v89
MA a,16,v,160,v,136
s_waitcnt vmcnt(18)
s_barrier
MA a,36,v,164,v,136
TR v,160,239,32768
CV 120,80,81
MA a,56,v,168,v,136
TR v,162,239,36864
CV 121,82,83
MA a,76,v,172,v,136
TR v,164,240,32768
CV 122,84,85
MA a,96,v,176,v,136
TR v,166,240,36864
CV 123,86,87
MA a,116,v,180,v,136
TR v,168,239,33280
s_add_u32 s76, s76, s29
MA a,136,v,184,v,136
TR v,170,239,37376
CV 124,88,89
MA a,156,v,188,v,136
TR v,172,240,33280
MA v,228,v,216,v,136
TR v,174,240,37376
MC v,96,v,200,v,8,148
)ASM"
R"ASM(TR v,176,239,33792
MA v,96,v,204,v,28
TR v,178,239,37888
MA v,96,v,208,v,48
TR v,180,240,33792
MA v,96,v,212,v,68
TR v,182,240,37888
MC v,100,a,252,v,68,148
TR v,184,239,34304
MA v,100,a,248,v,48
TR v,186,239,38400
s_waitcnt lgkmcnt(6)
MA v,100,a,244,v,28
TR v,188,240,34304
MA v,100,a,240,v,8
TR v,190,240,38400
MA a,0,v,160,v,120
v_exp_f32 v90, v90
MA a,20,v,164,v,120
v_exp_f32 v91, v91
MA a,40,v,168,v,120
v_exp_f32 v92, v92
MA a,60,v,172,v,120
s_waitcnt lgkmcnt(0)
CV 125,90,91
MA a,80,v,176,v,120
v_exp_f32 v93, v93
MA a,100,v,180,v,120
v_exp_f32 v94, v94
MA a,120,v,184,v,120
v_exp_f32 v95, v95
MA a,140,v,188,v,120
v_exp_f32 v96, v96
MA v,192,v,216,v,120
s_add_u32 m0, s64, 49152
s_mov_b32 s68, 57344
MC v,104,v,200,v,12,152
v_exp_f32 v97, v97
MA v,104,v,204,v,32
v_exp_f32 v98, v98
MA v,104,v,208,v,52
v_exp_f32 v99, v99
MA v,104,v,212,v,72
buffer_load_dwordx4 v241, s[40:43], s68 offen offset:0 nt lds
MC v,108,a,252,v,72,152
CV 126,92,93
CV 127,94,95
MA v,108,a,248,v,52
v_exp_f32 v100, v100
MA v,108,a,244,v,32
v_exp_f32 v101, v101
MA v,108,a,240,v,12
buffer_load_dwordx4 v242, s[40:43], s68 offen offset:1024 nt lds
MA a,4,v,160,v,124
v_exp_f32 v102, v102
MA a,24,v,164,v,124
v_exp_f32 v103, v103
MA a,44,v,168,v,124
v_exp_f32 v104, v104
MA a,64,v,172,v,124
buffer_load_dwordx4 v241, s[40:43], s68 offen offset:2048 nt lds
MA a,84,v,176,v,124
v_exp_f32 v105, v105
MA a,104,v,180,v,124
v_exp_f32 v106, v106
MA a,124,v,184,v,124
v_exp_f32 v107, v107
MA a,144,v,188,v,124
buffer_load_dwordx4 v242, s[40:43], s68 offen offset:3072 nt lds
MA v,196,v,216,v,124
v_exp_f32 v108, v108
MC v,112,v,200,v,16,156
R4 v,200,238,0
CV 128,96,97
MA v,112,v,204,v,36
R4 v,204,238,512
CV 129,98,99
MA v,112,v,208,v,56
R4 v,208,238,1024
CV 130,100,101
MA v,112,v,212,v,76
R4 v,212,238,1536
CV 131,102,103
MC v,116,a,252,v,76,156
R4 a,252,238,5632
CV 132,104,105
MA v,116,a,248,v,56
R4 a,248,238,5120
CV 133,106,107
MA v,116,a,244,v,36
R4 a,244,238,4608
v_lshrrev_b32 v234, 3, v255
MA v,116,a,240,v,16
R4 a,240,238,4096
v_lshlrev_b32 v234, 16, v234
MA a,8,v,160,v,128
v_exp_f32 v109, v109
MA a,28,v,164,v,128
v_exp_f32 v110, v110
MA a,48,v,168,v,128
v_exp_f32 v111, v111
MA a,68,v,172,v,128
v_exp_f32 v112, v112
MA a,88,v,176,v,128
v_exp_f32 v113, v113
MA a,108,v,180,v,128
v_exp_f32 v114, v114
MA a,128,v,184,v,128
s_add_u32 m0, s64, 81920
s_mov_b32 s69, 49152
MA a,148,v,188,v,128
v_exp_f32 v115, v115
MA v,220,v,216,v,128
s_waitcnt lgkmcnt(0)
CV 134,108,109
MC v,80,v,200,v,0,140
v_exp_f32 v116, v116
MA v,80,v,204,v,20
buffer_load_dwordx4 v241, s[44:47], s69 offen offset:0 nt lds
MA v,80,v,208,v,40
v_exp_f32 v117, v117
MA v,80,v,212,v,60
v_exp_f32 v118, v118
MC v,84,a,252,v,60,140
CV 135,110,111
CV 136,112,113
MA v,84,a,248,v,40
buffer_load_dwordx4 v242, s[44:47], s69 offen offset:1024 nt lds
MA v,84,a,244,v,20
v_exp_f32 v119, v119
MA v,84,a,240,v,0
v_exp_f32 v80, v80
MA a,12,v,160,v,132
v_exp_f32 v81, v81
MA a,32,v,164,v,132
buffer_load_dwordx4 v241, s[44:47], s69 offen offset:2048 nt lds
MA a,52,v,168,v,132
v_exp_f32 v82, v82
MA a,72,v,172,v,132
v_exp_f32 v83, v83
MA a,92,v,176,v,132
v_exp_f32 v84, v84
MA a,112,v,180,v,132
buffer_load_dwordx4 v242, s[44:47], s69 offen offset:3072 nt lds
MA a,132,v,184,v,132
v_exp_f32 v85, v85
MA a,152,v,188,v,132
v_exp_f32 v86, v86
MA v,224,v,216,v,132
v_exp_f32 v87, v87
MC v,88,v,200,v,4,144
CV 137,114,115
CV 138,116,117
MA v,88,v,204,v,24
s_memrealtime s[98:99]
CV 139,118,119
MA v,88,v,208,v,44
CV 120,80,81
CV 121,82,83
MA v,88,v,212,v,64
CV 122,84,85
CV 123,86,87
MC v,92,a,252,v,64,144
s_waitcnt lgkmcnt(0)
s_sub_i32 s98, s98, s76
MA v,92,a,248,v,44
s_cmp_lt_i32 s98, 0
s_cbranch_scc1 PACE0_4%=
PACED0_4%=:
MA v,92,a,244,v,24
v_exp_f32 v88, v88
MA v,92,a,240,v,4
v_exp_f32 v89, v89
MA a,16,v,160,v,136
s_waitcnt vmcnt(17)
s_barrier
MA a,36,v,164,v,136
TR v,160,239,49152
s_add_u32 s76, s76, s29
MA a,56,v,168,v,136
TR v,162,239,53248
CV 124,88,89
MA a,76,v,172,v,136
TR v,164,240,49152
v_add_lshl_u32 v234, v234, v255, 8
MA a,96,v,176,v,136
TR v,166,240,53248
s_lshr_b32 s72, s64, 12
MA a,116,v,180,v,136
TR v,168,239,49664
s_lshl3_add_u32 s72, s72, s28
MA a,136,v,184,v,136
TR v,170,239,53760
s_lshl_b32 s72, s72, 3
MA a,156,v,188,v,136
TR v,172,240,49664
MA v,228,v,216,v,136
TR v,174,240,53760
MC v,96,v,200,v,8,148
TR v,176,239,50176
MA v,96,v,204,v,28
TR v,178,239,54272
MA v,96,v,208,v,48
TR v,180,240,50176
MA v,96,v,212,v,68
TR v,182,240,54272
MC v,100,a,252,v,68,148
TR v,184,239,50688
MA v,100,a,248,v,48
TR v,186,239,54784
s_waitcnt lgkmcnt(6)
MA v,100,a,244,v,28
TR v,188,240,50688
MA v,100,a,240,v,8
TR v,190,240,54784
MA a,0,v,160,v,120
v_exp_f32 v90, v90
MA a,20,v,164,v,120
v_exp_f32 v91, v91
MA a,40,v,168,v,120
v_exp_f32 v92, v92
MA a,60,v,172,v,120
s_waitcnt lgkmcnt(0)
CV 125,90,91
MA a,80,v,176,v,120
v_exp_f32 v93, v93
MA a,100,v,180,v,120
v_exp_f32 v94, v94
MA a,120,v,184,v,120
v_exp_f32 v95, v95
MA a,140,v,188,v,120
v_exp_f32 v96, v96
MA v,192,v,216,v,120
s_add_u32 m0, s64, 0
s_mov_b32 s70, 65536
MC v,104,v,200,v,12,152
v_exp_f32 v97, v97
MA v,104,v,204,v,32
v_exp_f32 v98, v98
MA v,104,v,208,v,52
v_exp_f32 v99, v99
MA v,104,v,212,v,72
buffer_load_dwordx4 v241, s[40:43], s70 offen offset:0 nt lds
MC v,108,a,252,v,72,152
CV 126,92,93
CV 127,94,95
MA v,108,a,248,v,52
v_exp_f32 v100, v100
MA v,108,a,244,v,32
v_exp_f32 v101, v101
MA v,108,a,240,v,12
buffer_load_dwordx4 v242, s[40:43], s70 offen offset:1024 nt lds
MA a,4,v,160,v,124
v_exp_f32 v102, v102
MA a,24,v,164,v,124
v_exp_f32 v103, v103
MA a,44,v,168,v,124
v_exp_f32 v104, v104
MA a,64,v,172,v,124
buffer_load_dwordx4 v241, s[40:43], s70 offen offset:2048 nt lds
MA a,84,v,176,v,124
v_exp_f32 v105, v105
MA a,104,v,180,v,124
v_exp_f32 v106, v106
MA a,124,v,184,v,124
v_exp_f32 v107, v107
MA a,144,v,188,v,124
buffer_load_dwordx4 v242, s[40:43], s70 offen offset:3072 nt lds
MA v,196,v,216,v,124
v_exp_f32 v108, v108
MC v,112,v,200,v,16,156
R4 v,200,238,16384
CV 128,96,97
MA v,112,v,204,v,36
R4 v,204,238,16896
CV 129,98,99
MA v,112,v,208,v,56
R4 v,208,238,17408
CV 130,100,101
MA v,112,v,212,v,76
R4 v,212,238,17920
CV 131,102,103
MC v,116,a,252,v,76,156
R4 a,252,238,22016
CV 132,104,105
MA v,116,a,248,v,56
R4 a,248,238,21504
CV 133,106,107
MA v,116,a,244,v,36
R4 a,244,238,20992
MA v,116,a,240,v,16
R4 a,240,238,20480
MA a,8,v,160,v,128
v_exp_f32 v109, v109
MA a,28,v,164,v,128
v_exp_f32 v110, v110
MA a,48,v,168,v,128
v_exp_f32 v111, v111
MA a,68,v,172,v,128
v_exp_f32 v112, v112
MA a,88,v,176,v,128
v_exp_f32 v113, v113
MA a,108,v,180,v,128
v_exp_f32 v114, v114
MA a,128,v,184,v,128
s_add_u32 m0, s64, 98304
s_mov_b32 s67, 57344
MA a,148,v,188,v,128
v_exp_f32 v115, v115
MA v,220,v,216,v,128
s_waitcnt lgkmcnt(0)
CV 134,108,109
MC v,80,v,200,v,0,140
v_exp_f32 v116, v116
MA v,80,v,204,v,20
buffer_load_dwordx4 v241, s[44:47], s67 offen offset:0 nt lds
MA v,80,v,208,v,40
v_exp_f32 v117, v117
MA v,80,v,212,v,60
v_exp_f32 v118, v118
MC v,84,a,252,v,60,140
CV 135,110,111
CV 136,112,113
MA v,84,a,248,v,40
buffer_load_dwordx4 v242, s[44:47], s67 offen offset:1024 nt lds
MA v,84,a,244,v,20
v_exp_f32 v119, v119
MA v,84,a,240,v,0
v_exp_f32 v80, v80
MA a,12,v,160,v,132
v_exp_f32 v81, v81
MA a,32,v,164,v,132
buffer_load_dwordx4 v241, s[44:47], s67 offen offset:2048 nt lds
MA a,52,v,168,v,132
v_exp_f32 v82, v82
MA a,72,v,172,v,132
v_exp_f32 v83, v83
MA a,92,v,176,v,132
v_exp_f32 v84, v84
MA a,112,v,180,v,132
buffer_load_dwordx4 v242, s[44:47], s67 offen offset:3072 nt lds
MA a,132,v,184,v,132
v_exp_f32 v85, v85
MA a,152,v,188,v,132
v_exp_f32 v86, v86
MA v,224,v,216,v,132
v_exp_f32 v87, v87
MC v,88,v,200,v,4,144
CV 137,114,115
CV 138,116,117
MA v,88,v,204,v,24
s_memrealtime s[98:99]
CV 139,118,119
MA v,88,v,208,v,44
CV 120,80,81
CV 121,82,83
MA v,88,v,212,v,64
CV 122,84,85
CV 123,86,87
MC v,92,a,252,v,64,144
s_waitcnt lgkmcnt(0)
s_sub_i32 s98, s98, s76
MA v,92,a,248,v,44
s_cmp_lt_i32 s98, 0
s_cbranch_scc1 PACE0_5%=
PACED0_5%=:
MA v,92,a,244,v,24
v_exp_f32 v88, v88
MA v,92,a,240,v,4
v_exp_f32 v89, v89
MA a,16,v,160,v,136
s_waitcnt vmcnt(17)
s_barrier
MA a,36,v,164,v,136
TR v,160,239,0
s_add_u32 s76, s76, s29
MA a,56,v,168,v,136
TR v,162,239,4096
CV 124,88,89
MA a,76,v,172,v,136
TR v,164,240,0
MA a,96,v,176,v,136
TR v,166,240,4096
MA a,116,v,180,v,136
TR v,168,239,512
MA a,136,v,184,v,136
TR v,170,239,4608
MA a,156,v,188,v,136
TR v,172,240,512
MA v,228,v,216,v,136
TR v,174,240,4608
MC v,96,v,200,v,8,148
TR v,176,239,1024
MA v,96,v,204,v,28
TR v,178,239,5120
MA v,96,v,208,v,48
TR v,180,240,1024
MA v,96,v,212,v,68
TR v,182,240,5120
MC v,100,a,252,v,68,148
TR v,184,239,1536
MA v,100,a,248,v,48
TR v,186,239,5632
s_waitcnt lgkmcnt(6)
MA v,100,a,244,v,28
TR v,188,240,1536
MA v,100,a,240,v,8
TR v,190,240,5632
MA a,0,v,160,v,120
v_exp_f32 v90, v90
MA a,20,v,164,v,120
v_exp_f32 v91, v91
MA a,40,v,168,v,120
v_exp_f32 v92, v92
MA a,60,v,172,v,120
s_waitcnt lgkmcnt(0)
CV 125,90,91
MA a,80,v,176,v,120
v_exp_f32 v93, v93
MA a,100,v,180,v,120
v_exp_f32 v94, v94
MA a,120,v,184,v,120
v_exp_f32 v95, v95
MA a,140,v,188,v,120
v_exp_f32 v96, v96
MA v,192,v,216,v,120
s_add_u32 m0, s64, 16384
s_mov_b32 s68, 73728
MC v,104,v,200,v,12,152
v_exp_f32 v97, v97
MA v,104,v,204,v,32
v_exp_f32 v98, v98
MA v,104,v,208,v,52
v_exp_f32 v99, v99
MA v,104,v,212,v,72
buffer_load_dwordx4 v241, s[40:43], s68 offen offset:0 nt lds
MC v,108,a,252,v,72,152
CV 126,92,93
CV 127,94,95
MA v,108,a,248,v,52
v_exp_f32 v100, v100
MA v,108,a,244,v,32
v_exp_f32 v101, v101
MA v,108,a,240,v,12
buffer_load_dwordx4 v242, s[40:43], s68 offen offset:1024 nt lds
MA a,4,v,160,v,124
v_exp_f32 v102, v102
MA a,24,v,164,v,124
v_exp_f32 v103, v103
MA a,44,v,168,v,124
v_exp_f32 v104, v104
MA a,64,v,172,v,124
buffer_load_dwordx4 v241, s[40:43], s68 offen offset:2048 nt lds
MA a,84,v,176,v,124
v_exp_f32 v105, v105
MA a,104,v,180,v,124
v_exp_f32 v106, v106
MA a,124,v,184,v,124
v_exp_f32 v107, v107
MA a,144,v,188,v,124
buffer_load_dwordx4 v242, s[40:43], s68 offen offset:3072 nt lds
MA v,196,v,216,v,124
v_exp_f32 v108, v108
MC v,112,v,200,v,16,156
R4 v,200,238,32768
CV 128,96,97
MA v,112,v,204,v,36
R4 v,204,238,33280
CV 129,98,99
MA v,112,v,208,v,56
R4 v,208,238,33792
CV 130,100,101
MA v,112,v,212,v,76
R4 v,212,238,34304
CV 131,102,103
MC v,116,a,252,v,76,156
R4 a,252,238,38400
s_waitcnt vmcnt(20)
MA v,116,a,248,v,56
R4 a,248,238,37888
CV 132,104,105
MA v,116,a,244,v,36
R4 a,244,238,37376
CV 133,106,107
MA v,116,a,240,v,16
R4 a,240,238,36864
v_readfirstlane_b32 s96, v232
MA a,8,v,160,v,128
v_exp_f32 v109, v109
MA a,28,v,164,v,128
v_exp_f32 v110, v110
MA a,48,v,168,v,128
v_exp_f32 v111, v111
MA a,68,v,172,v,128
v_exp_f32 v112, v112
MA a,88,v,176,v,128
v_exp_f32 v113, v113
MA a,108,v,180,v,128
v_exp_f32 v114, v114
MA a,128,v,184,v,128
s_add_u32 m0, s64, 114688
s_mov_b32 s69, 65536
MA a,148,v,188,v,128
v_exp_f32 v115, v115
MA v,220,v,216,v,128
s_waitcnt lgkmcnt(0)
CV 134,108,109
MC v,80,v,200,v,0,140
v_exp_f32 v116, v116
MA v,80,v,204,v,20
buffer_load_dwordx4 v241, s[44:47], s69 offen offset:0 nt lds
MA v,80,v,208,v,40
v_exp_f32 v117, v117
MA v,80,v,212,v,60
v_exp_f32 v118, v118
MC v,84,a,252,v,60,140
CV 135,110,111
CV 136,112,113
MA v,84,a,248,v,40
buffer_load_dwordx4 v242, s[44:47], s69 offen offset:1024 nt lds
MA v,84,a,244,v,20
v_exp_f32 v119, v119
MA v,84,a,240,v,0
v_exp_f32 v80, v80
MA a,12,v,160,v,132
v_exp_f32 v81, v81
MA a,32,v,164,v,132
buffer_load_dwordx4 v241, s[44:47], s69 offen offset:2048 nt lds
MA a,52,v,168,v,132
v_exp_f32 v82, v82
MA a,72,v,172,v,132
v_exp_f32 v83, v83
MA a,92,v,176,v,132
v_exp_f32 v84, v84
MA a,112,v,180,v,132
buffer_load_dwordx4 v242, s[44:47], s69 offen offset:3072 nt lds
MA a,132,v,184,v,132
v_exp_f32 v85, v85
MA a,152,v,188,v,132
v_exp_f32 v86, v86
MA v,224,v,216,v,132
v_exp_f32 v87, v87
MC v,88,v,200,v,4,144
buffer_store_dwordx2 v[232:233], v234, s[60:63], s72 offen sc1
MA v,88,v,204,v,24
s_memrealtime s[98:99]
CV 137,114,115
MA v,88,v,208,v,44
CV 138,116,117
CV 139,118,119
MA v,88,v,212,v,64
v_readfirstlane_b32 s97, v233
CV 120,80,81
MC v,92,a,252,v,64,144
s_waitcnt lgkmcnt(0)
s_sub_i32 s98, s98, s76
)ASM"
R"ASM(MA v,92,a,248,v,44
s_cmp_lt_i32 s98, 0
s_cbranch_scc1 PACE0_6%=
PACED0_6%=:
MA v,92,a,244,v,24
v_exp_f32 v88, v88
MA v,92,a,240,v,4
v_exp_f32 v89, v89
MA a,16,v,160,v,136
s_waitcnt vmcnt(17)
s_barrier
MA a,36,v,164,v,136
TR v,160,239,16384
s_add_u32 s96, s96, 1
MA a,56,v,168,v,136
TR v,162,239,20480
CV 121,82,83
MA a,76,v,172,v,136
TR v,164,240,16384
CV 122,84,85
MA a,96,v,176,v,136
TR v,166,240,20480
CV 123,86,87
MA a,116,v,180,v,136
TR v,168,239,16896
s_add_u32 s76, s76, s29
MA a,136,v,184,v,136
TR v,170,239,20992
CV 124,88,89
MA a,156,v,188,v,136
TR v,172,240,16896
MA v,228,v,216,v,136
TR v,174,240,20992
MC v,96,v,200,v,8,148
TR v,176,239,17408
MA v,96,v,204,v,28
TR v,178,239,21504
MA v,96,v,208,v,48
TR v,180,240,17408
MA v,96,v,212,v,68
TR v,182,240,21504
MC v,100,a,252,v,68,148
TR v,184,239,17920
MA v,100,a,248,v,48
TR v,186,239,22016
s_waitcnt lgkmcnt(6)
MA v,100,a,244,v,28
TR v,188,240,17920
MA v,100,a,240,v,8
TR v,190,240,22016
MA a,0,v,160,v,120
v_exp_f32 v90, v90
MA a,20,v,164,v,120
v_exp_f32 v91, v91
MA a,40,v,168,v,120
v_exp_f32 v92, v92
MA a,60,v,172,v,120
s_waitcnt lgkmcnt(0)
CV 125,90,91
MA a,80,v,176,v,120
v_exp_f32 v93, v93
MA a,100,v,180,v,120
v_exp_f32 v94, v94
MA a,120,v,184,v,120
v_exp_f32 v95, v95
MA a,140,v,188,v,120
v_exp_f32 v96, v96
MA v,192,v,216,v,120
s_add_u32 m0, s64, 32768
s_mov_b32 s70, 81920
MC v,104,v,200,v,12,152
v_exp_f32 v97, v97
MA v,104,v,204,v,32
v_exp_f32 v98, v98
MA v,104,v,208,v,52
v_exp_f32 v99, v99
MA v,104,v,212,v,72
buffer_load_dwordx4 v241, s[40:43], s70 offen offset:0 nt lds
MC v,108,a,252,v,72,152
CV 126,92,93
CV 127,94,95
MA v,108,a,248,v,52
v_exp_f32 v100, v100
MA v,108,a,244,v,32
v_exp_f32 v101, v101
MA v,108,a,240,v,12
buffer_load_dwordx4 v242, s[40:43], s70 offen offset:1024 nt lds
MA a,4,v,160,v,124
v_exp_f32 v102, v102
MA a,24,v,164,v,124
v_exp_f32 v103, v103
MA a,44,v,168,v,124
v_exp_f32 v104, v104
MA a,64,v,172,v,124
buffer_load_dwordx4 v241, s[40:43], s70 offen offset:2048 nt lds
MA a,84,v,176,v,124
v_exp_f32 v105, v105
MA a,104,v,180,v,124
v_exp_f32 v106, v106
MA a,124,v,184,v,124
v_exp_f32 v107, v107
MA a,144,v,188,v,124
buffer_load_dwordx4 v242, s[40:43], s70 offen offset:3072 nt lds
MA v,196,v,216,v,124
v_exp_f32 v108, v108
MC v,112,v,200,v,16,156
R4 v,200,238,49152
CV 128,96,97
MA v,112,v,204,v,36
R4 v,204,238,49664
CV 129,98,99
MA v,112,v,208,v,56
R4 v,208,238,50176
CV 130,100,101
MA v,112,v,212,v,76
R4 v,212,238,50688
CV 131,102,103
MC v,116,a,252,v,76,156
R4 a,252,238,54784
CV 132,104,105
MA v,116,a,248,v,56
R4 a,248,238,54272
CV 133,106,107
MA v,116,a,244,v,36
R4 a,244,238,53760
MA v,116,a,240,v,16
R4 a,240,238,53248
MA a,8,v,160,v,128
v_exp_f32 v109, v109
MA a,28,v,164,v,128
v_exp_f32 v110, v110
MA a,48,v,168,v,128
v_exp_f32 v111, v111
MA a,68,v,172,v,128
v_exp_f32 v112, v112
MA a,88,v,176,v,128
v_exp_f32 v113, v113
MA a,108,v,180,v,128
v_exp_f32 v114, v114
MA a,128,v,184,v,128
v_exp_f32 v115, v115
MA a,148,v,188,v,128
v_exp_f32 v116, v116
MA v,220,v,216,v,128
s_waitcnt lgkmcnt(0)
CV 134,108,109
MC v,80,v,200,v,0,140
v_exp_f32 v117, v117
MA v,80,v,204,v,20
v_exp_f32 v118, v118
MA v,80,v,208,v,40
v_exp_f32 v119, v119
MA v,80,v,212,v,60
CV 135,110,111
CV 136,112,113
MC v,84,a,252,v,60,140
CV 137,114,115
CV 138,116,117
MA v,84,a,248,v,40
CV 139,118,119
MA v,84,a,244,v,20
v_exp_f32 v80, v80
MA v,84,a,240,v,0
v_exp_f32 v81, v81
MA a,12,v,160,v,132
v_exp_f32 v82, v82
MA a,32,v,164,v,132
v_exp_f32 v83, v83
MA a,52,v,168,v,132
v_exp_f32 v84, v84
MA a,72,v,172,v,132
v_exp_f32 v85, v85
MA a,92,v,176,v,132
v_exp_f32 v86, v86
MA a,112,v,180,v,132
v_exp_f32 v87, v87
MA a,132,v,184,v,132
CV 120,80,81
CV 121,82,83
MA a,152,v,188,v,132
CV 122,84,85
CV 123,86,87
MA v,224,v,216,v,132
MC v,88,v,200,v,4,144
MA v,88,v,204,v,24
s_memrealtime s[98:99]
MA v,88,v,208,v,44
MA v,88,v,212,v,64
MC v,92,a,252,v,64,144
s_waitcnt lgkmcnt(0)
s_sub_i32 s98, s98, s76
MA v,92,a,248,v,44
s_cmp_lt_i32 s98, 0
s_cbranch_scc1 PACE0_7%=
PACED0_7%=:
MA v,92,a,244,v,24
v_exp_f32 v88, v88
MA v,92,a,240,v,4
v_exp_f32 v89, v89
MA a,16,v,160,v,136
s_waitcnt vmcnt(13)
s_barrier
MA a,36,v,164,v,136
TR v,160,239,32768
s_add_u32 s76, s76, s29
MA a,56,v,168,v,136
TR v,162,239,36864
CV 124,88,89
MA a,76,v,172,v,136
TR v,164,240,32768
MA a,96,v,176,v,136
TR v,166,240,36864
MA a,116,v,180,v,136
TR v,168,239,33280
MA a,136,v,184,v,136
TR v,170,239,37376
MA a,156,v,188,v,136
TR v,172,240,33280
MA v,228,v,216,v,136
TR v,174,240,37376
MC v,96,v,200,v,8,148
TR v,176,239,33792
MA v,96,v,204,v,28
TR v,178,239,37888
MA v,96,v,208,v,48
TR v,180,240,33792
MA v,96,v,212,v,68
TR v,182,240,37888
MC v,100,a,252,v,68,148
TR v,184,239,34304
MA v,100,a,248,v,48
TR v,186,239,38400
s_waitcnt lgkmcnt(6)
MA v,100,a,244,v,28
TR v,188,240,34304
MA v,100,a,240,v,8
TR v,190,240,38400
MA a,0,v,160,v,120
v_exp_f32 v90, v90
MA a,20,v,164,v,120
v_exp_f32 v91, v91
MA a,40,v,168,v,120
v_exp_f32 v92, v92
MA a,60,v,172,v,120
s_waitcnt lgkmcnt(0)
CV 125,90,91
MA a,80,v,176,v,120
v_exp_f32 v93, v93
MA a,100,v,180,v,120
v_exp_f32 v94, v94
MA a,120,v,184,v,120
v_exp_f32 v95, v95
MA a,140,v,188,v,120
v_exp_f32 v96, v96
MA v,192,v,216,v,120
s_add_u32 m0, s64, 65536
s_mov_b32 s67, 73728
MC v,104,v,200,v,12,152
v_exp_f32 v97, v97
MA v,104,v,204,v,32
v_exp_f32 v98, v98
MA v,104,v,208,v,52
v_exp_f32 v99, v99
MA v,104,v,212,v,72
buffer_load_dwordx4 v241, s[44:47], s67 offen offset:0 nt lds
MC v,108,a,252,v,72,152
CV 126,92,93
CV 127,94,95
MA v,108,a,248,v,52
v_exp_f32 v100, v100
MA v,108,a,244,v,32
v_exp_f32 v101, v101
MA v,108,a,240,v,12
buffer_load_dwordx4 v242, s[44:47], s67 offen offset:1024 nt lds
MA a,4,v,160,v,124
v_exp_f32 v102, v102
MA a,24,v,164,v,124
v_exp_f32 v103, v103
MA a,44,v,168,v,124
v_exp_f32 v104, v104
MA a,64,v,172,v,124
buffer_load_dwordx4 v241, s[44:47], s67 offen offset:2048 nt lds
MA a,84,v,176,v,124
v_exp_f32 v105, v105
MA a,104,v,180,v,124
v_exp_f32 v106, v106
MA a,124,v,184,v,124
v_exp_f32 v107, v107
MA a,144,v,188,v,124
buffer_load_dwordx4 v242, s[44:47], s67 offen offset:3072 nt lds
MA v,196,v,216,v,124
v_exp_f32 v108, v108
MC v,112,v,200,v,16,156
R4 v,200,238,0
CV 128,96,97
MA v,112,v,204,v,36
R4 v,204,238,512
CV 129,98,99
MA v,112,v,208,v,56
R4 v,208,238,1024
CV 130,100,101
MA v,112,v,212,v,76
R4 v,212,238,1536
CV 131,102,103
MC v,116,a,252,v,76,156
R4 a,252,238,5632
CV 132,104,105
MA v,116,a,248,v,56
R4 a,248,238,5120
CV 133,106,107
MA v,116,a,244,v,36
R4 a,244,238,4608
MA v,116,a,240,v,16
R4 a,240,238,4096
MA a,8,v,160,v,128
v_exp_f32 v109, v109
MA a,28,v,164,v,128
v_exp_f32 v110, v110
MA a,48,v,168,v,128
v_exp_f32 v111, v111
MA a,68,v,172,v,128
v_exp_f32 v112, v112
MA a,88,v,176,v,128
v_exp_f32 v113, v113
MA a,108,v,180,v,128
v_exp_f32 v114, v114
MA a,128,v,184,v,128
s_add_u32 m0, s64, 49152
s_mov_b32 s68, 90112
MA a,148,v,188,v,128
v_exp_f32 v115, v115
MA v,220,v,216,v,128
s_waitcnt lgkmcnt(0)
CV 134,108,109
MC v,80,v,200,v,0,140
v_exp_f32 v116, v116
MA v,80,v,204,v,20
buffer_load_dwordx4 v241, s[40:43], s68 offen offset:0 nt lds
MA v,80,v,208,v,40
v_exp_f32 v117, v117
MA v,80,v,212,v,60
v_exp_f32 v118, v118
MC v,84,a,252,v,60,140
CV 135,110,111
CV 136,112,113
MA v,84,a,248,v,40
buffer_load_dwordx4 v242, s[40:43], s68 offen offset:1024 nt lds
MA v,84,a,244,v,20
v_exp_f32 v119, v119
MA v,84,a,240,v,0
v_exp_f32 v80, v80
MA a,12,v,160,v,132
v_exp_f32 v81, v81
MA a,32,v,164,v,132
buffer_load_dwordx4 v241, s[40:43], s68 offen offset:2048 nt lds
MA a,52,v,168,v,132
v_exp_f32 v82, v82
MA a,72,v,172,v,132
v_exp_f32 v83, v83
MA a,92,v,176,v,132
v_exp_f32 v84, v84
MA a,112,v,180,v,132
buffer_load_dwordx4 v242, s[40:43], s68 offen offset:3072 nt lds
MA a,132,v,184,v,132
v_exp_f32 v85, v85
MA a,152,v,188,v,132
v_exp_f32 v86, v86
MA v,224,v,216,v,132
v_exp_f32 v87, v87
MC v,88,v,200,v,4,144
CV 137,114,115
CV 138,116,117
MA v,88,v,204,v,24
s_memrealtime s[98:99]
CV 139,118,119
MA v,88,v,208,v,44
CV 120,80,81
CV 121,82,83
MA v,88,v,212,v,64
CV 122,84,85
CV 123,86,87
MC v,92,a,252,v,64,144
s_waitcnt lgkmcnt(0)
s_sub_i32 s98, s98, s76
MA v,92,a,248,v,44
s_cmp_lt_i32 s98, 0
s_cbranch_scc1 PACE0_8%=
PACED0_8%=:
MA v,92,a,244,v,24
v_exp_f32 v88, v88
MA v,92,a,240,v,4
v_exp_f32 v89, v89
MA a,16,v,160,v,136
s_waitcnt vmcnt(13)
s_barrier
MA a,36,v,164,v,136
TR v,160,239,49152
s_add_u32 s76, s76, s29
MA a,56,v,168,v,136
TR v,162,239,53248
CV 124,88,89
MA a,76,v,172,v,136
TR v,164,240,49152
MA a,96,v,176,v,136
TR v,166,240,53248
MA a,116,v,180,v,136
TR v,168,239,49664
MA a,136,v,184,v,136
TR v,170,239,53760
MA a,156,v,188,v,136
TR v,172,240,49664
MA v,228,v,216,v,136
TR v,174,240,53760
MC v,96,v,200,v,8,148
TR v,176,239,50176
MA v,96,v,204,v,28
TR v,178,239,54272
MA v,96,v,208,v,48
TR v,180,240,50176
MA v,96,v,212,v,68
TR v,182,240,54272
MC v,100,a,252,v,68,148
TR v,184,239,50688
MA v,100,a,248,v,48
TR v,186,239,54784
s_waitcnt lgkmcnt(6)
MA v,100,a,244,v,28
TR v,188,240,50688
MA v,100,a,240,v,8
TR v,190,240,54784
MA a,0,v,160,v,120
v_exp_f32 v90, v90
MA a,20,v,164,v,120
v_exp_f32 v91, v91
MA a,40,v,168,v,120
v_exp_f32 v92, v92
MA a,60,v,172,v,120
s_waitcnt lgkmcnt(0)
CV 125,90,91
MA a,80,v,176,v,120
v_exp_f32 v93, v93
MA a,100,v,180,v,120
v_exp_f32 v94, v94
MA a,120,v,184,v,120
v_exp_f32 v95, v95
MA a,140,v,188,v,120
v_exp_f32 v96, v96
MA v,192,v,216,v,120
s_add_u32 m0, s64, 81920
s_mov_b32 s69, 81920
MC v,104,v,200,v,12,152
v_exp_f32 v97, v97
MA v,104,v,204,v,32
v_exp_f32 v98, v98
MA v,104,v,208,v,52
v_exp_f32 v99, v99
MA v,104,v,212,v,72
buffer_load_dwordx4 v241, s[44:47], s69 offen offset:0 nt lds
MC v,108,a,252,v,72,152
CV 126,92,93
CV 127,94,95
MA v,108,a,248,v,52
v_exp_f32 v100, v100
MA v,108,a,244,v,32
v_exp_f32 v101, v101
MA v,108,a,240,v,12
buffer_load_dwordx4 v242, s[44:47], s69 offen offset:1024 nt lds
MA a,4,v,160,v,124
v_exp_f32 v102, v102
MA a,24,v,164,v,124
v_exp_f32 v103, v103
MA a,44,v,168,v,124
v_exp_f32 v104, v104
MA a,64,v,172,v,124
buffer_load_dwordx4 v241, s[44:47], s69 offen offset:2048 nt lds
MA a,84,v,176,v,124
v_exp_f32 v105, v105
MA a,104,v,180,v,124
v_exp_f32 v106, v106
MA a,124,v,184,v,124
v_exp_f32 v107, v107
MA a,144,v,188,v,124
buffer_load_dwordx4 v242, s[44:47], s69 offen offset:3072 nt lds
MA v,196,v,216,v,124
v_exp_f32 v108, v108
MC v,112,v,200,v,16,156
R4 v,200,238,16384
CV 128,96,97
MA v,112,v,204,v,36
R4 v,204,238,16896
CV 129,98,99
MA v,112,v,208,v,56
R4 v,208,238,17408
CV 130,100,101
MA v,112,v,212,v,76
R4 v,212,238,17920
CV 131,102,103
MC v,116,a,252,v,76,156
R4 a,252,238,22016
CV 132,104,105
MA v,116,a,248,v,56
R4 a,248,238,21504
CV 133,106,107
MA v,116,a,244,v,36
R4 a,244,238,20992
MA v,116,a,240,v,16
R4 a,240,238,20480
MA a,8,v,160,v,128
v_exp_f32 v109, v109
MA a,28,v,164,v,128
v_exp_f32 v110, v110
MA a,48,v,168,v,128
v_exp_f32 v111, v111
MA a,68,v,172,v,128
v_exp_f32 v112, v112
MA a,88,v,176,v,128
v_exp_f32 v113, v113
MA a,108,v,180,v,128
v_exp_f32 v114, v114
MA a,128,v,184,v,128
s_add_u32 m0, s64, 0
s_mov_b32 s70, 98304
MA a,148,v,188,v,128
v_exp_f32 v115, v115
MA v,220,v,216,v,128
s_waitcnt lgkmcnt(0)
CV 134,108,109
MC v,80,v,200,v,0,140
v_exp_f32 v116, v116
MA v,80,v,204,v,20
buffer_load_dwordx4 v241, s[40:43], s70 offen offset:0 nt lds
MA v,80,v,208,v,40
v_exp_f32 v117, v117
MA v,80,v,212,v,60
v_exp_f32 v118, v118
MC v,84,a,252,v,60,140
CV 135,110,111
CV 136,112,113
MA v,84,a,248,v,40
buffer_load_dwordx4 v242, s[40:43], s70 offen offset:1024 nt lds
MA v,84,a,244,v,20
v_exp_f32 v119, v119
MA v,84,a,240,v,0
v_exp_f32 v80, v80
MA a,12,v,160,v,132
v_exp_f32 v81, v81
MA a,32,v,164,v,132
buffer_load_dwordx4 v241, s[40:43], s70 offen offset:2048 nt lds
MA a,52,v,168,v,132
v_exp_f32 v82, v82
MA a,72,v,172,v,132
v_exp_f32 v83, v83
MA a,92,v,176,v,132
v_exp_f32 v84, v84
MA a,112,v,180,v,132
buffer_load_dwordx4 v242, s[40:43], s70 offen offset:3072 nt lds
MA a,132,v,184,v,132
v_exp_f32 v85, v85
MA a,152,v,188,v,132
v_exp_f32 v86, v86
MA v,224,v,216,v,132
v_exp_f32 v87, v87
MC v,88,v,200,v,4,144
CV 137,114,115
CV 138,116,117
MA v,88,v,204,v,24
s_memrealtime s[98:99]
CV 139,118,119
MA v,88,v,208,v,44
CV 120,80,81
CV 121,82,83
MA v,88,v,212,v,64
CV 122,84,85
)ASM"
R"ASM(CV 123,86,87
MC v,92,a,252,v,64,144
s_waitcnt lgkmcnt(0)
s_sub_i32 s98, s98, s76
MA v,92,a,248,v,44
s_cmp_lt_i32 s98, 0
s_cbranch_scc1 PACE0_9%=
PACED0_9%=:
MA v,92,a,244,v,24
v_exp_f32 v88, v88
MA v,92,a,240,v,4
v_exp_f32 v89, v89
MA a,16,v,160,v,136
s_waitcnt vmcnt(12)
s_barrier
MA a,36,v,164,v,136
TR v,160,239,0
s_add_u32 s76, s76, s29
MA a,56,v,168,v,136
TR v,162,239,4096
CV 124,88,89
MA a,76,v,172,v,136
TR v,164,240,0
MA a,96,v,176,v,136
TR v,166,240,4096
MA a,116,v,180,v,136
TR v,168,239,512
MA a,136,v,184,v,136
TR v,170,239,4608
MA a,156,v,188,v,136
TR v,172,240,512
MA v,228,v,216,v,136
TR v,174,240,4608
MC v,96,v,200,v,8,148
TR v,176,239,1024
MA v,96,v,204,v,28
TR v,178,239,5120
MA v,96,v,208,v,48
TR v,180,240,1024
MA v,96,v,212,v,68
TR v,182,240,5120
MC v,100,a,252,v,68,148
TR v,184,239,1536
MA v,100,a,248,v,48
TR v,186,239,5632
s_waitcnt lgkmcnt(6)
MA v,100,a,244,v,28
TR v,188,240,1536
MA v,100,a,240,v,8
TR v,190,240,5632
MA a,0,v,160,v,120
v_exp_f32 v90, v90
MA a,20,v,164,v,120
v_exp_f32 v91, v91
MA a,40,v,168,v,120
v_exp_f32 v92, v92
MA a,60,v,172,v,120
s_waitcnt lgkmcnt(0)
CV 125,90,91
MA a,80,v,176,v,120
v_exp_f32 v93, v93
MA a,100,v,180,v,120
v_exp_f32 v94, v94
MA a,120,v,184,v,120
v_exp_f32 v95, v95
MA a,140,v,188,v,120
v_exp_f32 v96, v96
MA v,192,v,216,v,120
s_add_u32 m0, s64, 98304
s_mov_b32 s67, 90112
MC v,104,v,200,v,12,152
v_exp_f32 v97, v97
MA v,104,v,204,v,32
v_exp_f32 v98, v98
MA v,104,v,208,v,52
v_exp_f32 v99, v99
MA v,104,v,212,v,72
buffer_load_dwordx4 v241, s[44:47], s67 offen offset:0 nt lds
MC v,108,a,252,v,72,152
CV 126,92,93
CV 127,94,95
MA v,108,a,248,v,52
v_exp_f32 v100, v100
MA v,108,a,244,v,32
v_exp_f32 v101, v101
MA v,108,a,240,v,12
buffer_load_dwordx4 v242, s[44:47], s67 offen offset:1024 nt lds
MA a,4,v,160,v,124
v_exp_f32 v102, v102
MA a,24,v,164,v,124
v_exp_f32 v103, v103
MA a,44,v,168,v,124
v_exp_f32 v104, v104
MA a,64,v,172,v,124
buffer_load_dwordx4 v241, s[44:47], s67 offen offset:2048 nt lds
MA a,84,v,176,v,124
v_exp_f32 v105, v105
MA a,104,v,180,v,124
v_exp_f32 v106, v106
MA a,124,v,184,v,124
v_exp_f32 v107, v107
MA a,144,v,188,v,124
buffer_load_dwordx4 v242, s[44:47], s67 offen offset:3072 nt lds
MA v,196,v,216,v,124
v_exp_f32 v108, v108
MC v,112,v,200,v,16,156
R4 v,200,238,32768
CV 128,96,97
MA v,112,v,204,v,36
R4 v,204,238,33280
CV 129,98,99
MA v,112,v,208,v,56
R4 v,208,238,33792
CV 130,100,101
MA v,112,v,212,v,76
R4 v,212,238,34304
CV 131,102,103
MC v,116,a,252,v,76,156
R4 a,252,238,38400
CV 132,104,105
MA v,116,a,248,v,56
R4 a,248,238,37888
CV 133,106,107
MA v,116,a,244,v,36
R4 a,244,238,37376
MA v,116,a,240,v,16
R4 a,240,238,36864
MA a,8,v,160,v,128
v_exp_f32 v109, v109
MA a,28,v,164,v,128
v_exp_f32 v110, v110
MA a,48,v,168,v,128
v_exp_f32 v111, v111
MA a,68,v,172,v,128
v_exp_f32 v112, v112
MA a,88,v,176,v,128
v_exp_f32 v113, v113
MA a,108,v,180,v,128
v_exp_f32 v114, v114
MA a,128,v,184,v,128
v_exp_f32 v115, v115
MA a,148,v,188,v,128
v_exp_f32 v116, v116
MA v,220,v,216,v,128
s_waitcnt lgkmcnt(0)
CV 134,108,109
MC v,80,v,200,v,0,140
v_exp_f32 v117, v117
MA v,80,v,204,v,20
v_exp_f32 v118, v118
MA v,80,v,208,v,40
v_exp_f32 v119, v119
MA v,80,v,212,v,60
CV 135,110,111
CV 136,112,113
MC v,84,a,252,v,60,140
CV 137,114,115
CV 138,116,117
MA v,84,a,248,v,40
CV 139,118,119
MA v,84,a,244,v,20
v_exp_f32 v80, v80
MA v,84,a,240,v,0
v_exp_f32 v81, v81
MA a,12,v,160,v,132
v_exp_f32 v82, v82
MA a,32,v,164,v,132
v_exp_f32 v83, v83
MA a,52,v,168,v,132
v_exp_f32 v84, v84
MA a,72,v,172,v,132
v_exp_f32 v85, v85
MA a,92,v,176,v,132
v_exp_f32 v86, v86
MA a,112,v,180,v,132
v_exp_f32 v87, v87
MA a,132,v,184,v,132
CV 120,80,81
CV 121,82,83
MA a,152,v,188,v,132
CV 122,84,85
CV 123,86,87
MA v,224,v,216,v,132
MC v,88,v,200,v,4,144
MA v,88,v,204,v,24
s_memrealtime s[98:99]
MA v,88,v,208,v,44
MA v,88,v,212,v,64
MC v,92,a,252,v,64,144
s_waitcnt lgkmcnt(0)
s_sub_i32 s98, s98, s76
MA v,92,a,248,v,44
s_cmp_lt_i32 s98, 0
s_cbranch_scc1 PACE0_10%=
PACED0_10%=:
MA v,92,a,244,v,24
v_exp_f32 v88, v88
MA v,92,a,240,v,4
v_exp_f32 v89, v89
MA a,16,v,160,v,136
s_waitcnt vmcnt(8)
s_barrier
MA a,36,v,164,v,136
TR v,160,239,16384
s_add_u32 s76, s76, s29
MA a,56,v,168,v,136
TR v,162,239,20480
CV 124,88,89
MA a,76,v,172,v,136
TR v,164,240,16384
MA a,96,v,176,v,136
TR v,166,240,20480
MA a,116,v,180,v,136
TR v,168,239,16896
MA a,136,v,184,v,136
TR v,170,239,20992
MA a,156,v,188,v,136
TR v,172,240,16896
MA v,228,v,216,v,136
TR v,174,240,20992
MC v,96,v,200,v,8,148
TR v,176,239,17408
MA v,96,v,204,v,28
TR v,178,239,21504
MA v,96,v,208,v,48
TR v,180,240,17408
MA v,96,v,212,v,68
TR v,182,240,21504
MC v,100,a,252,v,68,148
TR v,184,239,17920
MA v,100,a,248,v,48
TR v,186,239,22016
s_waitcnt lgkmcnt(6)
MA v,100,a,244,v,28
TR v,188,240,17920
MA v,100,a,240,v,8
TR v,190,240,22016
MA a,0,v,160,v,120
v_exp_f32 v90, v90
MA a,20,v,164,v,120
v_exp_f32 v91, v91
MA a,40,v,168,v,120
v_exp_f32 v92, v92
MA a,60,v,172,v,120
s_waitcnt lgkmcnt(0)
CV 125,90,91
MA a,80,v,176,v,120
v_exp_f32 v93, v93
MA a,100,v,180,v,120
s_mov_b32 m0, 161792
s_min_u32 s94, s30, 2
MA a,120,v,184,v,120
s_sub_u32 s30, s30, s94
s_mov_b32 s68, 49152
MA a,140,v,188,v,120
v_exp_f32 v94, v94
MA v,192,v,216,v,120
v_exp_f32 v95, v95
MC v,104,v,200,v,12,152
buffer_load_dword v255, s[92:95], s68 idxen lds
MA v,104,v,204,v,32
s_add_u32 m0, s64, 16384
s_mov_b32 s68, 106496
MA v,104,v,208,v,52
v_exp_f32 v96, v96
MA v,104,v,212,v,72
v_exp_f32 v97, v97
MC v,108,a,252,v,72,152
buffer_load_dwordx4 v241, s[40:43], s68 offen offset:0 nt lds
MA v,108,a,248,v,52
CV 126,92,93
CV 127,94,95
MA v,108,a,244,v,32
v_exp_f32 v98, v98
MA v,108,a,240,v,12
v_exp_f32 v99, v99
MA a,4,v,160,v,124
buffer_load_dwordx4 v242, s[40:43], s68 offen offset:1024 nt lds
MA a,24,v,164,v,124
v_exp_f32 v100, v100
MA a,44,v,168,v,124
v_exp_f32 v101, v101
MA a,64,v,172,v,124
v_exp_f32 v102, v102
MA a,84,v,176,v,124
buffer_load_dwordx4 v241, s[40:43], s68 offen offset:2048 nt lds
MA a,104,v,180,v,124
v_exp_f32 v103, v103
MA a,124,v,184,v,124
v_exp_f32 v104, v104
MA a,144,v,188,v,124
v_exp_f32 v105, v105
MA v,196,v,216,v,124
buffer_load_dwordx4 v242, s[40:43], s68 offen offset:3072 nt lds
MC v,112,v,200,v,16,156
R4 v,200,238,49152
CV 128,96,97
MA v,112,v,204,v,36
R4 v,204,238,49664
CV 129,98,99
MA v,112,v,208,v,56
R4 v,208,238,50176
CV 130,100,101
MA v,112,v,212,v,76
R4 v,212,238,50688
CV 131,102,103
MC v,116,a,252,v,76,156
R4 a,252,238,54784
CV 132,104,105
MA v,116,a,248,v,56
R4 a,248,238,54272
MA v,116,a,244,v,36
R4 a,244,238,53760
MA v,116,a,240,v,16
R4 a,240,238,53248
MA a,8,v,160,v,128
v_exp_f32 v106, v106
MA a,28,v,164,v,128
v_exp_f32 v107, v107
MA a,48,v,168,v,128
v_exp_f32 v108, v108
MA a,68,v,172,v,128
v_exp_f32 v109, v109
MA a,88,v,176,v,128
v_exp_f32 v110, v110
MA a,108,v,180,v,128
v_exp_f32 v111, v111
MA a,128,v,184,v,128
s_add_u32 m0, s64, 114688
s_mov_b32 s69, 98304
MA a,148,v,188,v,128
v_exp_f32 v112, v112
MA v,220,v,216,v,128
s_waitcnt lgkmcnt(0)
CV 133,106,107
MC v,80,v,200,v,0,140
v_exp_f32 v113, v113
MA v,80,v,204,v,20
buffer_load_dwordx4 v241, s[44:47], s69 offen offset:0 nt lds
MA v,80,v,208,v,40
v_exp_f32 v114, v114
MA v,80,v,212,v,60
v_exp_f32 v115, v115
MC v,84,a,252,v,60,140
CV 134,108,109
CV 135,110,111
MA v,84,a,248,v,40
buffer_load_dwordx4 v242, s[44:47], s69 offen offset:1024 nt lds
MA v,84,a,244,v,20
v_exp_f32 v116, v116
MA v,84,a,240,v,0
v_exp_f32 v117, v117
MA a,12,v,160,v,132
v_exp_f32 v118, v118
MA a,32,v,164,v,132
buffer_load_dwordx4 v241, s[44:47], s69 offen offset:2048 nt lds
MA a,52,v,168,v,132
v_exp_f32 v119, v119
MA a,72,v,172,v,132
v_exp_f32 v80, v80
MA a,92,v,176,v,132
v_exp_f32 v81, v81
MA a,112,v,180,v,132
buffer_load_dwordx4 v242, s[44:47], s69 offen offset:3072 nt lds
MA a,132,v,184,v,132
v_exp_f32 v82, v82
MA a,152,v,188,v,132
v_exp_f32 v83, v83
MA v,224,v,216,v,132
v_exp_f32 v84, v84
MC v,88,v,200,v,4,144
v_exp_f32 v85, v85
MA v,88,v,204,v,24
s_memrealtime s[98:99]
CV 136,112,113
MA v,88,v,208,v,44
v_exp_f32 v86, v86
MA v,88,v,212,v,64
v_exp_f32 v87, v87
MC v,92,a,252,v,64,144
s_waitcnt lgkmcnt(0)
s_sub_i32 s98, s98, s76
MA v,92,a,248,v,44
s_cmp_lt_i32 s98, 0
s_cbranch_scc1 PACE0_11%=
PACED0_11%=:
CV 137,114,115
CV 138,116,117
CV 139,118,119
MA v,92,a,244,v,24
v_exp_f32 v88, v88
MA v,92,a,240,v,4
v_exp_f32 v89, v89
MA a,16,v,160,v,136
s_waitcnt vmcnt(9)
s_barrier
MA a,36,v,164,v,136
TR v,160,239,32768
CV 120,80,81
MA a,56,v,168,v,136
TR v,162,239,36864
CV 121,82,83
MA a,76,v,172,v,136
TR v,164,240,32768
CV 122,84,85
MA a,96,v,176,v,136
TR v,166,240,36864
CV 123,86,87
MA a,116,v,180,v,136
TR v,168,239,33280
s_add_u32 s76, s76, s29
MA a,136,v,184,v,136
TR v,170,239,37376
CV 124,88,89
MA a,156,v,188,v,136
TR v,172,240,33280
MA v,228,v,216,v,136
TR v,174,240,37376
MC v,96,v,200,v,8,148
TR v,176,239,33792
MA v,96,v,204,v,28
TR v,178,239,37888
MA v,96,v,208,v,48
TR v,180,240,33792
MA v,96,v,212,v,68
TR v,182,240,37888
MC v,100,a,252,v,68,148
TR v,184,239,34304
MA v,100,a,248,v,48
TR v,186,239,38400
s_waitcnt lgkmcnt(6)
MA v,100,a,244,v,28
TR v,188,240,34304
MA v,100,a,240,v,8
TR v,190,240,38400
MA a,0,v,160,v,120
v_exp_f32 v90, v90
MA a,20,v,164,v,120
v_exp_f32 v91, v91
MA a,40,v,168,v,120
v_exp_f32 v92, v92
MA a,60,v,172,v,120
s_waitcnt lgkmcnt(0)
CV 125,90,91
MA a,80,v,176,v,120
v_exp_f32 v93, v93
MA a,100,v,180,v,120
v_exp_f32 v94, v94
MA a,120,v,184,v,120
v_exp_f32 v95, v95
MA a,140,v,188,v,120
v_exp_f32 v96, v96
MA v,192,v,216,v,120
s_add_u32 m0, s64, 32768
s_mov_b32 s70, 114688
MC v,104,v,200,v,12,152
v_exp_f32 v97, v97
MA v,104,v,204,v,32
v_exp_f32 v98, v98
MA v,104,v,208,v,52
v_exp_f32 v99, v99
MA v,104,v,212,v,72
buffer_load_dwordx4 v241, s[40:43], s70 offen offset:0 nt lds
MC v,108,a,252,v,72,152
CV 126,92,93
CV 127,94,95
MA v,108,a,248,v,52
v_exp_f32 v100, v100
MA v,108,a,244,v,32
v_exp_f32 v101, v101
MA v,108,a,240,v,12
buffer_load_dwordx4 v242, s[40:43], s70 offen offset:1024 nt lds
MA a,4,v,160,v,124
v_exp_f32 v102, v102
MA a,24,v,164,v,124
v_exp_f32 v103, v103
MA a,44,v,168,v,124
v_exp_f32 v104, v104
MA a,64,v,172,v,124
buffer_load_dwordx4 v241, s[40:43], s70 offen offset:2048 nt lds
MA a,84,v,176,v,124
v_exp_f32 v105, v105
MA a,104,v,180,v,124
v_exp_f32 v106, v106
MA a,124,v,184,v,124
v_exp_f32 v107, v107
MA a,144,v,188,v,124
buffer_load_dwordx4 v242, s[40:43], s70 offen offset:3072 nt lds
MA v,196,v,216,v,124
v_exp_f32 v108, v108
MC v,112,v,200,v,16,156
R4 v,200,238,0
CV 128,96,97
MA v,112,v,204,v,36
R4 v,204,238,512
CV 129,98,99
MA v,112,v,208,v,56
R4 v,208,238,1024
CV 130,100,101
MA v,112,v,212,v,76
R4 v,212,238,1536
CV 131,102,103
MC v,116,a,252,v,76,156
R4 a,252,238,5632
CV 132,104,105
MA v,116,a,248,v,56
R4 a,248,238,5120
CV 133,106,107
MA v,116,a,244,v,36
R4 a,244,238,4608
MA v,116,a,240,v,16
R4 a,240,238,4096
MA a,8,v,160,v,128
v_exp_f32 v109, v109
MA a,28,v,164,v,128
v_exp_f32 v110, v110
MA a,48,v,168,v,128
v_exp_f32 v111, v111
MA a,68,v,172,v,128
v_exp_f32 v112, v112
MA a,88,v,176,v,128
v_exp_f32 v113, v113
MA a,108,v,180,v,128
v_exp_f32 v114, v114
MA a,128,v,184,v,128
s_add_u32 m0, s64, 65536
s_mov_b32 s67, 106496
MA a,148,v,188,v,128
v_exp_f32 v115, v115
MA v,220,v,216,v,128
s_waitcnt lgkmcnt(0)
CV 134,108,109
MC v,80,v,200,v,0,140
v_exp_f32 v116, v116
MA v,80,v,204,v,20
buffer_load_dwordx4 v241, s[44:47], s67 offen offset:0 nt lds
MA v,80,v,208,v,40
v_exp_f32 v117, v117
MA v,80,v,212,v,60
v_exp_f32 v118, v118
MC v,84,a,252,v,60,140
CV 135,110,111
CV 136,112,113
MA v,84,a,248,v,40
buffer_load_dwordx4 v242, s[44:47], s67 offen offset:1024 nt lds
MA v,84,a,244,v,20
v_exp_f32 v119, v119
MA v,84,a,240,v,0
v_exp_f32 v80, v80
MA a,12,v,160,v,132
v_exp_f32 v81, v81
MA a,32,v,164,v,132
buffer_load_dwordx4 v241, s[44:47], s67 offen offset:2048 nt lds
MA a,52,v,168,v,132
v_exp_f32 v82, v82
MA a,72,v,172,v,132
v_exp_f32 v83, v83
MA a,92,v,176,v,132
v_exp_f32 v84, v84
MA a,112,v,180,v,132
buffer_load_dwordx4 v242, s[44:47], s67 offen offset:3072 nt lds
MA a,132,v,184,v,132
v_exp_f32 v85, v85
MA a,152,v,188,v,132
v_exp_f32 v86, v86
MA v,224,v,216,v,132
v_exp_f32 v87, v87
MC v,88,v,200,v,4,144
CV 137,114,115
CV 138,116,117
)ASM"
R"ASM(MA v,88,v,204,v,24
s_memrealtime s[98:99]
CV 139,118,119
MA v,88,v,208,v,44
CV 120,80,81
CV 121,82,83
MA v,88,v,212,v,64
CV 122,84,85
CV 123,86,87
MC v,92,a,252,v,64,144
s_waitcnt lgkmcnt(0)
s_sub_i32 s98, s98, s76
MA v,92,a,248,v,44
s_cmp_lt_i32 s98, 0
s_cbranch_scc1 PACE0_12%=
PACED0_12%=:
MA v,92,a,244,v,24
v_exp_f32 v88, v88
MA v,92,a,240,v,4
v_exp_f32 v89, v89
MA a,16,v,160,v,136
s_waitcnt vmcnt(8)
s_barrier
MA a,36,v,164,v,136
TR v,160,239,49152
s_add_u32 s76, s76, s29
MA a,56,v,168,v,136
TR v,162,239,53248
CV 124,88,89
MA a,76,v,172,v,136
TR v,164,240,49152
MA a,96,v,176,v,136
TR v,166,240,53248
MA a,116,v,180,v,136
TR v,168,239,49664
MA a,136,v,184,v,136
TR v,170,239,53760
MA a,156,v,188,v,136
TR v,172,240,49664
MA v,228,v,216,v,136
TR v,174,240,53760
MC v,96,v,200,v,8,148
TR v,176,239,50176
MA v,96,v,204,v,28
TR v,178,239,54272
MA v,96,v,208,v,48
TR v,180,240,50176
MA v,96,v,212,v,68
TR v,182,240,54272
MC v,100,a,252,v,68,148
TR v,184,239,50688
MA v,100,a,248,v,48
TR v,186,239,54784
s_waitcnt lgkmcnt(6)
MA v,100,a,244,v,28
TR v,188,240,50688
MA v,100,a,240,v,8
TR v,190,240,54784
MA a,0,v,160,v,120
v_exp_f32 v90, v90
MA a,20,v,164,v,120
v_exp_f32 v91, v91
MA a,40,v,168,v,120
v_exp_f32 v92, v92
MA a,60,v,172,v,120
s_waitcnt lgkmcnt(0)
CV 125,90,91
MA a,80,v,176,v,120
v_exp_f32 v93, v93
MA a,100,v,180,v,120
v_exp_f32 v94, v94
MA a,120,v,184,v,120
v_exp_f32 v95, v95
MA a,140,v,188,v,120
v_exp_f32 v96, v96
MA v,192,v,216,v,120
s_add_u32 m0, s64, 49152
s_mov_b32 s68, 122880
MC v,104,v,200,v,12,152
v_exp_f32 v97, v97
MA v,104,v,204,v,32
v_exp_f32 v98, v98
MA v,104,v,208,v,52
v_exp_f32 v99, v99
MA v,104,v,212,v,72
buffer_load_dwordx4 v241, s[40:43], s68 offen offset:0 nt lds
MC v,108,a,252,v,72,152
CV 126,92,93
CV 127,94,95
MA v,108,a,248,v,52
v_exp_f32 v100, v100
MA v,108,a,244,v,32
v_exp_f32 v101, v101
MA v,108,a,240,v,12
buffer_load_dwordx4 v242, s[40:43], s68 offen offset:1024 nt lds
MA a,4,v,160,v,124
v_exp_f32 v102, v102
MA a,24,v,164,v,124
v_exp_f32 v103, v103
MA a,44,v,168,v,124
v_exp_f32 v104, v104
MA a,64,v,172,v,124
buffer_load_dwordx4 v241, s[40:43], s68 offen offset:2048 nt lds
MA a,84,v,176,v,124
v_exp_f32 v105, v105
MA a,104,v,180,v,124
v_exp_f32 v106, v106
MA a,124,v,184,v,124
v_exp_f32 v107, v107
MA a,144,v,188,v,124
buffer_load_dwordx4 v242, s[40:43], s68 offen offset:3072 nt lds
MA v,196,v,216,v,124
v_exp_f32 v108, v108
MC v,112,v,200,v,16,156
R4 v,200,238,16384
CV 128,96,97
MA v,112,v,204,v,36
R4 v,204,238,16896
CV 129,98,99
MA v,112,v,208,v,56
R4 v,208,238,17408
CV 130,100,101
MA v,112,v,212,v,76
R4 v,212,238,17920
CV 131,102,103
MC v,116,a,252,v,76,156
R4 a,252,238,22016
CV 132,104,105
MA v,116,a,248,v,56
R4 a,248,238,21504
CV 133,106,107
MA v,116,a,244,v,36
R4 a,244,238,20992
MA v,116,a,240,v,16
R4 a,240,238,20480
MA a,8,v,160,v,128
v_exp_f32 v109, v109
MA a,28,v,164,v,128
v_exp_f32 v110, v110
MA a,48,v,168,v,128
v_exp_f32 v111, v111
MA a,68,v,172,v,128
v_exp_f32 v112, v112
MA a,88,v,176,v,128
v_exp_f32 v113, v113
MA a,108,v,180,v,128
v_exp_f32 v114, v114
MA a,128,v,184,v,128
s_add_u32 m0, s64, 81920
s_mov_b32 s69, 114688
MA a,148,v,188,v,128
v_exp_f32 v115, v115
MA v,220,v,216,v,128
s_waitcnt lgkmcnt(0)
CV 134,108,109
MC v,80,v,200,v,0,140
v_exp_f32 v116, v116
MA v,80,v,204,v,20
buffer_load_dwordx4 v241, s[44:47], s69 offen offset:0 nt lds
MA v,80,v,208,v,40
v_exp_f32 v117, v117
MA v,80,v,212,v,60
v_exp_f32 v118, v118
MC v,84,a,252,v,60,140
CV 135,110,111
CV 136,112,113
MA v,84,a,248,v,40
buffer_load_dwordx4 v242, s[44:47], s69 offen offset:1024 nt lds
MA v,84,a,244,v,20
v_exp_f32 v119, v119
MA v,84,a,240,v,0
v_exp_f32 v80, v80
MA a,12,v,160,v,132
v_exp_f32 v81, v81
MA a,32,v,164,v,132
buffer_load_dwordx4 v241, s[44:47], s69 offen offset:2048 nt lds
MA a,52,v,168,v,132
v_exp_f32 v82, v82
MA a,72,v,172,v,132
v_exp_f32 v83, v83
MA a,92,v,176,v,132
v_exp_f32 v84, v84
MA a,112,v,180,v,132
buffer_load_dwordx4 v242, s[44:47], s69 offen offset:3072 nt lds
MA a,132,v,184,v,132
v_exp_f32 v85, v85
MA a,152,v,188,v,132
v_exp_f32 v86, v86
MA v,224,v,216,v,132
v_exp_f32 v87, v87
MC v,88,v,200,v,4,144
CV 137,114,115
CV 138,116,117
MA v,88,v,204,v,24
s_memrealtime s[98:99]
CV 139,118,119
MA v,88,v,208,v,44
CV 120,80,81
CV 121,82,83
MA v,88,v,212,v,64
CV 122,84,85
CV 123,86,87
MC v,92,a,252,v,64,144
s_waitcnt lgkmcnt(0)
s_sub_i32 s98, s98, s76
MA v,92,a,248,v,44
s_cmp_lt_i32 s98, 0
s_cbranch_scc1 PACE0_13%=
PACED0_13%=:
MA v,92,a,244,v,24
v_exp_f32 v88, v88
MA v,92,a,240,v,4
v_exp_f32 v89, v89
MA a,16,v,160,v,136
s_waitcnt vmcnt(8)
s_barrier
MA a,36,v,164,v,136
TR v,160,239,0
s_add_u32 s76, s76, s29
MA a,56,v,168,v,136
TR v,162,239,4096
CV 124,88,89
MA a,76,v,172,v,136
TR v,164,240,0
MA a,96,v,176,v,136
TR v,166,240,4096
MA a,116,v,180,v,136
TR v,168,239,512
MA a,136,v,184,v,136
TR v,170,239,4608
MA a,156,v,188,v,136
TR v,172,240,512
MA v,228,v,216,v,136
TR v,174,240,4608
MC v,96,v,200,v,8,148
TR v,176,239,1024
MA v,96,v,204,v,28
TR v,178,239,5120
MA v,96,v,208,v,48
TR v,180,240,1024
MA v,96,v,212,v,68
TR v,182,240,5120
MC v,100,a,252,v,68,148
TR v,184,239,1536
MA v,100,a,248,v,48
TR v,186,239,5632
s_waitcnt lgkmcnt(6)
MA v,100,a,244,v,28
TR v,188,240,1536
MA v,100,a,240,v,8
TR v,190,240,5632
MA a,0,v,160,v,120
v_exp_f32 v90, v90
MA a,20,v,164,v,120
v_exp_f32 v91, v91
MA a,40,v,168,v,120
v_exp_f32 v92, v92
MA a,60,v,172,v,120
s_waitcnt lgkmcnt(0)
CV 125,90,91
MA a,80,v,176,v,120
v_exp_f32 v93, v93
MA a,100,v,180,v,120
v_exp_f32 v94, v94
MA a,120,v,184,v,120
v_exp_f32 v95, v95
MA a,140,v,188,v,120
v_exp_f32 v96, v96
MA v,192,v,216,v,120
v_exp_f32 v97, v97
MC v,104,v,200,v,12,152
v_exp_f32 v98, v98
MA v,104,v,204,v,32
v_exp_f32 v99, v99
MA v,104,v,208,v,52
v_exp_f32 v100, v100
MA v,104,v,212,v,72
v_exp_f32 v101, v101
MC v,108,a,252,v,72,152
CV 126,92,93
CV 127,94,95
MA v,108,a,248,v,52
v_exp_f32 v102, v102
MA v,108,a,244,v,32
v_exp_f32 v103, v103
MA v,108,a,240,v,12
v_exp_f32 v104, v104
MA a,4,v,160,v,124
v_exp_f32 v105, v105
MA a,24,v,164,v,124
v_exp_f32 v106, v106
MA a,44,v,168,v,124
v_exp_f32 v107, v107
MA a,64,v,172,v,124
v_exp_f32 v108, v108
MA a,84,v,176,v,124
v_exp_f32 v109, v109
MA a,104,v,180,v,124
v_exp_f32 v110, v110
MA a,124,v,184,v,124
v_exp_f32 v111, v111
MA a,144,v,188,v,124
CV 128,96,97
CV 129,98,99
MA v,196,v,216,v,124
CV 130,100,101
CV 131,102,103
MC v,112,v,200,v,16,156
R4 v,200,238,32768
CV 132,104,105
MA v,112,v,204,v,36
R4 v,204,238,33280
CV 133,106,107
MA v,112,v,208,v,56
R4 v,208,238,33792
CV 134,108,109
MA v,112,v,212,v,76
R4 v,212,238,34304
CV 135,110,111
MC v,116,a,252,v,76,156
R4 a,252,238,38400
MA v,116,a,248,v,56
R4 a,248,238,37888
MA v,116,a,244,v,36
R4 a,244,238,37376
MA v,116,a,240,v,16
R4 a,240,238,36864
MA a,8,v,160,v,128
v_exp_f32 v112, v112
MA a,28,v,164,v,128
v_exp_f32 v113, v113
MA a,48,v,168,v,128
v_exp_f32 v114, v114
MA a,68,v,172,v,128
v_exp_f32 v115, v115
MA a,88,v,176,v,128
v_exp_f32 v116, v116
MA a,108,v,180,v,128
v_exp_f32 v117, v117
MA a,128,v,184,v,128
s_add_u32 m0, s64, 98304
s_mov_b32 s70, 122880
MA a,148,v,188,v,128
v_exp_f32 v118, v118
MA v,220,v,216,v,128
s_waitcnt lgkmcnt(0)
CV 136,112,113
MC v,80,v,200,v,0,140
v_exp_f32 v119, v119
MA v,80,v,204,v,20
buffer_load_dwordx4 v241, s[44:47], s70 offen offset:0 nt lds
MA v,80,v,208,v,40
CV 137,114,115
CV 138,116,117
MA v,80,v,212,v,60
CV 139,118,119
MC v,84,a,252,v,60,140
MA v,84,a,248,v,40
buffer_load_dwordx4 v242, s[44:47], s70 offen offset:1024 nt lds
MA v,84,a,244,v,20
v_exp_f32 v80, v80
MA v,84,a,240,v,0
v_exp_f32 v81, v81
MA a,12,v,160,v,132
v_exp_f32 v82, v82
MA a,32,v,164,v,132
buffer_load_dwordx4 v241, s[44:47], s70 offen offset:2048 nt lds
MA a,52,v,168,v,132
v_exp_f32 v83, v83
MA a,72,v,172,v,132
v_exp_f32 v84, v84
MA a,92,v,176,v,132
v_exp_f32 v85, v85
MA a,112,v,180,v,132
buffer_load_dwordx4 v242, s[44:47], s70 offen offset:3072 nt lds
MA a,132,v,184,v,132
v_exp_f32 v86, v86
MA a,152,v,188,v,132
v_exp_f32 v87, v87
MA v,224,v,216,v,132
CV 120,80,81
CV 121,82,83
MC v,88,v,200,v,4,144
CV 122,84,85
CV 123,86,87
MA v,88,v,204,v,24
s_memrealtime s[98:99]
MA v,88,v,208,v,44
MA v,88,v,212,v,64
MC v,92,a,252,v,64,144
s_waitcnt lgkmcnt(0)
s_sub_i32 s98, s98, s76
MA v,92,a,248,v,44
s_cmp_lt_i32 s98, 0
s_cbranch_scc1 PACE0_14%=
PACED0_14%=:
MA v,92,a,244,v,24
v_exp_f32 v88, v88
MA v,92,a,240,v,4
v_exp_f32 v89, v89
MA a,16,v,160,v,136
s_waitcnt vmcnt(4)
s_barrier
MA a,36,v,164,v,136
TR v,160,239,16384
s_add_u32 s76, s76, s29
MA a,56,v,168,v,136
TR v,162,239,20480
CV 124,88,89
MA a,76,v,172,v,136
TR v,164,240,16384
MA a,96,v,176,v,136
TR v,166,240,20480
MA a,116,v,180,v,136
TR v,168,239,16896
MA a,136,v,184,v,136
TR v,170,239,20992
MA a,156,v,188,v,136
TR v,172,240,16896
MA v,228,v,216,v,136
TR v,174,240,20992
MC v,96,v,200,v,8,148
TR v,176,239,17408
MA v,96,v,204,v,28
TR v,178,239,21504
MA v,96,v,208,v,48
TR v,180,240,17408
MA v,96,v,212,v,68
TR v,182,240,21504
MC v,100,a,252,v,68,148
TR v,184,239,17920
MA v,100,a,248,v,48
TR v,186,239,22016
s_waitcnt lgkmcnt(6)
MA v,100,a,244,v,28
TR v,188,240,17920
MA v,100,a,240,v,8
TR v,190,240,22016
MA a,0,v,160,v,120
v_exp_f32 v90, v90
MA a,20,v,164,v,120
v_exp_f32 v91, v91
MA a,40,v,168,v,120
v_exp_f32 v92, v92
MA a,60,v,172,v,120
s_waitcnt lgkmcnt(0)
CV 125,90,91
MA a,80,v,176,v,120
v_exp_f32 v93, v93
MA a,100,v,180,v,120
v_exp_f32 v94, v94
MA a,120,v,184,v,120
v_exp_f32 v95, v95
MA a,140,v,188,v,120
v_exp_f32 v96, v96
MA v,192,v,216,v,120
s_mov_b32 m0, 161792
s_add_u32 s12, s12, 0xffd00200
s_addc_u32 s13, s13, -1
MC v,104,v,200,v,12,152
s_mov_b32 s14, 1057280
s_mov_b32 s67, 0
MA v,104,v,204,v,32
v_exp_f32 v97, v97
MA v,104,v,208,v,52
v_exp_f32 v98, v98
MA v,104,v,212,v,72
buffer_load_dword v252, s[12:15], s67 offen lds
MC v,108,a,252,v,72,152
s_mov_b32 m0, s65
s_mov_b32 s68, 0
MA v,108,a,248,v,52
CV 126,92,93
CV 127,94,95
MA v,108,a,244,v,32
v_exp_f32 v99, v99
MA v,108,a,240,v,12
buffer_load_dwordx4 v243, s[32:35], s68 offen offset:0 lds
MA a,4,v,160,v,124
v_exp_f32 v100, v100
MA a,24,v,164,v,124
v_exp_f32 v101, v101
MA a,44,v,168,v,124
v_exp_f32 v102, v102
MA a,64,v,172,v,124
buffer_load_dwordx4 v244, s[32:35], s68 offen offset:1024 lds
MA a,84,v,176,v,124
buffer_load_dwordx4 v245, s[32:35], s68 offen offset:2048 lds
MA a,104,v,180,v,124
buffer_load_dwordx4 v246, s[32:35], s68 offen offset:3072 lds
MA a,124,v,184,v,124
s_add_u32 m0, s65, 4096
s_mov_b32 s69, 32768
MA a,144,v,188,v,124
buffer_load_dwordx4 v243, s[32:35], s69 offen offset:0 lds
MA v,196,v,216,v,124
v_exp_f32 v103, v103
MC v,112,v,200,v,16,156
R4 v,200,238,49152
CV 128,96,97
MA v,112,v,204,v,36
R4 v,204,238,49664
CV 129,98,99
MA v,112,v,208,v,56
R4 v,208,238,50176
CV 130,100,101
MA v,112,v,212,v,76
R4 v,212,238,50688
CV 131,102,103
MC v,116,a,252,v,76,156
R4 a,252,238,54784
MA v,116,a,248,v,56
R4 a,248,238,54272
MA v,116,a,244,v,36
R4 a,244,238,53760
MA v,116,a,240,v,16
R4 a,240,238,53248
MA a,8,v,160,v,128
v_exp_f32 v104, v104
MA a,28,v,164,v,128
v_exp_f32 v105, v105
MA a,48,v,168,v,128
v_exp_f32 v106, v106
MA a,68,v,172,v,128
v_exp_f32 v107, v107
MA a,88,v,176,v,128
v_exp_f32 v108, v108
MA a,108,v,180,v,128
v_exp_f32 v109, v109
MA a,128,v,184,v,128
s_add_u32 m0, s64, 114688
s_mov_b32 s70, 512
MA a,148,v,188,v,128
v_exp_f32 v110, v110
MA v,220,v,216,v,128
s_waitcnt lgkmcnt(0)
CV 132,104,105
MC v,80,v,200,v,0,140
v_exp_f32 v111, v111
MA v,80,v,204,v,20
buffer_load_dwordx4 v247, s[36:39], s70 offen offset:0 lds
MA v,80,v,208,v,40
v_exp_f32 v112, v112
MA v,80,v,212,v,60
v_exp_f32 v113, v113
MC v,84,a,252,v,60,140
CV 133,106,107
CV 134,108,109
MA v,84,a,248,v,40
buffer_load_dwordx4 v248, s[36:39], s70 offen offset:1024 lds
CV 135,110,111
MA v,84,a,244,v,20
v_exp_f32 v114, v114
MA v,84,a,240,v,0
v_exp_f32 v115, v115
MA a,12,v,160,v,132
v_exp_f32 v116, v116
MA a,32,v,164,v,132
buffer_load_dwordx4 v249, s[36:39], s70 offen offset:2048 lds
MA a,52,v,168,v,132
v_exp_f32 v117, v117
MA a,72,v,172,v,132
v_exp_f32 v118, v118
MA a,92,v,176,v,132
v_exp_f32 v119, v119
MA a,112,v,180,v,132
buffer_load_dwordx4 v250, s[36:39], s70 offen offset:3072 lds
MA a,132,v,184,v,132
s_add_u32 m0, s64, 65536
)ASM"
R"ASM(s_mov_b32 s67, 640
MA a,152,v,188,v,132
v_exp_f32 v80, v80
MA v,224,v,216,v,132
v_exp_f32 v81, v81
MC v,88,v,200,v,4,144
buffer_load_dwordx4 v247, s[36:39], s67 offen offset:0 lds
MA v,88,v,204,v,24
v_exp_f32 v82, v82
MA v,88,v,208,v,44
v_exp_f32 v83, v83
MA v,88,v,212,v,64
v_exp_f32 v84, v84
MC v,92,a,252,v,64,144
buffer_load_dwordx4 v248, s[36:39], s67 offen offset:1024 lds
MA v,92,a,248,v,44
buffer_load_dwordx4 v249, s[36:39], s67 offen offset:2048 lds
CV 136,112,113
CV 137,114,115
CV 138,116,117
CV 139,118,119
MA v,92,a,244,v,24
buffer_load_dwordx4 v250, s[36:39], s67 offen offset:3072 lds
MA v,92,a,240,v,4
v_exp_f32 v85, v85
MA a,16,v,160,v,136
s_waitcnt vmcnt(14)
s_barrier
MA a,36,v,164,v,136
TR v,160,239,32768
CV 120,80,81
MA a,56,v,168,v,136
TR v,162,239,36864
CV 121,82,83
MA a,76,v,172,v,136
TR v,164,240,32768
CV 122,84,85
MA a,96,v,176,v,136
TR v,166,240,36864
MA a,116,v,180,v,136
TR v,168,239,33280
MA a,136,v,184,v,136
TR v,170,239,37376
MA a,156,v,188,v,136
TR v,172,240,33280
MA v,228,v,216,v,136
TR v,174,240,37376
MC v,96,v,200,v,8,148
TR v,176,239,33792
v_exp_f32 v86, v86
MA v,96,v,204,v,28
TR v,178,239,37888
v_exp_f32 v87, v87
MA v,96,v,208,v,48
TR v,180,240,33792
CV 123,86,87
MA v,96,v,212,v,68
TR v,182,240,37888
MC v,100,a,252,v,68,148
TR v,184,239,34304
MA v,100,a,248,v,48
TR v,186,239,38400
s_waitcnt lgkmcnt(6)
MA v,100,a,244,v,28
TR v,188,240,34304
MA v,100,a,240,v,8
TR v,190,240,38400
MA a,0,v,160,v,120
v_exp_f32 v88, v88
MA a,20,v,164,v,120
v_exp_f32 v89, v89
MA a,40,v,168,v,120
v_exp_f32 v90, v90
MA a,60,v,172,v,120
s_waitcnt lgkmcnt(0)
CV 124,88,89
MA a,80,v,176,v,120
v_exp_f32 v91, v91
MA a,100,v,180,v,120
v_exp_f32 v92, v92
MA a,120,v,184,v,120
v_exp_f32 v93, v93
MA a,140,v,188,v,120
v_exp_f32 v94, v94
MA v,192,v,216,v,120
s_add_u32 m0, s64, 81920
s_mov_b32 s68, 524800
MC v,104,v,200,v,12,152
v_exp_f32 v95, v95
MA v,104,v,204,v,32
v_exp_f32 v96, v96
MA v,104,v,208,v,52
v_exp_f32 v97, v97
MA v,104,v,212,v,72
buffer_load_dwordx4 v247, s[36:39], s68 offen offset:0 lds
MC v,108,a,252,v,72,152
CV 125,90,91
CV 126,92,93
MA v,108,a,248,v,52
CV 127,94,95
CV 128,96,97
MA v,108,a,244,v,32
v_exp_f32 v98, v98
MA v,108,a,240,v,12
buffer_load_dwordx4 v248, s[36:39], s68 offen offset:1024 lds
MA a,4,v,160,v,124
v_exp_f32 v99, v99
MA a,24,v,164,v,124
v_exp_f32 v100, v100
MA a,44,v,168,v,124
v_exp_f32 v101, v101
MA a,64,v,172,v,124
buffer_load_dwordx4 v249, s[36:39], s68 offen offset:2048 lds
MA a,84,v,176,v,124
v_exp_f32 v102, v102
MA a,104,v,180,v,124
v_exp_f32 v103, v103
MA a,124,v,184,v,124
v_exp_f32 v104, v104
MA a,144,v,188,v,124
buffer_load_dwordx4 v250, s[36:39], s68 offen offset:3072 lds
MA v,196,v,216,v,124
s_mov_b32 m0, s66
s_mov_b32 s69, 128
MC v,112,v,200,v,16,156
v_exp_f32 v105, v105
MA v,112,v,204,v,36
v_exp_f32 v106, v106
MA v,112,v,208,v,56
buffer_load_dwordx4 v243, s[32:35], s69 offen offset:0 lds
MA v,112,v,212,v,76
v_exp_f32 v107, v107
MC v,116,a,252,v,76,156
CV 129,98,99
CV 130,100,101
MA v,116,a,248,v,56
CV 131,102,103
CV 132,104,105
MA v,116,a,244,v,36
buffer_load_dwordx4 v244, s[32:35], s69 offen offset:1024 lds
MA v,116,a,240,v,16
v_exp_f32 v108, v108
MA a,8,v,160,v,128
v_exp_f32 v109, v109
MA a,28,v,164,v,128
v_exp_f32 v110, v110
MA a,48,v,168,v,128
buffer_load_dwordx4 v245, s[32:35], s69 offen offset:2048 lds
v_exp_f32 v111, v111
MA a,68,v,172,v,128
v_rcp_f32 v232, v192
MA a,88,v,176,v,128
v_exp_f32 v112, v112
MA a,108,v,180,v,128
v_exp_f32 v113, v113
v_rcp_f32 v233, v196
MA a,128,v,184,v,128
buffer_load_dwordx4 v246, s[32:35], s69 offen offset:3072 lds
CV 133,106,107
CV 134,108,109
CV 135,110,111
v_exp_f32 v114, v114
MA a,148,v,188,v,128
s_add_u32 m0, s66, 4096
s_mov_b32 s70, 32896
v_exp_f32 v115, v115
MA v,220,v,216,v,128
v_exp_f32 v116, v116
MA a,12,v,160,v,132
v_exp_f32 v117, v117
MA a,32,v,164,v,132
buffer_load_dwordx4 v243, s[32:35], s70 offen offset:0 lds
v_exp_f32 v118, v118
MA a,52,v,168,v,132
s_waitcnt vmcnt(13)
s_barrier
v_exp_f32 v119, v119
v_rcp_f32 v234, v220
MA a,72,v,172,v,132
TR v,208,253,49152
CV 136,112,113
MA a,92,v,176,v,132
TR v,210,253,49408
CV 137,114,115
MA a,112,v,180,v,132
TR v,212,253,51200
CV 138,116,117
MA a,132,v,184,v,132
TR v,214,253,51456
CV 139,118,119
MA a,152,v,188,v,132
R4 a,240,251,0
v_add_f32 v80, v192, v196
MA v,224,v,216,v,132
R4 a,244,251,512
MA v,228,v,216,v,136
R4 a,248,251,2048
MA a,16,v,160,v,136
R4 a,252,251,2560
MA a,36,v,164,v,136
R4 v,160,251,4096
v_add_f32 v81, v220, v224
MA a,56,v,168,v,136
R4 v,164,251,4608
v_add_f32 v80, v80, v81
MA a,76,v,172,v,136
R4 v,168,251,6144
v_add_f32 v80, v80, v228
MA a,96,v,176,v,136
R4 v,172,251,6656
v_cmp_nge_f32 vcc, s83, v80
MA a,116,v,180,v,136
R4 v,176,251,8192
MA a,136,v,184,v,136
R4 v,180,251,8704
v_rcp_f32 v235, v224
v_rcp_f32 v236, v228
MA a,156,v,188,v,136
s_cbranch_vccnz SLOWCALL0%=
SLOWRET0%=:
TR v,216,254,49152
s_waitcnt lgkmcnt(4)
TR v,218,254,49408
MZ v,0,v,208,a,240
TR v,200,253,49664
s_add_u32 m0, s64, 98304
MA v,0,v,212,a,244
TR v,202,253,49920
s_mov_b32 s67, 524928
MZ v,4,v,212,a,252
TR v,220,254,51200
v_add_u32 v251, s71, v251
MA v,4,v,208,a,248
TR v,204,253,51712
AR 224,0
MZ v,8,v,208,v,160
TR v,206,253,51968
AR 225,1
MA v,8,v,212,v,164
TR v,222,254,51456
s_waitcnt lgkmcnt(4)
MZ v,12,v,212,v,172
buffer_load_dwordx4 v247, s[36:39], s67 offen offset:0 lds
MA v,12,v,208,v,168
AR 226,2
AR 227,3
MZ v,16,v,208,v,176
AR 228,20
AR 229,21
MA v,16,v,212,v,180
AR 230,22
AR 231,23
MZ v,80,v,216,a,240
TR v,208,254,49664
s_waitcnt lgkmcnt(1)
MA v,80,v,220,a,244
TR v,210,254,49920
v_mul_f32 v224, v232, v224
MZ v,88,v,220,a,252
buffer_load_dwordx4 v248, s[36:39], s67 offen offset:1024 lds
MA v,88,v,216,a,248
TR v,212,254,51712
v_mul_f32 v225, v232, v225
MZ v,96,v,216,v,160
TR v,214,254,51968
v_mul_f32 v226, v232, v226
MA v,96,v,220,v,164
v_mul_f32 v227, v232, v227
v_mul_f32 v228, v232, v228
MZ v,104,v,220,v,172
buffer_load_dwordx4 v249, s[36:39], s67 offen offset:2048 lds
MA v,104,v,216,v,168
buffer_load_dwordx4 v250, s[36:39], s67 offen offset:3072 lds
MZ v,112,v,216,v,176
s_add_u32 m0, s64, 114688
s_mov_b32 s68, 1049088
MA v,112,v,220,v,180
s_waitcnt vmcnt(13) lgkmcnt(0)
s_barrier
MZ v,20,v,200,a,240
TR v,216,253,0
v_mul_f32 v229, v232, v229
MA v,20,v,204,a,244
TR v,218,253,256
v_mul_f32 v230, v232, v230
MZ v,24,v,204,a,252
buffer_load_dwordx4 v247, s[36:39], s68 offen offset:0 lds
MA v,24,v,200,a,248
TR v,220,253,2048
v_mul_f32 v231, v232, v231
MZ v,28,v,200,v,160
TR v,222,253,2304
CV 224,224,225
MA v,28,v,204,v,164
CV 225,226,227
CV 226,228,229
MZ v,32,v,204,v,172
buffer_load_dwordx4 v248, s[36:39], s68 offen offset:1024 lds
MA v,32,v,200,v,168
CV 227,230,231
AW 160,224
MZ v,36,v,200,v,176
AW 161,225
AW 162,226
MA v,36,v,204,v,180
AW 163,227
AR 224,4
MZ v,84,v,208,a,240
TR v,200,254,0
AR 225,5
MA v,84,v,212,a,244
TR v,202,254,256
AR 226,6
MZ v,92,v,212,a,252
buffer_load_dwordx4 v249, s[36:39], s68 offen offset:2048 lds
MA v,92,v,208,a,248
TR v,204,254,2048
AR 227,7
MZ v,100,v,208,v,160
TR v,206,254,2304
AR 228,24
MA v,100,v,212,v,164
AR 229,25
AR 230,26
MZ v,108,v,212,v,172
buffer_load_dwordx4 v250, s[36:39], s68 offen offset:3072 lds
MA v,108,v,208,v,168
s_mov_b32 m0, s65
s_mov_b32 s69, 256
MZ v,116,v,208,v,176
AR 231,27
v_mul_f32 v224, v233, v224
MA v,116,v,212,v,180
s_waitcnt lgkmcnt(0)
v_mul_f32 v225, v233, v225
MZ v,40,v,216,a,240
TR v,208,253,512
v_mul_f32 v226, v233, v226
MA v,40,v,220,a,244
TR v,210,253,768
v_mul_f32 v227, v233, v227
MZ v,44,v,220,a,252
buffer_load_dwordx4 v243, s[32:35], s69 offen offset:0 lds
MA v,44,v,216,a,248
TR v,212,253,2560
v_mul_f32 v228, v233, v228
MZ v,48,v,216,v,160
TR v,214,253,2816
v_mul_f32 v229, v233, v229
MA v,48,v,220,v,164
v_mul_f32 v230, v233, v230
v_mul_f32 v231, v233, v231
MZ v,52,v,220,v,172
buffer_load_dwordx4 v244, s[32:35], s69 offen offset:1024 lds
MA v,52,v,216,v,168
CV 224,224,225
CV 225,226,227
MZ v,56,v,216,v,176
CV 226,228,229
CV 227,230,231
MA v,56,v,220,v,180
AW 164,224
AW 165,225
MZ v,120,v,200,a,240
TR v,216,254,512
AW 166,226
MA v,120,v,204,a,244
TR v,218,254,768
AW 167,227
MZ v,124,v,204,a,252
buffer_load_dwordx4 v245, s[32:35], s69 offen offset:2048 lds
MA v,124,v,200,a,248
TR v,220,254,2560
AR 224,8
MZ v,128,v,200,v,160
TR v,222,254,2816
AR 225,9
MA v,128,v,204,v,164
AR 226,10
AR 227,11
MZ v,132,v,204,v,172
buffer_load_dwordx4 v246, s[32:35], s69 offen offset:3072 lds
MA v,132,v,200,v,168
s_add_u32 m0, s65, 4096
s_mov_b32 s70, 33024
MZ v,136,v,200,v,176
buffer_load_dwordx4 v243, s[32:35], s70 offen offset:0 lds
MA v,136,v,204,v,180
s_waitcnt vmcnt(13) lgkmcnt(0)
s_barrier
MZ v,60,v,208,a,240
TR v,200,253,16384
s_add_u32 m0, s64, 0
MA v,60,v,212,a,244
TR v,202,253,16640
s_mov_b32 s68, 262144
MZ v,64,v,212,a,252
buffer_load_dwordx4 v241, s[40:43], s68 offen offset:0 nt lds
MA v,64,v,208,a,248
TR v,204,253,18432
AR 228,28
MZ v,68,v,208,v,160
TR v,206,253,18688
AR 229,29
MA v,68,v,212,v,164
R4 v,184,251,0
AR 230,30
MZ v,72,v,212,v,172
R4 v,188,251,512
AR 231,31
MA v,72,v,208,v,168
buffer_load_dwordx4 v242, s[40:43], s68 offen offset:1024 nt lds
MZ v,76,v,208,v,176
R4 v,192,251,2048
v_mul_f32 v224, v234, v224
MA v,76,v,212,v,180
R4 v,196,251,2560
v_mul_f32 v225, v234, v225
MZ v,140,v,216,a,240
TR v,208,254,16384
v_mul_f32 v226, v234, v226
MA v,140,v,220,a,244
TR v,210,254,16640
v_mul_f32 v227, v234, v227
MZ v,144,v,220,a,252
buffer_load_dwordx4 v241, s[40:43], s68 offen offset:2048 nt lds
MA v,144,v,216,a,248
TR v,212,254,18432
v_mul_f32 v228, v234, v228
MZ v,148,v,216,v,160
TR v,214,254,18688
v_mul_f32 v229, v234, v229
MA v,148,v,220,v,164
R4 a,240,251,4096
v_mul_f32 v230, v234, v230
MZ v,152,v,220,v,172
R4 a,244,251,4608
v_mul_f32 v231, v234, v231
MA v,152,v,216,v,168
buffer_load_dwordx4 v242, s[40:43], s68 offen offset:3072 nt lds
MZ v,156,v,216,v,176
R4 a,248,251,6144
s_mov_b32 m0, 161792
MA v,156,v,220,v,180
s_waitcnt lgkmcnt(2)
R4 a,252,251,6656
MA v,0,v,200,v,184
R4 v,160,251,8192
s_add_u32 s12, s12, 0x180000
s_addc_u32 s13, s13, 0
MA v,0,v,204,v,188
R4 v,164,251,8704
s_mov_b32 s14, 532992
MA v,4,v,204,v,196
s_mov_b32 s67, 0
v_subrev_u32 v251, s71, v251
MA v,4,v,200,v,192
TR v,216,253,16896
CV 224,224,225
MA v,8,v,200,a,240
TR v,218,253,17152
s_waitcnt lgkmcnt(3)
MA v,8,v,204,a,244
TR v,220,253,18944
CV 225,226,227
MA v,12,v,204,a,252
TR v,222,253,19200
CV 226,228,229
MA v,12,v,200,a,248
buffer_load_dword v252, s[12:15], s67 offen lds
MA v,16,v,200,v,160
s_waitcnt lgkmcnt(2)
s_add_u32 m0, s64, 65536
MA v,16,v,204,v,164
s_mov_b32 s69, 1049216
CV 227,230,231
MA v,80,v,208,v,184
TR v,200,254,16896
AW 168,224
MA v,80,v,212,v,188
TR v,202,254,17152
AW 169,225
MA v,88,v,212,v,196
buffer_load_dwordx4 v247, s[36:39], s69 offen offset:0 lds
MA v,88,v,208,v,192
TR v,204,254,18944
AW 170,226
MA v,96,v,208,a,240
TR v,206,254,19200
AW 171,227
MA v,96,v,212,a,244
AR 224,12
AR 225,13
MA v,104,v,212,a,252
buffer_load_dwordx4 v248, s[36:39], s69 offen offset:1024 lds
MA v,104,v,208,a,248
buffer_load_dwordx4 v249, s[36:39], s69 offen offset:2048 lds
MA v,112,v,208,v,160
buffer_load_dwordx4 v250, s[36:39], s69 offen offset:3072 lds
MA v,112,v,212,v,164
s_waitcnt vmcnt(18) lgkmcnt(0)
s_barrier
MA v,20,v,216,v,184
TR v,208,253,32768
s_add_u32 m0, s64, 81920
MA v,20,v,220,v,188
TR v,210,253,33024
s_mov_b32 s70, 1573376
MA v,24,v,220,v,196
buffer_load_dwordx4 v247, s[36:39], s70 offen offset:0 lds
MA v,24,v,216,v,192
TR v,212,253,34816
AR 226,14
MA v,28,v,216,a,240
TR v,214,253,35072
AR 227,15
MA v,28,v,220,a,244
AR 228,32
AR 229,33
MA v,32,v,220,a,252
buffer_load_dwordx4 v248, s[36:39], s70 offen offset:1024 lds
MA v,32,v,216,a,248
AR 230,34
AR 231,35
MA v,36,v,216,v,160
v_mul_f32 v224, v235, v224
v_mul_f32 v225, v235, v225
MA v,36,v,220,v,164
v_mul_f32 v226, v235, v226
v_mul_f32 v227, v235, v227
MA v,84,v,200,v,184
TR v,216,254,32768
v_mul_f32 v228, v235, v228
MA v,84,v,204,v,188
TR v,218,254,33024
v_mul_f32 v229, v235, v229
MA v,92,v,204,v,196
buffer_load_dwordx4 v249, s[36:39], s70 offen offset:2048 lds
MA v,92,v,200,v,192
TR v,220,254,34816
v_mul_f32 v230, v235, v230
MA v,100,v,200,a,240
TR v,222,254,35072
)ASM"
R"ASM(v_mul_f32 v231, v235, v231
MA v,100,v,204,a,244
CV 224,224,225
CV 225,226,227
MA v,108,v,204,a,252
buffer_load_dwordx4 v250, s[36:39], s70 offen offset:3072 lds
MA v,108,v,200,a,248
s_mov_b32 m0, s66
s_mov_b32 s67, 384
MA v,116,v,200,v,160
CV 226,228,229
CV 227,230,231
MA v,116,v,204,v,164
s_waitcnt lgkmcnt(0)
AW 172,224
MA v,40,v,208,v,184
TR v,200,253,33280
AW 173,225
MA v,40,v,212,v,188
TR v,202,253,33536
AW 174,226
MA v,44,v,212,v,196
buffer_load_dwordx4 v243, s[32:35], s67 offen offset:0 lds
MA v,44,v,208,v,192
TR v,204,253,35328
AW 175,227
MA v,48,v,208,a,240
TR v,206,253,35584
AR 224,16
MA v,48,v,212,a,244
AR 225,17
AR 226,18
MA v,52,v,212,a,252
buffer_load_dwordx4 v244, s[32:35], s67 offen offset:1024 lds
MA v,52,v,208,a,248
AR 227,19
AR 228,36
MA v,56,v,208,v,160
AR 229,37
AR 230,38
MA v,56,v,212,v,164
AR 231,39
v_mul_f32 v224, v236, v224
MA v,120,v,216,v,184
TR v,208,254,33280
v_mul_f32 v225, v236, v225
MA v,120,v,220,v,188
TR v,210,254,33536
v_mul_f32 v226, v236, v226
MA v,124,v,220,v,196
buffer_load_dwordx4 v245, s[32:35], s67 offen offset:2048 lds
MA v,124,v,216,v,192
TR v,212,254,35328
v_mul_f32 v227, v236, v227
MA v,128,v,216,a,240
TR v,214,254,35584
v_mul_f32 v228, v236, v228
MA v,128,v,220,a,244
v_mul_f32 v229, v236, v229
v_mul_f32 v230, v236, v230
MA v,132,v,220,a,252
buffer_load_dwordx4 v246, s[32:35], s67 offen offset:3072 lds
MA v,132,v,216,a,248
s_add_u32 m0, s66, 4096
s_mov_b32 s68, 33152
MA v,136,v,216,v,160
v_mul_f32 v231, v236, v231
CV 224,224,225
MA v,136,v,220,v,164
s_waitcnt vmcnt(17) lgkmcnt(0)
s_barrier
MA v,60,v,200,v,184
TR v,216,253,49152
CV 225,226,227
MA v,60,v,204,v,188
TR v,218,253,49408
CV 226,228,229
MA v,64,v,204,v,196
buffer_load_dwordx4 v243, s[32:35], s68 offen offset:0 lds
MA v,64,v,200,v,192
TR v,220,253,51200
s_add_u32 m0, s64, 98304
MA v,68,v,200,a,240
TR v,222,253,51456
s_mov_b32 s69, 1573504
MA v,68,v,204,a,244
R4 v,168,251,0
CV 227,230,231
MA v,72,v,204,a,252
R4 v,172,251,512
AW 176,224
MA v,72,v,200,a,248
buffer_load_dwordx4 v247, s[36:39], s69 offen offset:0 lds
MA v,76,v,200,v,160
R4 v,176,251,2048
AW 177,225
MA v,76,v,204,v,164
R4 v,180,251,2560
AW 178,226
MA v,140,v,208,v,184
TR v,200,254,49152
AW 179,227
MA v,140,v,212,v,188
TR v,202,254,49408
AR 224,40
MA v,144,v,212,v,196
buffer_load_dwordx4 v248, s[36:39], s69 offen offset:1024 lds
MA v,144,v,208,v,192
TR v,204,254,51200
AR 225,41
MA v,148,v,208,a,240
TR v,206,254,51456
AR 226,42
MA v,148,v,212,a,244
R4 v,184,251,4096
AR 227,43
MA v,152,v,212,a,252
R4 v,188,251,4608
AR 228,60
MA v,152,v,208,a,248
buffer_load_dwordx4 v249, s[36:39], s69 offen offset:2048 lds
MA v,156,v,208,v,160
R4 v,192,251,6144
AR 229,61
MA v,156,v,212,v,164
s_waitcnt lgkmcnt(2)
R4 v,196,251,6656
MA v,0,v,216,v,168
R4 a,240,251,8192
AR 230,62
MA v,0,v,220,v,172
R4 a,244,251,8704
AR 231,63
MA v,4,v,220,v,180
buffer_load_dwordx4 v250, s[36:39], s69 offen offset:3072 lds
MA v,4,v,216,v,176
TR v,208,253,49664
s_add_u32 m0, s64, 16384
MA v,8,v,216,v,184
TR v,210,253,49920
s_waitcnt lgkmcnt(3)
MA v,8,v,220,v,188
TR v,212,253,51712
s_mov_b32 s67, 270336
MA v,12,v,220,v,196
TR v,214,253,51968
v_add_u32 v251, s71, v251
MA v,12,v,216,v,192
buffer_load_dwordx4 v241, s[40:43], s67 offen offset:0 nt lds
MA v,16,v,216,a,240
s_waitcnt lgkmcnt(2)
v_mul_f32 v224, v232, v224
MA v,16,v,220,a,244
v_mul_f32 v225, v232, v225
v_mul_f32 v226, v232, v226
MA v,80,v,200,v,168
TR v,216,254,49664
v_mul_f32 v227, v232, v227
MA v,80,v,204,v,172
TR v,218,254,49920
v_mul_f32 v228, v232, v228
MA v,88,v,204,v,180
buffer_load_dwordx4 v242, s[40:43], s67 offen offset:1024 nt lds
MA v,88,v,200,v,176
TR v,220,254,51712
v_mul_f32 v229, v232, v229
MA v,96,v,200,v,184
TR v,222,254,51968
v_mul_f32 v230, v232, v230
MA v,96,v,204,v,188
v_mul_f32 v231, v232, v231
CV 224,224,225
MA v,104,v,204,v,196
buffer_load_dwordx4 v241, s[40:43], s67 offen offset:2048 nt lds
MA v,104,v,200,v,192
CV 225,226,227
CV 226,228,229
MA v,112,v,200,a,240
CV 227,230,231
AW 180,224
MA v,112,v,204,a,244
s_waitcnt vmcnt(16) lgkmcnt(0)
s_barrier
MA v,20,v,208,v,168
TR v,200,253,0
AW 181,225
MA v,20,v,212,v,172
TR v,202,253,256
AW 182,226
MA v,24,v,212,v,180
buffer_load_dwordx4 v242, s[40:43], s67 offen offset:3072 nt lds
MA v,24,v,208,v,176
TR v,204,253,2048
s_mov_b32 m0, 161792
MA v,28,v,208,v,184
TR v,206,253,2304
s_add_u32 s12, s12, 0x100000
s_addc_u32 s13, s13, 0
MA v,28,v,212,v,188
s_mov_b32 s14, 532992
s_mov_b32 s70, 0
MA v,32,v,212,v,196
buffer_load_dword v252, s[12:15], s70 offen lds
MA v,32,v,208,v,192
s_add_u32 m0, s64, 114688
s_mov_b32 s68, 2097664
MA v,36,v,208,a,240
AW 183,227
AR 224,44
MA v,36,v,212,a,244
AR 225,45
AR 226,46
MA v,84,v,216,v,168
TR v,208,254,0
AR 227,47
MA v,84,v,220,v,172
TR v,210,254,256
AR 228,64
MA v,92,v,220,v,180
buffer_load_dwordx4 v247, s[36:39], s68 offen offset:0 lds
MA v,92,v,216,v,176
TR v,212,254,2048
AR 229,65
MA v,100,v,216,v,184
TR v,214,254,2304
AR 230,66
MA v,100,v,220,v,188
AR 231,67
v_mul_f32 v224, v233, v224
MA v,108,v,220,v,196
buffer_load_dwordx4 v248, s[36:39], s68 offen offset:1024 lds
MA v,108,v,216,v,192
v_mul_f32 v225, v233, v225
v_mul_f32 v226, v233, v226
MA v,116,v,216,a,240
v_mul_f32 v227, v233, v227
v_mul_f32 v228, v233, v228
MA v,116,v,220,a,244
s_waitcnt lgkmcnt(0)
v_mul_f32 v229, v233, v229
MA v,40,v,200,v,168
TR v,216,253,512
v_mul_f32 v230, v233, v230
MA v,40,v,204,v,172
TR v,218,253,768
v_mul_f32 v231, v233, v231
MA v,44,v,204,v,180
buffer_load_dwordx4 v249, s[36:39], s68 offen offset:2048 lds
MA v,44,v,200,v,176
TR v,220,253,2560
CV 224,224,225
MA v,48,v,200,v,184
TR v,222,253,2816
CV 225,226,227
MA v,48,v,204,v,188
CV 226,228,229
CV 227,230,231
MA v,52,v,204,v,196
buffer_load_dwordx4 v250, s[36:39], s68 offen offset:3072 lds
MA v,52,v,200,v,192
s_mov_b32 m0, s65
s_mov_b32 s69, 512
MA v,56,v,200,a,240
AW 184,224
AW 185,225
MA v,56,v,204,a,244
AW 186,226
AW 187,227
MA v,120,v,208,v,168
TR v,200,254,512
AR 224,48
MA v,120,v,212,v,172
TR v,202,254,768
AR 225,49
MA v,124,v,212,v,180
buffer_load_dwordx4 v243, s[32:35], s69 offen offset:0 lds
MA v,124,v,208,v,176
TR v,204,254,2560
AR 226,50
MA v,128,v,208,v,184
TR v,206,254,2816
AR 227,51
MA v,128,v,212,v,188
AR 228,68
AR 229,69
MA v,132,v,212,v,196
buffer_load_dwordx4 v244, s[32:35], s69 offen offset:1024 lds
MA v,132,v,208,v,192
AR 230,70
AR 231,71
MA v,136,v,208,a,240
v_mul_f32 v224, v234, v224
v_mul_f32 v225, v234, v225
MA v,136,v,212,a,244
s_waitcnt vmcnt(15) lgkmcnt(0)
s_barrier
MA v,60,v,216,v,168
TR v,208,253,16384
v_mul_f32 v226, v234, v226
MA v,60,v,220,v,172
TR v,210,253,16640
v_mul_f32 v227, v234, v227
MA v,64,v,220,v,180
buffer_load_dwordx4 v245, s[32:35], s69 offen offset:2048 lds
MA v,64,v,216,v,176
TR v,212,253,18432
v_mul_f32 v228, v234, v228
MA v,68,v,216,v,184
TR v,214,253,18688
v_mul_f32 v229, v234, v229
MA v,68,v,220,v,188
R4 a,248,251,0
v_mul_f32 v230, v234, v230
MA v,72,v,220,v,196
R4 a,252,251,512
v_mul_f32 v231, v234, v231
MA v,72,v,216,v,192
buffer_load_dwordx4 v246, s[32:35], s69 offen offset:3072 lds
MA v,76,v,216,a,240
R4 v,160,251,2048
s_add_u32 m0, s65, 4096
MA v,76,v,220,a,244
R4 v,164,251,2560
s_mov_b32 s70, 33280
MA v,140,v,200,v,168
TR v,216,254,16384
CV 224,224,225
MA v,140,v,204,v,172
TR v,218,254,16640
CV 225,226,227
MA v,144,v,204,v,180
buffer_load_dwordx4 v243, s[32:35], s70 offen offset:0 lds
MA v,144,v,200,v,176
TR v,220,254,18432
s_add_u32 m0, s64, 65536
MA v,148,v,200,v,184
TR v,222,254,18688
s_mov_b32 s67, 2097792
MA v,148,v,204,v,188
R4 v,168,251,4096
CV 226,228,229
MA v,152,v,204,v,196
R4 v,172,251,4608
CV 227,230,231
MA v,152,v,200,v,192
buffer_load_dwordx4 v247, s[36:39], s67 offen offset:0 lds
MA v,156,v,200,a,240
R4 v,176,251,6144
AW 188,224
MA v,156,v,204,a,244
s_waitcnt lgkmcnt(2)
R4 v,180,251,6656
MA v,0,v,208,a,248
R4 v,184,251,8192
AW 189,225
MA v,0,v,212,a,252
R4 v,188,251,8704
AW 190,226
MA v,4,v,212,v,164
buffer_load_dwordx4 v248, s[36:39], s67 offen offset:1024 lds
MA v,4,v,208,v,160
TR v,200,253,16896
v_subrev_u32 v251, s71, v251
MA v,8,v,208,v,168
TR v,202,253,17152
s_waitcnt lgkmcnt(3)
MA v,8,v,212,v,172
TR v,204,253,18944
AW 191,227
MA v,12,v,212,v,180
TR v,206,253,19200
AR 224,52
MA v,12,v,208,v,176
buffer_load_dwordx4 v249, s[36:39], s67 offen offset:2048 lds
MA v,16,v,208,v,184
s_waitcnt lgkmcnt(2)
AR 225,53
MA v,16,v,212,v,188
AR 226,54
AR 227,55
MA v,80,v,216,a,248
TR v,208,254,16896
AR 228,72
MA v,80,v,220,a,252
TR v,210,254,17152
AR 229,73
MA v,88,v,220,v,164
buffer_load_dwordx4 v250, s[36:39], s67 offen offset:3072 lds
MA v,88,v,216,v,160
TR v,212,254,18944
AR 230,74
MA v,96,v,216,v,168
TR v,214,254,19200
AR 231,75
MA v,96,v,220,v,172
v_mul_f32 v224, v235, v224
v_mul_f32 v225, v235, v225
MA v,104,v,220,v,180
s_add_u32 m0, s64, 81920
s_mov_b32 s68, 2621952
MA v,104,v,216,v,176
v_mul_f32 v226, v235, v226
v_mul_f32 v227, v235, v227
MA v,112,v,216,v,184
v_mul_f32 v228, v235, v228
v_mul_f32 v229, v235, v229
MA v,112,v,220,v,188
s_waitcnt vmcnt(18) lgkmcnt(0)
s_barrier
MA v,20,v,200,a,248
TR v,216,253,32768
v_mul_f32 v230, v235, v230
MA v,20,v,204,a,252
TR v,218,253,33024
v_mul_f32 v231, v235, v231
MA v,24,v,204,v,164
buffer_load_dwordx4 v247, s[36:39], s68 offen offset:0 lds
MA v,24,v,200,v,160
TR v,220,253,34816
CV 224,224,225
MA v,28,v,200,v,168
TR v,222,253,35072
CV 225,226,227
MA v,28,v,204,v,172
CV 226,228,229
CV 227,230,231
MA v,32,v,204,v,180
buffer_load_dwordx4 v248, s[36:39], s68 offen offset:1024 lds
MA v,32,v,200,v,176
AW 192,224
AW 193,225
MA v,36,v,200,v,184
AW 194,226
AW 195,227
MA v,36,v,204,v,188
AR 224,56
AR 225,57
MA v,84,v,208,a,248
TR v,200,254,32768
AR 226,58
MA v,84,v,212,a,252
TR v,202,254,33024
AR 227,59
MA v,92,v,212,v,164
buffer_load_dwordx4 v249, s[36:39], s68 offen offset:2048 lds
MA v,92,v,208,v,160
TR v,204,254,34816
AR 228,76
MA v,100,v,208,v,168
TR v,206,254,35072
AR 229,77
MA v,100,v,212,v,172
AR 230,78
AR 231,79
MA v,108,v,212,v,180
buffer_load_dwordx4 v250, s[36:39], s68 offen offset:3072 lds
MA v,108,v,208,v,176
s_mov_b32 m0, s66
s_mov_b32 s69, 640
MA v,116,v,208,v,184
v_mul_f32 v224, v236, v224
v_mul_f32 v225, v236, v225
MA v,116,v,212,v,188
s_waitcnt lgkmcnt(0)
v_mul_f32 v226, v236, v226
MA v,40,v,216,a,248
TR v,208,253,33280
v_mul_f32 v227, v236, v227
MA v,40,v,220,a,252
TR v,210,253,33536
v_mul_f32 v228, v236, v228
MA v,44,v,220,v,164
buffer_load_dwordx4 v243, s[32:35], s69 offen offset:0 lds
MA v,44,v,216,v,160
TR v,212,253,35328
v_mul_f32 v229, v236, v229
MA v,48,v,216,v,168
TR v,214,253,35584
v_mul_f32 v230, v236, v230
MA v,48,v,220,v,172
v_mul_f32 v231, v236, v231
CV 224,224,225
MA v,52,v,220,v,180
buffer_load_dwordx4 v244, s[32:35], s69 offen offset:1024 lds
MA v,52,v,216,v,176
CV 225,226,227
CV 226,228,229
MA v,56,v,216,v,184
CV 227,230,231
AW 196,224
MA v,56,v,220,v,188
AW 197,225
AW 198,226
MA v,120,v,200,a,248
TR v,216,254,33280
AW 199,227
MA v,120,v,204,a,252
TR v,218,254,33536
AR 224,80
MA v,124,v,204,v,164
buffer_load_dwordx4 v245, s[32:35], s69 offen offset:2048 lds
MA v,124,v,200,v,160
TR v,220,254,35328
AR 225,81
MA v,128,v,200,v,168
TR v,222,254,35584
AR 226,82
MA v,128,v,204,v,172
AR 227,83
AR 228,100
MA v,132,v,204,v,180
buffer_load_dwordx4 v246, s[32:35], s69 offen offset:3072 lds
MA v,132,v,200,v,176
s_add_u32 m0, s66, 4096
s_mov_b32 s70, 33408
MA v,136,v,200,v,184
AR 229,101
AR 230,102
MA v,136,v,204,v,188
s_waitcnt vmcnt(12) lgkmcnt(0)
s_barrier
MA v,60,v,208,a,248
TR v,200,253,49152
AR 231,103
MA v,60,v,212,a,252
TR v,202,253,49408
v_mul_f32 v224, v232, v224
MA v,64,v,212,v,164
buffer_load_dwordx4 v243, s[32:35], s70 offen offset:0 lds
MA v,64,v,208,v,160
TR v,204,253,51200
s_add_u32 m0, s64, 32768
MA v,68,v,208,v,168
TR v,206,253,51456
s_mov_b32 s67, 278528
MA v,68,v,212,v,172
R4 v,192,251,0
v_mul_f32 v225, v232, v225
MA v,72,v,212,v,180
R4 v,196,251,512
v_mul_f32 v226, v232, v226
MA v,72,v,208,v,176
buffer_load_dwordx4 v241, s[40:43], s67 offen offset:0 nt lds
MA v,76,v,208,v,184
R4 a,240,251,2048
v_mul_f32 v227, v232, v227
MA v,76,v,212,v,188
R4 a,244,251,2560
v_mul_f32 v228, v232, v228
MA v,140,v,216,a,248
TR v,208,254,49152
)ASM"
R"ASM(v_mul_f32 v229, v232, v229
MA v,140,v,220,a,252
TR v,210,254,49408
v_mul_f32 v230, v232, v230
MA v,144,v,220,v,164
buffer_load_dwordx4 v242, s[40:43], s67 offen offset:1024 nt lds
MA v,144,v,216,v,160
TR v,212,254,51200
v_mul_f32 v231, v232, v231
MA v,148,v,216,v,168
TR v,214,254,51456
CV 224,224,225
MA v,148,v,220,v,172
R4 a,248,251,4096
CV 225,226,227
MA v,152,v,220,v,180
R4 a,252,251,4608
CV 226,228,229
MA v,152,v,216,v,176
buffer_load_dwordx4 v241, s[40:43], s67 offen offset:2048 nt lds
MA v,156,v,216,v,184
R4 v,160,251,6144
CV 227,230,231
MA v,156,v,220,v,188
s_waitcnt lgkmcnt(2)
R4 v,164,251,6656
MA v,0,v,200,v,192
R4 v,168,251,8192
AW 200,224
MA v,0,v,204,v,196
R4 v,172,251,8704
AW 201,225
MA v,4,v,204,a,244
buffer_load_dwordx4 v242, s[40:43], s67 offen offset:3072 nt lds
MA v,4,v,200,a,240
TR v,216,253,49664
s_add_u32 m0, s64, 98304
MA v,8,v,200,a,248
TR v,218,253,49920
s_waitcnt lgkmcnt(3)
MA v,8,v,204,a,252
TR v,220,253,51712
s_mov_b32 s68, 2622080
MA v,12,v,204,v,164
TR v,222,253,51968
v_add_u32 v251, s71, v251
MA v,12,v,200,v,160
buffer_load_dwordx4 v247, s[36:39], s68 offen offset:0 lds
MA v,16,v,200,v,168
s_waitcnt lgkmcnt(2)
AW 202,226
MA v,16,v,204,v,172
AW 203,227
AR 224,84
MA v,80,v,208,v,192
TR v,200,254,49664
AR 225,85
MA v,80,v,212,v,196
TR v,202,254,49920
AR 226,86
MA v,88,v,212,a,244
buffer_load_dwordx4 v248, s[36:39], s68 offen offset:1024 lds
MA v,88,v,208,a,240
TR v,204,254,51712
AR 227,87
MA v,96,v,208,a,248
TR v,206,254,51968
AR 228,104
MA v,96,v,212,a,252
AR 229,105
AR 230,106
MA v,104,v,212,v,164
buffer_load_dwordx4 v249, s[36:39], s68 offen offset:2048 lds
MA v,104,v,208,v,160
buffer_load_dwordx4 v250, s[36:39], s68 offen offset:3072 lds
MA v,112,v,208,v,168
s_add_u32 m0, s64, 114688
s_mov_b32 s69, 3146240
MA v,112,v,212,v,172
s_waitcnt vmcnt(17) lgkmcnt(0)
s_barrier
MA v,20,v,216,v,192
TR v,208,253,0
AR 231,107
MA v,20,v,220,v,196
TR v,210,253,256
v_mul_f32 v224, v233, v224
MA v,24,v,220,a,244
buffer_load_dwordx4 v247, s[36:39], s69 offen offset:0 lds
MA v,24,v,216,a,240
TR v,212,253,2048
v_mul_f32 v225, v233, v225
MA v,28,v,216,a,248
TR v,214,253,2304
v_mul_f32 v226, v233, v226
MA v,28,v,220,a,252
v_mul_f32 v227, v233, v227
v_mul_f32 v228, v233, v228
MA v,32,v,220,v,164
buffer_load_dwordx4 v248, s[36:39], s69 offen offset:1024 lds
MA v,32,v,216,v,160
v_mul_f32 v229, v233, v229
v_mul_f32 v230, v233, v230
MA v,36,v,216,v,168
v_mul_f32 v231, v233, v231
CV 224,224,225
MA v,36,v,220,v,172
CV 225,226,227
CV 226,228,229
MA v,84,v,200,v,192
TR v,216,254,0
CV 227,230,231
MA v,84,v,204,v,196
TR v,218,254,256
AW 204,224
MA v,92,v,204,a,244
buffer_load_dwordx4 v249, s[36:39], s69 offen offset:2048 lds
MA v,92,v,200,a,240
TR v,220,254,2048
AW 205,225
MA v,100,v,200,a,248
TR v,222,254,2304
AW 206,226
MA v,100,v,204,a,252
AW 207,227
AR 224,88
MA v,108,v,204,v,164
buffer_load_dwordx4 v250, s[36:39], s69 offen offset:3072 lds
MA v,108,v,200,v,160
s_mov_b32 m0, s65
s_mov_b32 s70, 768
MA v,116,v,200,v,168
AR 225,89
AR 226,90
MA v,116,v,204,v,172
s_waitcnt lgkmcnt(0)
AR 227,91
MA v,40,v,208,v,192
TR v,200,253,512
AR 228,108
MA v,40,v,212,v,196
TR v,202,253,768
AR 229,109
MA v,44,v,212,a,244
buffer_load_dwordx4 v243, s[32:35], s70 offen offset:0 lds
MA v,44,v,208,a,240
TR v,204,253,2560
AR 230,110
MA v,48,v,208,a,248
TR v,206,253,2816
AR 231,111
MA v,48,v,212,a,252
v_mul_f32 v224, v234, v224
v_mul_f32 v225, v234, v225
MA v,52,v,212,v,164
buffer_load_dwordx4 v244, s[32:35], s70 offen offset:1024 lds
MA v,52,v,208,v,160
v_mul_f32 v226, v234, v226
v_mul_f32 v227, v234, v227
MA v,56,v,208,v,168
v_mul_f32 v228, v234, v228
v_mul_f32 v229, v234, v229
MA v,56,v,212,v,172
v_mul_f32 v230, v234, v230
v_mul_f32 v231, v234, v231
MA v,120,v,216,v,192
TR v,208,254,512
CV 224,224,225
MA v,120,v,220,v,196
TR v,210,254,768
CV 225,226,227
MA v,124,v,220,a,244
buffer_load_dwordx4 v245, s[32:35], s70 offen offset:2048 lds
MA v,124,v,216,a,240
TR v,212,254,2560
CV 226,228,229
MA v,128,v,216,a,248
TR v,214,254,2816
CV 227,230,231
MA v,128,v,220,a,252
AW 208,224
AW 209,225
MA v,132,v,220,v,164
buffer_load_dwordx4 v246, s[32:35], s70 offen offset:3072 lds
MA v,132,v,216,v,160
s_add_u32 m0, s65, 4096
s_mov_b32 s67, 33536
MA v,136,v,216,v,168
AW 210,226
AW 211,227
MA v,136,v,220,v,172
s_waitcnt vmcnt(16) lgkmcnt(0)
s_barrier
MA v,60,v,200,v,192
TR v,216,253,16384
AR 224,92
MA v,60,v,204,v,196
TR v,218,253,16640
AR 225,93
MA v,64,v,204,a,244
buffer_load_dwordx4 v243, s[32:35], s67 offen offset:0 lds
MA v,64,v,200,a,240
TR v,220,253,18432
s_add_u32 m0, s64, 65536
MA v,68,v,200,a,248
TR v,222,253,18688
s_mov_b32 s68, 262144
MA v,68,v,204,a,252
R4 v,176,251,0
AR 226,94
MA v,72,v,204,v,164
R4 v,180,251,512
AR 227,95
MA v,72,v,200,v,160
buffer_load_dwordx4 v241, s[44:47], s68 offen offset:0 nt lds
MA v,76,v,200,v,168
R4 v,184,251,2048
AR 228,112
MA v,76,v,204,v,172
R4 v,188,251,2560
AR 229,113
MA v,140,v,208,v,192
TR v,200,254,16384
AR 230,114
MA v,140,v,212,v,196
TR v,202,254,16640
AR 231,115
MA v,144,v,212,a,244
buffer_load_dwordx4 v242, s[44:47], s68 offen offset:1024 nt lds
MA v,144,v,208,a,240
TR v,204,254,18432
v_mul_f32 v224, v235, v224
MA v,148,v,208,a,248
TR v,206,254,18688
v_mul_f32 v225, v235, v225
MA v,148,v,212,a,252
R4 v,192,251,4096
v_mul_f32 v226, v235, v226
MA v,152,v,212,v,164
R4 v,196,251,4608
v_mul_f32 v227, v235, v227
MA v,152,v,208,v,160
buffer_load_dwordx4 v241, s[44:47], s68 offen offset:2048 nt lds
MA v,156,v,208,v,168
R4 a,240,251,6144
v_mul_f32 v228, v235, v228
MA v,156,v,212,v,172
s_waitcnt lgkmcnt(2)
R4 a,244,251,6656
MA v,0,v,216,v,176
R4 a,248,251,8192
v_mul_f32 v229, v235, v229
MA v,0,v,220,v,180
R4 a,252,251,8704
v_mul_f32 v230, v235, v230
MA v,4,v,220,v,188
buffer_load_dwordx4 v242, s[44:47], s68 offen offset:3072 nt lds
MA v,4,v,216,v,184
TR v,208,253,16896
v_subrev_u32 v251, s71, v251
MA v,8,v,216,v,192
TR v,210,253,17152
s_waitcnt lgkmcnt(3)
MA v,8,v,220,v,196
TR v,212,253,18944
v_mul_f32 v231, v235, v231
MA v,12,v,220,a,244
TR v,214,253,19200
CV 224,224,225
MA v,12,v,216,a,240
CV 225,226,227
CV 226,228,229
MA v,16,v,216,a,248
s_waitcnt lgkmcnt(2)
CV 227,230,231
MA v,16,v,220,a,252
AW 212,224
AW 213,225
MA v,80,v,200,v,176
TR v,216,254,16896
AW 214,226
MA v,80,v,204,v,180
TR v,218,254,17152
AW 215,227
MA v,88,v,204,v,188
AR 224,96
AR 225,97
MA v,88,v,200,v,184
TR v,220,254,18944
AR 226,98
MA v,96,v,200,v,192
TR v,222,254,19200
AR 227,99
MA v,96,v,204,v,196
AR 228,116
AR 229,117
MA v,104,v,204,a,244
s_add_u32 m0, s64, 81920
s_mov_b32 s69, 3146368
MA v,104,v,200,a,240
AR 230,118
AR 231,119
MA v,112,v,200,a,248
v_mul_f32 v224, v236, v224
v_mul_f32 v225, v236, v225
MA v,112,v,204,a,252
s_waitcnt vmcnt(13) lgkmcnt(0)
s_barrier
MA v,20,v,208,v,176
TR v,200,253,32768
v_mul_f32 v226, v236, v226
MA v,20,v,212,v,180
TR v,202,253,33024
v_mul_f32 v227, v236, v227
MA v,24,v,212,v,188
buffer_load_dwordx4 v247, s[36:39], s69 offen offset:0 lds
MA v,24,v,208,v,184
TR v,204,253,34816
v_mul_f32 v228, v236, v228
MA v,28,v,208,v,192
TR v,206,253,35072
v_mul_f32 v229, v236, v229
MA v,28,v,212,v,196
v_mul_f32 v230, v236, v230
v_mul_f32 v231, v236, v231
MA v,32,v,212,a,244
buffer_load_dwordx4 v248, s[36:39], s69 offen offset:1024 lds
MA v,32,v,208,a,240
CV 224,224,225
CV 225,226,227
MA v,36,v,208,a,248
CV 226,228,229
CV 227,230,231
MA v,36,v,212,a,252
AW 216,224
AW 217,225
MA v,84,v,216,v,176
TR v,208,254,32768
AW 218,226
MA v,84,v,220,v,180
TR v,210,254,33024
AW 219,227
MA v,92,v,220,v,188
buffer_load_dwordx4 v249, s[36:39], s69 offen offset:2048 lds
MA v,92,v,216,v,184
TR v,212,254,34816
AR 224,120
MA v,100,v,216,v,192
TR v,214,254,35072
AR 225,121
MA v,100,v,220,v,196
AR 226,122
AR 227,123
MA v,108,v,220,a,244
buffer_load_dwordx4 v250, s[36:39], s69 offen offset:3072 lds
MA v,108,v,216,a,240
s_mov_b32 m0, s66
s_mov_b32 s70, 896
MA v,116,v,216,a,248
AR 228,140
AR 229,141
MA v,116,v,220,a,252
s_waitcnt lgkmcnt(0)
AR 230,142
MA v,40,v,200,v,176
TR v,216,253,33280
AR 231,143
MA v,40,v,204,v,180
TR v,218,253,33536
v_mul_f32 v224, v232, v224
MA v,44,v,204,v,188
buffer_load_dwordx4 v243, s[32:35], s70 offen offset:0 lds
MA v,44,v,200,v,184
TR v,220,253,35328
v_mul_f32 v225, v232, v225
MA v,48,v,200,v,192
TR v,222,253,35584
v_mul_f32 v226, v232, v226
MA v,48,v,204,v,196
v_mul_f32 v227, v232, v227
v_mul_f32 v228, v232, v228
MA v,52,v,204,a,244
buffer_load_dwordx4 v244, s[32:35], s70 offen offset:1024 lds
MA v,52,v,200,a,240
v_mul_f32 v229, v232, v229
v_mul_f32 v230, v232, v230
MA v,56,v,200,a,248
v_mul_f32 v231, v232, v231
CV 224,224,225
MA v,56,v,204,a,252
CV 225,226,227
CV 226,228,229
MA v,120,v,208,v,176
TR v,200,254,33280
CV 227,230,231
MA v,120,v,212,v,180
TR v,202,254,33536
AW 220,224
MA v,124,v,212,v,188
buffer_load_dwordx4 v245, s[32:35], s70 offen offset:2048 lds
MA v,124,v,208,v,184
TR v,204,254,35328
AW 221,225
MA v,128,v,208,v,192
TR v,206,254,35584
AW 222,226
MA v,128,v,212,v,196
AW 223,227
AR 224,124
MA v,132,v,212,a,244
buffer_load_dwordx4 v246, s[32:35], s70 offen offset:3072 lds
MA v,132,v,208,a,240
s_add_u32 m0, s66, 4096
s_mov_b32 s67, 33664
MA v,136,v,208,a,248
AR 225,125
AR 226,126
MA v,136,v,212,a,252
s_waitcnt vmcnt(12) lgkmcnt(0)
s_barrier
MA v,60,v,216,v,176
TR v,208,253,49152
AR 227,127
MA v,60,v,220,v,180
TR v,210,253,49408
AR 228,144
MA v,64,v,220,v,188
buffer_load_dwordx4 v243, s[32:35], s67 offen offset:0 lds
MA v,64,v,216,v,184
TR v,212,253,51200
s_add_u32 m0, s64, 98304
MA v,68,v,216,v,192
TR v,214,253,51456
s_mov_b32 s68, 3670528
MA v,68,v,220,v,196
R4 v,160,251,0
AR 229,145
MA v,72,v,220,a,244
R4 v,164,251,512
AR 230,146
MA v,72,v,216,a,240
buffer_load_dwordx4 v247, s[36:39], s68 offen offset:0 lds
MA v,76,v,216,a,248
R4 v,168,251,2048
AR 231,147
MA v,76,v,220,a,252
R4 v,172,251,2560
v_mul_f32 v224, v233, v224
MA v,140,v,200,v,176
TR v,216,254,49152
v_mul_f32 v225, v233, v225
MA v,140,v,204,v,180
TR v,218,254,49408
v_mul_f32 v226, v233, v226
MA v,144,v,204,v,188
buffer_load_dwordx4 v248, s[36:39], s68 offen offset:1024 lds
MA v,144,v,200,v,184
TR v,220,254,51200
v_mul_f32 v227, v233, v227
MA v,148,v,200,v,192
TR v,222,254,51456
v_mul_f32 v228, v233, v228
MA v,148,v,204,v,196
R4 v,176,251,4096
v_mul_f32 v229, v233, v229
MA v,152,v,204,a,244
R4 v,180,251,4608
v_mul_f32 v230, v233, v230
MA v,152,v,200,a,240
buffer_load_dwordx4 v249, s[36:39], s68 offen offset:2048 lds
MA v,156,v,200,a,248
R4 v,184,251,6144
v_mul_f32 v231, v233, v231
MA v,156,v,204,a,252
s_waitcnt lgkmcnt(2)
R4 v,188,251,6656
MA v,0,v,208,v,160
R4 v,192,251,8192
CV 224,224,225
MA v,0,v,212,v,164
R4 v,196,251,8704
CV 225,226,227
MA v,4,v,212,v,172
buffer_load_dwordx4 v250, s[36:39], s68 offen offset:3072 lds
MA v,4,v,208,v,168
TR v,200,253,49664
v_add_u32 v251, s71, v251
MA v,8,v,208,v,176
TR v,202,253,49920
s_waitcnt lgkmcnt(3)
MA v,8,v,212,v,180
TR v,204,253,51712
CV 226,228,229
MA v,12,v,212,v,188
TR v,206,253,51968
CV 227,230,231
MA v,12,v,208,v,184
AW 224,224
AW 225,225
MA v,16,v,208,v,192
s_waitcnt lgkmcnt(2)
AW 226,226
MA v,16,v,212,v,196
AW 227,227
AR 224,128
MA v,80,v,216,v,160
TR v,208,254,49664
AR 225,129
MA v,80,v,220,v,164
TR v,210,254,49920
AR 226,130
MA v,88,v,220,v,172
AR 227,131
AR 228,148
MA v,88,v,216,v,168
TR v,212,254,51712
AR 229,149
MA v,96,v,216,v,176
TR v,214,254,51968
AR 230,150
MA v,96,v,220,v,180
AR 231,151
v_mul_f32 v224, v234, v224
MA v,104,v,220,v,188
s_add_u32 m0, s64, 114688
s_mov_b32 s69, 3670656
MA v,104,v,216,v,184
v_mul_f32 v225, v234, v225
v_mul_f32 v226, v234, v226
MA v,112,v,216,v,192
v_mul_f32 v227, v234, v227
v_mul_f32 v228, v234, v228
MA v,112,v,220,v,196
s_waitcnt vmcnt(9) lgkmcnt(0)
s_barrier
MA v,20,v,200,v,160
TR v,216,253,16384
v_mul_f32 v229, v234, v229
MA v,20,v,204,v,164
TR v,218,253,16640
v_mul_f32 v230, v234, v230
MA v,24,v,204,v,172
buffer_load_dwordx4 v247, s[36:39], s69 offen offset:0 lds
MA v,24,v,200,v,168
TR v,220,253,18432
v_mul_f32 v231, v234, v231
MA v,28,v,200,v,176
TR v,222,253,18688
CV 224,224,225
MA v,28,v,204,v,180
CV 225,226,227
CV 226,228,229
MA v,32,v,204,v,188
buffer_load_dwordx4 v248, s[36:39], s69 offen offset:1024 lds
)ASM"
R"ASM(MA v,32,v,200,v,184
CV 227,230,231
AW 228,224
MA v,36,v,200,v,192
AW 229,225
AW 230,226
MA v,36,v,204,v,196
AW 231,227
AR 224,132
MA v,84,v,208,v,160
TR v,200,254,16384
AR 225,133
MA v,84,v,212,v,164
TR v,202,254,16640
AR 226,134
MA v,92,v,212,v,172
buffer_load_dwordx4 v249, s[36:39], s69 offen offset:2048 lds
MA v,92,v,208,v,168
TR v,204,254,18432
AR 227,135
MA v,100,v,208,v,176
TR v,206,254,18688
AR 228,152
MA v,100,v,212,v,180
AR 229,153
AR 230,154
MA v,108,v,212,v,188
buffer_load_dwordx4 v250, s[36:39], s69 offen offset:3072 lds
MA v,108,v,208,v,184
AR 231,155
v_mul_f32 v224, v235, v224
MA v,116,v,208,v,192
v_mul_f32 v225, v235, v225
v_mul_f32 v226, v235, v226
MA v,116,v,212,v,196
s_waitcnt lgkmcnt(0)
v_mul_f32 v227, v235, v227
MA v,40,v,216,v,160
TR v,208,253,16896
v_mul_f32 v228, v235, v228
MA v,40,v,220,v,164
TR v,210,253,17152
v_mul_f32 v229, v235, v229
MA v,44,v,220,v,172
v_mul_f32 v230, v235, v230
v_mul_f32 v231, v235, v231
MA v,44,v,216,v,168
TR v,212,253,18944
CV 224,224,225
MA v,48,v,216,v,176
TR v,214,253,19200
CV 225,226,227
MA v,48,v,220,v,180
CV 226,228,229
CV 227,230,231
MA v,52,v,220,v,188
AW 232,224
AW 233,225
MA v,52,v,216,v,184
AW 234,226
AW 235,227
MA v,56,v,216,v,192
AR 224,136
AR 225,137
MA v,56,v,220,v,196
AR 226,138
AR 227,139
MA v,120,v,200,v,160
TR v,216,254,16896
AR 228,156
MA v,120,v,204,v,164
TR v,218,254,17152
AR 229,157
MA v,124,v,204,v,172
AR 230,158
AR 231,159
MA v,124,v,200,v,168
TR v,220,254,18944
v_mul_f32 v224, v236, v224
MA v,128,v,200,v,176
TR v,222,254,19200
v_mul_f32 v225, v236, v225
MA v,128,v,204,v,180
v_mul_f32 v226, v236, v226
v_mul_f32 v227, v236, v227
MA v,132,v,204,v,188
s_add_u32 m0, s64, 81920
s_mov_b32 s70, 270336
MA v,132,v,200,v,184
v_mul_f32 v228, v236, v228
v_mul_f32 v229, v236, v229
MA v,136,v,200,v,192
v_mul_f32 v230, v236, v230
v_mul_f32 v231, v236, v231
MA v,136,v,204,v,196
s_waitcnt vmcnt(4) lgkmcnt(0)
s_barrier
MA v,60,v,208,v,160
TR v,200,253,32768
CV 224,224,225
MA v,60,v,212,v,164
TR v,202,253,33024
CV 225,226,227
MA v,64,v,212,v,172
buffer_load_dwordx4 v241, s[44:47], s70 offen offset:0 nt lds
MA v,64,v,208,v,168
TR v,204,253,34816
CV 226,228,229
MA v,68,v,208,v,176
TR v,206,253,35072
CV 227,230,231
MA v,68,v,212,v,180
R4 a,240,251,0
AW 236,224
MA v,72,v,212,v,188
R4 a,244,251,512
AW 237,225
MA v,72,v,208,v,184
buffer_load_dwordx4 v242, s[44:47], s70 offen offset:1024 nt lds
MA v,76,v,208,v,192
R4 a,248,251,2048
AW 238,226
MA v,76,v,212,v,196
R4 a,252,251,2560
AW 239,227
MA v,140,v,216,v,160
TR v,208,254,32768
MA v,140,v,220,v,164
TR v,210,254,33024
MA v,144,v,220,v,172
buffer_load_dwordx4 v241, s[44:47], s70 offen offset:2048 nt lds
MA v,144,v,216,v,168
TR v,212,254,34816
MA v,148,v,216,v,176
TR v,214,254,35072
MA v,148,v,220,v,180
R4 v,160,251,4096
MA v,152,v,220,v,188
R4 v,164,251,4608
MA v,152,v,216,v,184
buffer_load_dwordx4 v242, s[44:47], s70 offen offset:3072 nt lds
MA v,156,v,216,v,192
R4 v,168,251,6144
MA v,156,v,220,v,196
s_waitcnt lgkmcnt(2)
R4 v,172,251,6656
MA v,0,v,200,a,240
R4 v,176,251,8192
MA v,0,v,204,a,244
R4 v,180,251,8704
MA v,4,v,204,a,252
MA v,4,v,200,a,248
TR v,216,253,33280
MA v,8,v,200,v,160
TR v,218,253,33536
s_waitcnt lgkmcnt(3)
MA v,8,v,204,v,164
TR v,220,253,35328
v_mul_f32 v0, s82, v0
MA v,12,v,204,v,172
TR v,222,253,35584
v_mul_f32 v1, s82, v1
MA v,12,v,200,v,168
v_mul_f32 v2, s82, v2
v_mul_f32 v3, s82, v3
MA v,16,v,200,v,176
s_waitcnt lgkmcnt(2)
CV 0,0,1
MA v,16,v,204,v,180
CV 1,2,3
v_mul_f32 v4, s82, v4
MA v,80,v,208,a,240
TR v,200,254,33280
v_mul_f32 v5, s82, v5
MA v,80,v,212,a,244
TR v,202,254,33536
v_mul_f32 v6, s82, v6
MA v,88,v,212,a,252
v_mul_f32 v7, s82, v7
CV 4,4,5
MA v,88,v,208,a,248
TR v,204,254,35328
CV 5,6,7
MA v,96,v,208,v,160
TR v,206,254,35584
v_mul_f32 v80, s82, v80
MA v,96,v,212,v,164
v_mul_f32 v81, s82, v81
v_mul_f32 v82, s82, v82
MA v,104,v,212,v,172
s_add_u32 m0, s64, 98304
s_mov_b32 s67, 278528
MA v,104,v,208,v,168
v_mul_f32 v83, s82, v83
CV 2,80,81
MA v,112,v,208,v,176
CV 3,82,83
v_mul_f32 v88, s82, v88
MA v,112,v,212,v,180
s_waitcnt vmcnt(4) lgkmcnt(0)
s_barrier
MA v,20,v,216,a,240
TR v,208,253,49152
v_mul_f32 v89, s82, v89
MA v,20,v,220,a,244
TR v,210,253,49408
v_mul_f32 v90, s82, v90
MA v,24,v,220,a,252
buffer_load_dwordx4 v241, s[44:47], s67 offen offset:0 nt lds
MA v,24,v,216,a,248
TR v,212,253,51200
v_mul_f32 v91, s82, v91
MA v,28,v,216,v,160
TR v,214,253,51456
v_mul_f32 v20, s82, v20
MA v,28,v,220,v,164
v_mul_f32 v21, s82, v21
v_mul_f32 v22, s82, v22
MA v,32,v,220,v,172
buffer_load_dwordx4 v242, s[44:47], s67 offen offset:1024 nt lds
MA v,32,v,216,v,168
v_mul_f32 v23, s82, v23
CV 20,20,21
MA v,36,v,216,v,176
CV 21,22,23
CV 6,88,89
MA v,36,v,220,v,180
CV 7,90,91
v_mul_f32 v24, s82, v24
MA v,84,v,200,a,240
TR v,216,254,49152
v_mul_f32 v25, s82, v25
MA v,84,v,204,a,244
TR v,218,254,49408
v_mul_f32 v26, s82, v26
MA v,92,v,204,a,252
buffer_load_dwordx4 v241, s[44:47], s67 offen offset:2048 nt lds
MA v,92,v,200,a,248
TR v,220,254,51200
v_mul_f32 v27, s82, v27
MA v,100,v,200,v,160
TR v,222,254,51456
v_mul_f32 v84, s82, v84
MA v,100,v,204,v,164
v_mul_f32 v85, s82, v85
v_mul_f32 v86, s82, v86
MA v,108,v,204,v,172
buffer_load_dwordx4 v242, s[44:47], s67 offen offset:3072 nt lds
MA v,108,v,200,v,168
s_add_u32 m0, s64, 49152
s_mov_b32 s68, 286720
MA v,116,v,200,v,176
v_mul_f32 v87, s82, v87
CV 22,84,85
MA v,116,v,204,v,180
s_waitcnt lgkmcnt(0)
CV 23,86,87
MA v,40,v,208,a,240
TR v,200,253,49664
CV 24,24,25
MA v,40,v,212,a,244
TR v,202,253,49920
CV 25,26,27
MA v,44,v,212,a,252
buffer_load_dwordx4 v241, s[40:43], s68 offen offset:0 nt lds
MA v,44,v,208,a,248
TR v,204,253,51712
v_mul_f32 v92, s82, v92
MA v,48,v,208,v,160
TR v,206,253,51968
v_mul_f32 v40, s82, v40
MA v,48,v,212,v,164
v_mul_f32 v41, s82, v41
v_mul_f32 v42, s82, v42
MA v,52,v,212,v,172
buffer_load_dwordx4 v242, s[40:43], s68 offen offset:1024 nt lds
MA v,52,v,208,v,168
v_mul_f32 v43, s82, v43
CV 40,40,41
MA v,56,v,208,v,176
CV 41,42,43
v_mul_f32 v93, s82, v93
MA v,56,v,212,v,180
v_mul_f32 v94, s82, v94
v_mul_f32 v95, s82, v95
MA v,120,v,216,a,240
TR v,208,254,49664
CV 26,92,93
MA v,120,v,220,a,244
TR v,210,254,49920
CV 27,94,95
MA v,124,v,220,a,252
buffer_load_dwordx4 v241, s[40:43], s68 offen offset:2048 nt lds
MA v,124,v,216,a,248
TR v,212,254,51712
v_mul_f32 v44, s82, v44
MA v,128,v,216,v,160
TR v,214,254,51968
v_mul_f32 v120, s82, v120
MA v,128,v,220,v,164
v_mul_f32 v121, s82, v121
v_mul_f32 v122, s82, v122
MA v,132,v,220,v,172
buffer_load_dwordx4 v242, s[40:43], s68 offen offset:3072 nt lds
MA v,132,v,216,v,168
s_add_u32 m0, s64, 114688
s_mov_b32 s69, 286720
MA v,136,v,216,v,176
v_mul_f32 v123, s82, v123
CV 42,120,121
MA v,136,v,220,v,180
s_waitcnt lgkmcnt(0)
s_barrier
MA v,60,v,200,a,240
buffer_load_dwordx4 v241, s[44:47], s69 offen offset:0 nt lds
MA v,60,v,204,a,244
CV 43,122,123
v_mul_f32 v45, s82, v45
MA v,64,v,204,a,252
v_mul_f32 v46, s82, v46
v_mul_f32 v47, s82, v47
MA v,64,v,200,a,248
CV 44,44,45
CV 45,46,47
MA v,68,v,200,v,160
buffer_load_dwordx4 v242, s[44:47], s69 offen offset:1024 nt lds
MA v,68,v,204,v,164
v_mul_f32 v60, s82, v60
v_mul_f32 v61, s82, v61
MA v,72,v,204,v,172
v_mul_f32 v62, s82, v62
v_mul_f32 v63, s82, v63
MA v,72,v,200,v,168
CV 60,60,61
CV 61,62,63
MA v,76,v,200,v,176
buffer_load_dwordx4 v241, s[44:47], s69 offen offset:2048 nt lds
MA v,76,v,204,v,180
v_mul_f32 v124, s82, v124
v_mul_f32 v125, s82, v125
MA v,140,v,208,a,240
R4 v,200,238,0
v_mul_f32 v126, s82, v126
MA v,140,v,212,a,244
R4 v,204,238,512
v_mul_f32 v127, s82, v127
MA v,144,v,212,a,252
buffer_load_dwordx4 v242, s[44:47], s69 offen offset:3072 nt lds
MA v,144,v,208,a,248
CV 46,124,125
CV 47,126,127
MA v,148,v,208,v,160
R4 a,240,238,4096
v_mul_f32 v140, s82, v140
MA v,148,v,212,v,164
R4 a,244,238,4608
v_mul_f32 v141, s82, v141
MA v,152,v,212,v,172
R4 a,248,238,5120
v_mul_f32 v142, s82, v142
MA v,152,v,208,v,168
R4 a,252,238,5632
v_mul_f32 v143, s82, v143
MA v,156,v,208,v,176
R4 v,208,238,1024
CV 62,140,141
MA v,156,v,212,v,180
R4 v,212,238,1536
s_waitcnt lgkmcnt(4)
MZ v,80,v,200,v,0
CV 63,142,143
v_mul_f32 v64, s82, v64
MA v,80,v,204,v,20
s_waitcnt lgkmcnt(1)
v_mul_f32 v65, s82, v65
MA v,80,v,208,v,40
s_waitcnt lgkmcnt(0)
v_mul_f32 v66, s82, v66
MA v,80,v,212,v,60
v_mul_f32 v67, s82, v67
CV 64,64,65
MZ v,84,a,252,v,60
s_add_u32 m0, s64, 0
s_mov_b32 s70, 294912
MA v,84,a,248,v,40
CV 65,66,67
v_mul_f32 v144, s82, v144
MA v,84,a,244,v,20
s_waitcnt lgkmcnt(0)
s_barrier
v_max3_f32 v120, v80, v81, v82
MA v,84,a,240,v,0
v_max_f32 v120, v120, v83
v_mov_b32 v121, v120
MZ v,88,v,200,v,4
buffer_load_dwordx4 v241, s[40:43], s70 offen offset:0 nt lds
v_mul_f32 v145, s82, v145
v_mul_f32 v146, s82, v146
v_mul_f32 v147, s82, v147
CV 66,144,145
CV 67,146,147
v_permlane32_swap_b32 v120, v121
MA v,88,v,204,v,24
v_max_f32 v120, v120, v121
v_mov_b32 v121, v120
MA v,88,v,208,v,44
s_nop 0
v_permlane16_swap_b32 v120, v121
v_max_f32 v120, v120, v121
v_sub_f32 v140, 0, v120
v_sub_f32 v141, 0, v120
v_sub_f32 v142, 0, v120
v_sub_f32 v143, 0, v120
v_add_f32 v80, v140, v80
v_exp_f32 v80, v80
MA v,88,v,212,v,64
v_add_f32 v81, v140, v81
v_exp_f32 v81, v81
MZ v,92,a,252,v,64
s_memrealtime s[98:99]
v_add_f32 v82, v140, v82
v_exp_f32 v82, v82
MA v,92,a,248,v,44
buffer_load_dwordx4 v242, s[40:43], s70 offen offset:1024 nt lds
v_mul_f32 v8, s82, v8
v_mul_f32 v9, s82, v9
v_mul_f32 v10, s82, v10
v_mul_f32 v11, s82, v11
CV 8,8,9
CV 9,10,11
v_mul_f32 v96, s82, v96
v_mul_f32 v97, s82, v97
v_mul_f32 v98, s82, v98
v_mul_f32 v99, s82, v99
CV 10,96,97
CV 11,98,99
v_add_f32 v83, v140, v83
v_exp_f32 v83, v83
MA v,92,a,244,v,24
buffer_load_dwordx4 v241, s[40:43], s70 offen offset:2048 nt lds
v_mul_f32 v28, s82, v28
v_mul_f32 v29, s82, v29
v_mul_f32 v30, s82, v30
v_mul_f32 v31, s82, v31
CV 28,28,29
CV 29,30,31
v_mul_f32 v100, s82, v100
v_mul_f32 v101, s82, v101
v_mul_f32 v102, s82, v102
v_mul_f32 v103, s82, v103
CV 30,100,101
CV 31,102,103
v_add_f32 v84, v140, v84
v_exp_f32 v84, v84
MA v,92,a,240,v,4
s_waitcnt lgkmcnt(0)
s_add_u32 s76, s98, s29
s_barrier
TR v,160,239,0
TR v,162,239,4096
TR v,164,240,0
TR v,166,240,4096
TR v,168,239,512
TR v,170,239,4608
TR v,172,240,512
TR v,174,240,4608
v_mul_f32 v48, s82, v48
v_mul_f32 v49, s82, v49
v_mul_f32 v50, s82, v50
v_mul_f32 v51, s82, v51
CV 48,48,49
CV 49,50,51
v_mul_f32 v128, s82, v128
v_mul_f32 v129, s82, v129
v_mul_f32 v130, s82, v130
v_mul_f32 v131, s82, v131
CV 50,128,129
CV 51,130,131
v_add_f32 v85, v140, v85
v_max3_f32 v124, v88, v89, v90
v_exp_f32 v85, v85
MZ v,96,v,200,v,8
TR v,176,239,1024
v_mul_f32 v68, s82, v68
v_mul_f32 v69, s82, v69
v_mul_f32 v70, s82, v70
v_mul_f32 v71, s82, v71
CV 68,68,69
CV 69,70,71
v_mul_f32 v148, s82, v148
v_mul_f32 v149, s82, v149
v_mul_f32 v150, s82, v150
v_mul_f32 v151, s82, v151
CV 70,148,149
CV 71,150,151
v_add_f32 v86, v140, v86
v_max_f32 v124, v124, v91
v_exp_f32 v86, v86
MA v,96,v,204,v,28
TR v,178,239,5120
v_add_f32 v87, v140, v87
v_mov_b32 v125, v124
v_exp_f32 v87, v87
MA v,96,v,208,v,48
TR v,180,240,1024
v_permlane32_swap_b32 v124, v125
MA v,96,v,212,v,68
TR v,182,240,5120
v_max_f32 v124, v124, v125
MZ v,100,a,252,v,68
TR v,184,239,1536
v_mov_b32 v125, v124
MA v,100,a,248,v,48
TR v,186,239,5632
v_permlane16_swap_b32 v124, v125
CV 120,80,81
CV 121,82,83
CV 122,84,85
CV 123,86,87
MA v,100,a,244,v,28
TR v,188,240,1536
v_and_b32 v216, 3, v255
v_max_f32 v124, v124, v125
MA v,100,a,240,v,8
s_waitcnt lgkmcnt(3)
TR v,190,240,5632
v_cmp_eq_u32 vcc, 0, v216
v_sub_f32 v144, 0, v124
v_max3_f32 v128, v96, v97, v98
MZ a,0,v,160,v,120
buffer_load_dwordx4 v242, s[40:43], s70 offen offset:3072 nt lds
v_mov_b32 v217, 0x3f803f80
MZ a,20,v,164,v,120
v_cndmask_b32 v216, 0, v217, vcc
v_sub_f32 v145, 0, v124
v_max_f32 v128, v128, v99
MZ a,40,v,168,v,120
v_mov_b32 v217, v216
v_sub_f32 v146, 0, v124
v_mov_b32 v129, v128
MZ a,60,v,172,v,120
v_mov_b32 v218, v216
v_sub_f32 v147, 0, v124
v_add_f32 v88, v144, v88
v_exp_f32 v88, v88
MZ a,80,v,176,v,120
v_mov_b32 v219, v216
v_add_f32 v89, v144, v89
v_permlane32_swap_b32 v128, v129
v_exp_f32 v89, v89
MZ a,100,v,180,v,120
s_waitcnt lgkmcnt(0)
v_add_f32 v90, v144, v90
)ASM"
R"ASM(v_max_f32 v128, v128, v129
v_exp_f32 v90, v90
MZ a,120,v,184,v,120
v_mul_f32 v12, s82, v12
v_mul_f32 v13, s82, v13
v_mul_f32 v14, s82, v14
v_mul_f32 v15, s82, v15
CV 12,12,13
CV 13,14,15
v_mul_f32 v104, s82, v104
v_mul_f32 v105, s82, v105
v_mul_f32 v106, s82, v106
v_mul_f32 v107, s82, v107
CV 14,104,105
CV 15,106,107
v_add_f32 v91, v144, v91
v_exp_f32 v91, v91
MZ a,140,v,188,v,120
v_mul_f32 v32, s82, v32
v_mul_f32 v33, s82, v33
v_mul_f32 v34, s82, v34
v_mul_f32 v35, s82, v35
CV 32,32,33
CV 33,34,35
v_mul_f32 v108, s82, v108
v_mul_f32 v109, s82, v109
v_mul_f32 v110, s82, v110
v_mul_f32 v111, s82, v111
CV 34,108,109
CV 35,110,111
v_add_f32 v92, v144, v92
v_mov_b32 v129, v128
v_exp_f32 v92, v92
MZ v,192,v,216,v,120
v_mul_f32 v52, s82, v52
v_mul_f32 v53, s82, v53
v_mul_f32 v54, s82, v54
v_mul_f32 v55, s82, v55
CV 52,52,53
CV 53,54,55
v_mul_f32 v132, s82, v132
v_mul_f32 v133, s82, v133
v_mul_f32 v134, s82, v134
v_mul_f32 v135, s82, v135
CV 54,132,133
CV 55,134,135
v_add_f32 v93, v144, v93
v_permlane16_swap_b32 v128, v129
v_exp_f32 v93, v93
MZ v,104,v,200,v,12
v_mul_f32 v72, s82, v72
v_mul_f32 v73, s82, v73
v_mul_f32 v74, s82, v74
v_mul_f32 v75, s82, v75
CV 72,72,73
CV 73,74,75
v_mul_f32 v152, s82, v152
v_mul_f32 v153, s82, v153
v_mul_f32 v154, s82, v154
v_mul_f32 v155, s82, v155
CV 74,152,153
CV 75,154,155
v_add_f32 v94, v144, v94
v_exp_f32 v94, v94
MA v,104,v,204,v,32
v_add_f32 v95, v144, v95
v_max_f32 v128, v128, v129
v_exp_f32 v95, v95
MA v,104,v,208,v,52
v_sub_f32 v148, 0, v128
v_sub_f32 v149, 0, v128
MA v,104,v,212,v,72
CV 124,88,89
CV 125,90,91
MZ v,108,a,252,v,72
CV 126,92,93
CV 127,94,95
MA v,108,a,248,v,52
v_sub_f32 v150, 0, v128
v_sub_f32 v151, 0, v128
MA v,108,a,244,v,32
v_max3_f32 v132, v104, v105, v106
v_add_f32 v96, v148, v96
MA v,108,a,240,v,12
v_exp_f32 v96, v96
MZ a,4,v,160,v,124
v_max_f32 v132, v132, v107
v_add_f32 v97, v148, v97
MZ a,24,v,164,v,124
v_mov_b32 v133, v132
v_add_f32 v98, v148, v98
MZ a,44,v,168,v,124
v_exp_f32 v97, v97
MZ a,64,v,172,v,124
v_permlane32_swap_b32 v132, v133
v_add_f32 v99, v148, v99
MZ a,84,v,176,v,124
v_exp_f32 v98, v98
MZ a,104,v,180,v,124
v_max_f32 v132, v132, v133
v_mul_f32 v16, s82, v16
MZ a,124,v,184,v,124
v_mul_f32 v17, s82, v17
v_mul_f32 v18, s82, v18
v_mul_f32 v19, s82, v19
CV 16,16,17
CV 17,18,19
v_mul_f32 v112, s82, v112
v_mul_f32 v113, s82, v113
v_mul_f32 v114, s82, v114
v_mul_f32 v115, s82, v115
CV 18,112,113
CV 19,114,115
v_exp_f32 v99, v99
MZ a,144,v,188,v,124
v_mul_f32 v36, s82, v36
v_mul_f32 v37, s82, v37
v_mul_f32 v38, s82, v38
v_mul_f32 v39, s82, v39
CV 36,36,37
CV 37,38,39
v_mul_f32 v116, s82, v116
v_mul_f32 v117, s82, v117
v_mul_f32 v118, s82, v118
v_mul_f32 v119, s82, v119
CV 38,116,117
CV 39,118,119
v_add_f32 v100, v148, v100
v_mov_b32 v133, v132
v_exp_f32 v100, v100
MZ v,196,v,216,v,124
v_mul_f32 v56, s82, v56
v_mul_f32 v57, s82, v57
v_mul_f32 v58, s82, v58
v_mul_f32 v59, s82, v59
CV 56,56,57
CV 57,58,59
v_mul_f32 v136, s82, v136
v_mul_f32 v137, s82, v137
v_mul_f32 v138, s82, v138
v_mul_f32 v139, s82, v139
CV 58,136,137
CV 59,138,139
v_add_f32 v101, v148, v101
v_permlane16_swap_b32 v132, v133
v_exp_f32 v101, v101
MZ v,112,v,200,v,16
R4 v,200,238,16384
v_mul_f32 v76, s82, v76
v_mul_f32 v77, s82, v77
v_mul_f32 v78, s82, v78
v_mul_f32 v79, s82, v79
CV 76,76,77
CV 77,78,79
v_mul_f32 v156, s82, v156
v_mul_f32 v157, s82, v157
v_mul_f32 v158, s82, v158
v_mul_f32 v159, s82, v159
CV 78,156,157
CV 79,158,159
v_add_f32 v102, v148, v102
v_exp_f32 v102, v102
MA v,112,v,204,v,36
R4 v,204,238,16896
v_add_f32 v103, v148, v103
v_max_f32 v132, v132, v133
v_exp_f32 v103, v103
MA v,112,v,208,v,56
R4 v,208,238,17408
v_sub_f32 v152, 0, v132
MA v,112,v,212,v,76
R4 v,212,238,17920
v_sub_f32 v153, 0, v132
MZ v,116,a,252,v,76
R4 a,252,238,22016
CV 128,96,97
MA v,116,a,248,v,56
R4 a,248,238,21504
CV 129,98,99
CV 130,100,101
CV 131,102,103
MA v,116,a,244,v,36
R4 a,244,238,20992
v_sub_f32 v154, 0, v132
MA v,116,a,240,v,16
R4 a,240,238,20480
v_sub_f32 v155, 0, v132
v_max3_f32 v136, v112, v113, v114
MZ a,8,v,160,v,128
v_add_f32 v104, v152, v104
v_max_f32 v136, v136, v115
MZ a,28,v,164,v,128
v_add_f32 v105, v152, v105
v_mov_b32 v137, v136
MZ a,48,v,168,v,128
v_exp_f32 v104, v104
MZ a,68,v,172,v,128
v_exp_f32 v105, v105
MZ a,88,v,176,v,128
v_add_f32 v106, v152, v106
v_permlane32_swap_b32 v136, v137
MZ a,108,v,180,v,128
v_add_f32 v107, v152, v107
v_max_f32 v136, v136, v137
v_exp_f32 v106, v106
MZ a,128,v,184,v,128
v_exp_f32 v107, v107
MZ a,148,v,188,v,128
v_add_f32 v108, v152, v108
v_mov_b32 v137, v136
v_exp_f32 v108, v108
MZ v,220,v,216,v,128
s_waitcnt lgkmcnt(0)
v_add_f32 v109, v152, v109
v_permlane16_swap_b32 v136, v137
v_exp_f32 v109, v109
MC v,80,v,200,v,0,140
v_add_f32 v110, v152, v110
v_exp_f32 v110, v110
MA v,80,v,204,v,20
v_add_f32 v111, v152, v111
v_max_f32 v136, v136, v137
v_exp_f32 v111, v111
MA v,80,v,208,v,40
v_sub_f32 v156, 0, v136
v_sub_f32 v157, 0, v136
MA v,80,v,212,v,60
CV 132,104,105
CV 133,106,107
MC v,84,a,252,v,60,140
CV 134,108,109
CV 135,110,111
MA v,84,a,248,v,40
v_sub_f32 v158, 0, v136
v_sub_f32 v159, 0, v136
MA v,84,a,244,v,20
v_exp_f32 v80, v80
MA v,84,a,240,v,0
v_exp_f32 v81, v81
MZ a,12,v,160,v,132
v_add_f32 v112, v156, v112
v_add_f32 v113, v156, v113
MZ a,32,v,164,v,132
v_exp_f32 v112, v112
MZ a,52,v,168,v,132
v_exp_f32 v113, v113
MZ a,72,v,172,v,132
v_add_f32 v114, v156, v114
v_add_f32 v115, v156, v115
MZ a,92,v,176,v,132
v_exp_f32 v114, v114
MZ a,112,v,180,v,132
v_exp_f32 v115, v115
MZ a,132,v,184,v,132
v_add_f32 v116, v156, v116
v_add_f32 v117, v156, v117
MZ a,152,v,188,v,132
v_exp_f32 v116, v116
MZ v,224,v,216,v,132
v_exp_f32 v117, v117
MC v,88,v,200,v,4,144
v_add_f32 v118, v156, v118
v_exp_f32 v118, v118
MA v,88,v,204,v,24
s_memrealtime s[98:99]
v_add_f32 v119, v156, v119
v_exp_f32 v119, v119
MA v,88,v,208,v,44
v_exp_f32 v82, v82
MA v,88,v,212,v,64
v_exp_f32 v83, v83
MC v,92,a,252,v,64,144
s_waitcnt lgkmcnt(0)
s_sub_i32 s98, s98, s76
MA v,92,a,248,v,44
s_cmp_lt_i32 s98, 0
s_cbranch_scc1 PACE1_1%=
PACED1_1%=:
CV 136,112,113
CV 137,114,115
CV 138,116,117
CV 139,118,119
MA v,92,a,244,v,24
v_exp_f32 v84, v84
MA v,92,a,240,v,4
v_exp_f32 v85, v85
MZ a,16,v,160,v,136
s_waitcnt vmcnt(16)
s_barrier
MZ a,36,v,164,v,136
TR v,160,239,16384
CV 120,80,81
MZ a,56,v,168,v,136
TR v,162,239,20480
CV 121,82,83
MZ a,76,v,172,v,136
TR v,164,240,16384
CV 122,84,85
MZ a,96,v,176,v,136
TR v,166,240,20480
s_add_u32 s76, s76, s29
MZ a,116,v,180,v,136
TR v,168,239,16896
MZ a,136,v,184,v,136
TR v,170,239,20992
MZ a,156,v,188,v,136
TR v,172,240,16896
MZ v,228,v,216,v,136
TR v,174,240,20992
MC v,96,v,200,v,8,148
TR v,176,239,17408
v_exp_f32 v86, v86
MA v,96,v,204,v,28
TR v,178,239,21504
v_exp_f32 v87, v87
MA v,96,v,208,v,48
TR v,180,240,17408
CV 123,86,87
MA v,96,v,212,v,68
TR v,182,240,21504
s_add_u32 s8, s28, 1
MC v,100,a,252,v,68,148
TR v,184,239,17920
s_and_b32 s8, s8, 7
MA v,100,a,248,v,48
TR v,186,239,22016
s_waitcnt lgkmcnt(6)
MA v,100,a,244,v,28
TR v,188,240,17920
s_lshl_b32 s8, s8, 7
MA v,100,a,240,v,8
TR v,190,240,22016
s_add_u32 s9, s28, 2
MA a,0,v,160,v,120
v_exp_f32 v88, v88
MA a,20,v,164,v,120
v_exp_f32 v89, v89
MA a,40,v,168,v,120
v_exp_f32 v90, v90
MA a,60,v,172,v,120
s_waitcnt lgkmcnt(0)
CV 124,88,89
MA a,80,v,176,v,120
v_exp_f32 v91, v91
MA a,100,v,180,v,120
v_exp_f32 v92, v92
MA a,120,v,184,v,120
v_exp_f32 v93, v93
MA a,140,v,188,v,120
v_exp_f32 v94, v94
MA v,192,v,216,v,120
s_add_u32 m0, s64, 16384
s_mov_b32 s67, 303104
MC v,104,v,200,v,12,152
v_exp_f32 v95, v95
MA v,104,v,204,v,32
v_exp_f32 v96, v96
MA v,104,v,208,v,52
v_exp_f32 v97, v97
MA v,104,v,212,v,72
buffer_load_dwordx4 v241, s[40:43], s67 offen offset:0 nt lds
MC v,108,a,252,v,72,152
CV 125,90,91
CV 126,92,93
MA v,108,a,248,v,52
CV 127,94,95
CV 128,96,97
MA v,108,a,244,v,32
v_exp_f32 v98, v98
MA v,108,a,240,v,12
buffer_load_dwordx4 v242, s[40:43], s67 offen offset:1024 nt lds
MA a,4,v,160,v,124
buffer_load_dwordx4 v241, s[40:43], s67 offen offset:2048 nt lds
MA a,24,v,164,v,124
buffer_load_dwordx4 v242, s[40:43], s67 offen offset:3072 nt lds
MA a,44,v,168,v,124
s_add_u32 m0, s64, 65536
s_mov_b32 s68, 294912
MA a,64,v,172,v,124
v_exp_f32 v99, v99
MA a,84,v,176,v,124
v_exp_f32 v100, v100
MA a,104,v,180,v,124
buffer_load_dwordx4 v241, s[44:47], s68 offen offset:0 nt lds
MA a,124,v,184,v,124
buffer_load_dwordx4 v242, s[44:47], s68 offen offset:1024 nt lds
MA a,144,v,188,v,124
buffer_load_dwordx4 v241, s[44:47], s68 offen offset:2048 nt lds
MA v,196,v,216,v,124
buffer_load_dwordx4 v242, s[44:47], s68 offen offset:3072 nt lds
v_exp_f32 v101, v101
MC v,112,v,200,v,16,156
R4 v,200,238,32768
v_exp_f32 v102, v102
MA v,112,v,204,v,36
R4 v,204,238,33280
v_exp_f32 v103, v103
MA v,112,v,208,v,56
R4 v,208,238,33792
CV 129,98,99
MA v,112,v,212,v,76
R4 v,212,238,34304
CV 130,100,101
MC v,116,a,252,v,76,156
R4 a,252,238,38400
CV 131,102,103
MA v,116,a,248,v,56
R4 a,248,238,37888
s_and_b32 s9, s9, 7
MA v,116,a,244,v,36
R4 a,244,238,37376
s_lshl_b32 s9, s9, 7
MA v,116,a,240,v,16
R4 a,240,238,36864
s_add_u32 s10, s28, 3
MA a,8,v,160,v,128
v_exp_f32 v104, v104
MA a,28,v,164,v,128
v_exp_f32 v105, v105
MA a,48,v,168,v,128
v_exp_f32 v106, v106
MA a,68,v,172,v,128
v_exp_f32 v107, v107
MA a,88,v,176,v,128
v_exp_f32 v108, v108
MA a,108,v,180,v,128
v_exp_f32 v109, v109
MA a,128,v,184,v,128
v_exp_f32 v110, v110
MA a,148,v,188,v,128
v_exp_f32 v111, v111
MA v,220,v,216,v,128
s_waitcnt lgkmcnt(0)
CV 132,104,105
MC v,80,v,200,v,0,140
v_exp_f32 v112, v112
MA v,80,v,204,v,20
v_exp_f32 v113, v113
MA v,80,v,208,v,40
v_exp_f32 v114, v114
MA v,80,v,212,v,60
v_exp_f32 v115, v115
MC v,84,a,252,v,60,140
CV 133,106,107
CV 134,108,109
MA v,84,a,248,v,40
CV 135,110,111
CV 136,112,113
MA v,84,a,244,v,20
v_exp_f32 v116, v116
MA v,84,a,240,v,0
v_exp_f32 v117, v117
MA a,12,v,160,v,132
v_exp_f32 v118, v118
MA a,32,v,164,v,132
v_exp_f32 v119, v119
MA a,52,v,168,v,132
v_exp_f32 v80, v80
MA a,72,v,172,v,132
v_exp_f32 v81, v81
MA a,92,v,176,v,132
v_exp_f32 v82, v82
MA a,112,v,180,v,132
v_exp_f32 v83, v83
MA a,132,v,184,v,132
v_exp_f32 v84, v84
MA a,152,v,188,v,132
v_exp_f32 v85, v85
MA v,224,v,216,v,132
v_exp_f32 v86, v86
MC v,88,v,200,v,4,144
v_exp_f32 v87, v87
MA v,88,v,204,v,24
s_memrealtime s[98:99]
CV 137,114,115
MA v,88,v,208,v,44
CV 138,116,117
CV 139,118,119
MA v,88,v,212,v,64
CV 120,80,81
CV 121,82,83
MC v,92,a,252,v,64,144
s_waitcnt lgkmcnt(0)
s_sub_i32 s98, s98, s76
MA v,92,a,248,v,44
s_cmp_lt_i32 s98, 0
s_cbranch_scc1 PACE1_2%=
PACED1_2%=:
MA v,92,a,244,v,24
v_exp_f32 v88, v88
MA v,92,a,240,v,4
v_exp_f32 v89, v89
MA a,16,v,160,v,136
s_waitcnt vmcnt(16)
s_barrier
MA a,36,v,164,v,136
TR v,160,239,32768
CV 122,84,85
MA a,56,v,168,v,136
TR v,162,239,36864
CV 123,86,87
MA a,76,v,172,v,136
TR v,164,240,32768
s_add_u32 s76, s76, s29
MA a,96,v,176,v,136
TR v,166,240,36864
CV 124,88,89
MA a,116,v,180,v,136
TR v,168,239,33280
s_and_b32 s10, s10, 7
MA a,136,v,184,v,136
TR v,170,239,37376
s_lshl_b32 s10, s10, 7
MA a,156,v,188,v,136
TR v,172,240,33280
s_add_u32 s11, s28, 4
MA v,228,v,216,v,136
TR v,174,240,37376
s_and_b32 s11, s11, 7
MC v,96,v,200,v,8,148
TR v,176,239,33792
s_lshl_b32 s11, s11, 7
MA v,96,v,204,v,28
TR v,178,239,37888
s_add_u32 s12, s28, 5
MA v,96,v,208,v,48
TR v,180,240,33792
s_and_b32 s12, s12, 7
MA v,96,v,212,v,68
TR v,182,240,37888
s_lshl_b32 s12, s12, 7
MC v,100,a,252,v,68,148
TR v,184,239,34304
s_add_u32 s13, s28, 6
MA v,100,a,248,v,48
TR v,186,239,38400
s_waitcnt lgkmcnt(6)
MA v,100,a,244,v,28
TR v,188,240,34304
s_and_b32 s13, s13, 7
MA v,100,a,240,v,8
TR v,190,240,38400
s_lshl_b32 s13, s13, 7
MA a,0,v,160,v,120
v_exp_f32 v90, v90
MA a,20,v,164,v,120
v_exp_f32 v91, v91
MA a,40,v,168,v,120
v_exp_f32 v92, v92
MA a,60,v,172,v,120
s_waitcnt lgkmcnt(0)
CV 125,90,91
MA a,80,v,176,v,120
v_exp_f32 v93, v93
MA a,100,v,180,v,120
v_exp_f32 v94, v94
MA a,120,v,184,v,120
v_exp_f32 v95, v95
MA a,140,v,188,v,120
v_exp_f32 v96, v96
MA v,192,v,216,v,120
s_add_u32 m0, s64, 32768
s_mov_b32 s69, 311296
MC v,104,v,200,v,12,152
v_exp_f32 v97, v97
MA v,104,v,204,v,32
v_exp_f32 v98, v98
MA v,104,v,208,v,52
v_exp_f32 v99, v99
)ASM"
R"ASM(MA v,104,v,212,v,72
buffer_load_dwordx4 v241, s[40:43], s69 offen offset:0 nt lds
MC v,108,a,252,v,72,152
CV 126,92,93
CV 127,94,95
MA v,108,a,248,v,52
v_exp_f32 v100, v100
MA v,108,a,244,v,32
v_exp_f32 v101, v101
MA v,108,a,240,v,12
buffer_load_dwordx4 v242, s[40:43], s69 offen offset:1024 nt lds
MA a,4,v,160,v,124
buffer_load_dwordx4 v241, s[40:43], s69 offen offset:2048 nt lds
MA a,24,v,164,v,124
buffer_load_dwordx4 v242, s[40:43], s69 offen offset:3072 nt lds
MA a,44,v,168,v,124
s_add_u32 m0, s64, 81920
s_mov_b32 s70, 303104
MA a,64,v,172,v,124
v_exp_f32 v102, v102
MA a,84,v,176,v,124
v_exp_f32 v103, v103
MA a,104,v,180,v,124
buffer_load_dwordx4 v241, s[44:47], s70 offen offset:0 nt lds
MA a,124,v,184,v,124
buffer_load_dwordx4 v242, s[44:47], s70 offen offset:1024 nt lds
MA a,144,v,188,v,124
buffer_load_dwordx4 v241, s[44:47], s70 offen offset:2048 nt lds
MA v,196,v,216,v,124
buffer_load_dwordx4 v242, s[44:47], s70 offen offset:3072 nt lds
MC v,112,v,200,v,16,156
R4 v,200,238,49152
CV 128,96,97
MA v,112,v,204,v,36
R4 v,204,238,49664
CV 129,98,99
MA v,112,v,208,v,56
R4 v,208,238,50176
CV 130,100,101
MA v,112,v,212,v,76
R4 v,212,238,50688
CV 131,102,103
MC v,116,a,252,v,76,156
R4 a,252,238,54784
s_add_u32 s14, s28, 7
MA v,116,a,248,v,56
R4 a,248,238,54272
s_and_b32 s14, s14, 7
MA v,116,a,244,v,36
R4 a,244,238,53760
s_lshl_b32 s14, s14, 7
MA v,116,a,240,v,16
R4 a,240,238,53248
s_add_u32 s15, s28, 8
MA a,8,v,160,v,128
v_exp_f32 v104, v104
MA a,28,v,164,v,128
v_exp_f32 v105, v105
MA a,48,v,168,v,128
v_exp_f32 v106, v106
MA a,68,v,172,v,128
v_exp_f32 v107, v107
MA a,88,v,176,v,128
v_exp_f32 v108, v108
MA a,108,v,180,v,128
v_exp_f32 v109, v109
MA a,128,v,184,v,128
v_exp_f32 v110, v110
MA a,148,v,188,v,128
v_exp_f32 v111, v111
MA v,220,v,216,v,128
s_waitcnt lgkmcnt(0)
CV 132,104,105
MC v,80,v,200,v,0,140
v_exp_f32 v112, v112
MA v,80,v,204,v,20
v_exp_f32 v113, v113
MA v,80,v,208,v,40
v_exp_f32 v114, v114
MA v,80,v,212,v,60
v_exp_f32 v115, v115
MC v,84,a,252,v,60,140
CV 133,106,107
CV 134,108,109
MA v,84,a,248,v,40
CV 135,110,111
CV 136,112,113
MA v,84,a,244,v,20
v_exp_f32 v116, v116
MA v,84,a,240,v,0
v_exp_f32 v117, v117
MA a,12,v,160,v,132
v_exp_f32 v118, v118
MA a,32,v,164,v,132
v_exp_f32 v119, v119
MA a,52,v,168,v,132
v_exp_f32 v80, v80
MA a,72,v,172,v,132
v_exp_f32 v81, v81
MA a,92,v,176,v,132
v_exp_f32 v82, v82
MA a,112,v,180,v,132
v_exp_f32 v83, v83
MA a,132,v,184,v,132
v_exp_f32 v84, v84
MA a,152,v,188,v,132
v_exp_f32 v85, v85
MA v,224,v,216,v,132
v_exp_f32 v86, v86
MC v,88,v,200,v,4,144
v_exp_f32 v87, v87
MA v,88,v,204,v,24
s_memrealtime s[98:99]
CV 137,114,115
MA v,88,v,208,v,44
CV 138,116,117
CV 139,118,119
MA v,88,v,212,v,64
CV 120,80,81
CV 121,82,83
MC v,92,a,252,v,64,144
s_waitcnt lgkmcnt(0)
s_sub_i32 s98, s98, s76
MA v,92,a,248,v,44
s_cmp_lt_i32 s98, 0
s_cbranch_scc1 PACE1_3%=
PACED1_3%=:
MA v,92,a,244,v,24
v_exp_f32 v88, v88
MA v,92,a,240,v,4
v_exp_f32 v89, v89
MA a,16,v,160,v,136
s_waitcnt vmcnt(16)
s_barrier
MA a,36,v,164,v,136
TR v,160,239,49152
CV 122,84,85
MA a,56,v,168,v,136
TR v,162,239,53248
CV 123,86,87
MA a,76,v,172,v,136
TR v,164,240,49152
s_add_u32 s76, s76, s29
MA a,96,v,176,v,136
TR v,166,240,53248
CV 124,88,89
MA a,116,v,180,v,136
TR v,168,239,49664
s_and_b32 s15, s15, 7
MA a,136,v,184,v,136
TR v,170,239,53760
s_lshl_b32 s15, s15, 7
MA a,156,v,188,v,136
TR v,172,240,49664
s_lshr_b32 s31, s64, 12
MA v,228,v,216,v,136
TR v,174,240,53760
s_lshl2_add_u32 s20, s28, s31
MC v,96,v,200,v,8,148
TR v,176,239,50176
s_lshl_b32 s23, s20, 6
MA v,96,v,204,v,28
TR v,178,239,54272
s_mul_i32 s20, s20, 5120
MA v,96,v,208,v,48
TR v,180,240,50176
s_lshl3_add_u32 s21, s31, s28
MA v,96,v,212,v,68
TR v,182,240,54272
s_lshl_b32 s21, s21, 3
MC v,100,a,252,v,68,148
TR v,184,239,50688
s_lshl_b32 s22, s28, 5
MA v,100,a,248,v,48
TR v,186,239,54784
s_waitcnt lgkmcnt(6)
MA v,100,a,244,v,28
TR v,188,240,50688
s_add_u32 s22, s22, s31
MA v,100,a,240,v,8
TR v,190,240,54784
s_mul_i32 s22, s22, 5120
MA a,0,v,160,v,120
v_exp_f32 v90, v90
MA a,20,v,164,v,120
v_exp_f32 v91, v91
MA a,40,v,168,v,120
v_exp_f32 v92, v92
MA a,60,v,172,v,120
s_waitcnt lgkmcnt(0)
CV 125,90,91
MA a,80,v,176,v,120
v_exp_f32 v93, v93
MA a,100,v,180,v,120
s_mov_b32 m0, 161792
s_min_u32 s94, s30, 2
MA a,120,v,184,v,120
s_sub_u32 s30, s30, s94
s_mov_b32 s69, 65536
MA a,140,v,188,v,120
v_exp_f32 v94, v94
MA v,192,v,216,v,120
v_exp_f32 v95, v95
MC v,104,v,200,v,12,152
buffer_load_dword v255, s[92:95], s69 idxen lds
MA v,104,v,204,v,32
s_add_u32 m0, s64, 49152
s_mov_b32 s67, 319488
MA v,104,v,208,v,52
v_exp_f32 v96, v96
MA v,104,v,212,v,72
v_exp_f32 v97, v97
MC v,108,a,252,v,72,152
buffer_load_dwordx4 v241, s[40:43], s67 offen offset:0 nt lds
MA v,108,a,248,v,52
CV 126,92,93
CV 127,94,95
MA v,108,a,244,v,32
v_exp_f32 v98, v98
MA v,108,a,240,v,12
v_exp_f32 v99, v99
MA a,4,v,160,v,124
buffer_load_dwordx4 v242, s[40:43], s67 offen offset:1024 nt lds
MA a,24,v,164,v,124
v_exp_f32 v100, v100
MA a,44,v,168,v,124
v_exp_f32 v101, v101
MA a,64,v,172,v,124
v_exp_f32 v102, v102
MA a,84,v,176,v,124
buffer_load_dwordx4 v241, s[40:43], s67 offen offset:2048 nt lds
MA a,104,v,180,v,124
v_exp_f32 v103, v103
MA a,124,v,184,v,124
v_exp_f32 v104, v104
MA a,144,v,188,v,124
v_exp_f32 v105, v105
MA v,196,v,216,v,124
buffer_load_dwordx4 v242, s[40:43], s67 offen offset:3072 nt lds
MC v,112,v,200,v,16,156
R4 v,200,238,0
CV 128,96,97
MA v,112,v,204,v,36
R4 v,204,238,512
CV 129,98,99
MA v,112,v,208,v,56
R4 v,208,238,1024
CV 130,100,101
MA v,112,v,212,v,76
R4 v,212,238,1536
CV 131,102,103
MC v,116,a,252,v,76,156
R4 a,252,238,5632
CV 132,104,105
MA v,116,a,248,v,56
R4 a,248,238,5120
v_lshlrev_b32 v249, 4, v255
MA v,116,a,244,v,36
R4 a,244,238,4608
v_add_u32 v250, 4096, v249
MA v,116,a,240,v,16
R4 a,240,238,4096
s_lshr_b32 s31, s64, 2
MA a,8,v,160,v,128
v_exp_f32 v106, v106
MA a,28,v,164,v,128
v_exp_f32 v107, v107
MA a,48,v,168,v,128
v_exp_f32 v108, v108
MA a,68,v,172,v,128
v_exp_f32 v109, v109
MA a,88,v,176,v,128
v_exp_f32 v110, v110
MA a,108,v,180,v,128
v_exp_f32 v111, v111
MA a,128,v,184,v,128
s_add_u32 m0, s64, 98304
s_mov_b32 s68, 311296
MA a,148,v,188,v,128
v_exp_f32 v112, v112
MA v,220,v,216,v,128
s_waitcnt lgkmcnt(0)
CV 133,106,107
MC v,80,v,200,v,0,140
v_exp_f32 v113, v113
MA v,80,v,204,v,20
buffer_load_dwordx4 v241, s[44:47], s68 offen offset:0 nt lds
MA v,80,v,208,v,40
v_exp_f32 v114, v114
MA v,80,v,212,v,60
v_exp_f32 v115, v115
MC v,84,a,252,v,60,140
CV 134,108,109
CV 135,110,111
MA v,84,a,248,v,40
buffer_load_dwordx4 v242, s[44:47], s68 offen offset:1024 nt lds
MA v,84,a,244,v,20
v_exp_f32 v116, v116
MA v,84,a,240,v,0
v_exp_f32 v117, v117
MA a,12,v,160,v,132
v_exp_f32 v118, v118
MA a,32,v,164,v,132
buffer_load_dwordx4 v241, s[44:47], s68 offen offset:2048 nt lds
MA a,52,v,168,v,132
v_exp_f32 v119, v119
MA a,72,v,172,v,132
v_exp_f32 v80, v80
MA a,92,v,176,v,132
v_exp_f32 v81, v81
MA a,112,v,180,v,132
buffer_load_dwordx4 v242, s[44:47], s68 offen offset:3072 nt lds
MA a,132,v,184,v,132
v_exp_f32 v82, v82
MA a,152,v,188,v,132
v_exp_f32 v83, v83
MA v,224,v,216,v,132
v_exp_f32 v84, v84
MC v,88,v,200,v,4,144
v_exp_f32 v85, v85
MA v,88,v,204,v,24
s_memrealtime s[98:99]
CV 136,112,113
MA v,88,v,208,v,44
v_exp_f32 v86, v86
MA v,88,v,212,v,64
v_exp_f32 v87, v87
MC v,92,a,252,v,64,144
s_waitcnt lgkmcnt(0)
s_sub_i32 s98, s98, s76
MA v,92,a,248,v,44
s_cmp_lt_i32 s98, 0
s_cbranch_scc1 PACE1_4%=
PACED1_4%=:
CV 137,114,115
CV 138,116,117
CV 139,118,119
MA v,92,a,244,v,24
v_exp_f32 v88, v88
MA v,92,a,240,v,4
v_exp_f32 v89, v89
MA a,16,v,160,v,136
s_waitcnt vmcnt(17)
s_barrier
MA a,36,v,164,v,136
TR v,160,239,0
CV 120,80,81
MA a,56,v,168,v,136
TR v,162,239,4096
CV 121,82,83
MA a,76,v,172,v,136
TR v,164,240,0
CV 122,84,85
MA a,96,v,176,v,136
TR v,166,240,4096
CV 123,86,87
MA a,116,v,180,v,136
TR v,168,239,512
s_add_u32 s76, s76, s29
MA a,136,v,184,v,136
TR v,170,239,4608
CV 124,88,89
MA a,156,v,188,v,136
TR v,172,240,512
s_add_u32 s31, s31, 131072
MA v,228,v,216,v,136
TR v,174,240,4608
v_add_u32 v251, s31, v249
MC v,96,v,200,v,8,148
TR v,176,239,1024
v_xor_b32 v252, 2048, v251
MA v,96,v,204,v,28
TR v,178,239,5120
v_and_b32 v232, 0x1ff, v239
MA v,96,v,208,v,48
TR v,180,240,1024
v_lshrrev_b32 v233, 5, v255
MA v,96,v,212,v,68
TR v,182,240,5120
v_lshl_add_u32 v232, v233, 10, v232
MC v,100,a,252,v,68,148
TR v,184,239,1536
s_mul_i32 s31, s26, 16896
MA v,100,a,248,v,48
TR v,186,239,5632
s_waitcnt lgkmcnt(6)
MA v,100,a,244,v,28
TR v,188,240,1536
s_add_u32 s31, s31, 65536
MA v,100,a,240,v,8
TR v,190,240,5632
v_add_u32 v247, s31, v232
MA a,0,v,160,v,120
v_exp_f32 v90, v90
MA a,20,v,164,v,120
v_exp_f32 v91, v91
MA a,40,v,168,v,120
v_exp_f32 v92, v92
MA a,60,v,172,v,120
s_waitcnt lgkmcnt(0)
CV 125,90,91
MA a,80,v,176,v,120
v_exp_f32 v93, v93
MA a,100,v,180,v,120
v_exp_f32 v94, v94
MA a,120,v,184,v,120
v_exp_f32 v95, v95
MA a,140,v,188,v,120
v_exp_f32 v96, v96
MA v,192,v,216,v,120
s_add_u32 m0, s64, 0
s_mov_b32 s69, 327680
MC v,104,v,200,v,12,152
v_exp_f32 v97, v97
MA v,104,v,204,v,32
v_exp_f32 v98, v98
MA v,104,v,208,v,52
v_exp_f32 v99, v99
MA v,104,v,212,v,72
buffer_load_dwordx4 v241, s[40:43], s69 offen offset:0 nt lds
MC v,108,a,252,v,72,152
CV 126,92,93
CV 127,94,95
MA v,108,a,248,v,52
v_exp_f32 v100, v100
MA v,108,a,244,v,32
v_exp_f32 v101, v101
MA v,108,a,240,v,12
buffer_load_dwordx4 v242, s[40:43], s69 offen offset:1024 nt lds
MA a,4,v,160,v,124
v_exp_f32 v102, v102
MA a,24,v,164,v,124
v_exp_f32 v103, v103
MA a,44,v,168,v,124
v_exp_f32 v104, v104
MA a,64,v,172,v,124
buffer_load_dwordx4 v241, s[40:43], s69 offen offset:2048 nt lds
MA a,84,v,176,v,124
v_exp_f32 v105, v105
MA a,104,v,180,v,124
v_exp_f32 v106, v106
MA a,124,v,184,v,124
v_exp_f32 v107, v107
MA a,144,v,188,v,124
buffer_load_dwordx4 v242, s[40:43], s69 offen offset:3072 nt lds
MA v,196,v,216,v,124
v_exp_f32 v108, v108
MC v,112,v,200,v,16,156
R4 v,200,238,16384
CV 128,96,97
MA v,112,v,204,v,36
R4 v,204,238,16896
CV 129,98,99
MA v,112,v,208,v,56
R4 v,208,238,17408
CV 130,100,101
MA v,112,v,212,v,76
R4 v,212,238,17920
CV 131,102,103
MC v,116,a,252,v,76,156
R4 a,252,238,22016
v_xor_b32 v248, 32, v247
MA v,116,a,248,v,56
R4 a,248,238,21504
v_bfe_u32 v232, v255, 4, 1
MA v,116,a,244,v,36
R4 a,244,238,20992
v_lshl_or_b32 v232, v232, 1, v233
MA v,116,a,240,v,16
R4 a,240,238,20480
v_and_b32 v233, 15, v255
MA a,8,v,160,v,128
v_exp_f32 v109, v109
MA a,28,v,164,v,128
v_exp_f32 v110, v110
MA a,48,v,168,v,128
v_exp_f32 v111, v111
MA a,68,v,172,v,128
v_exp_f32 v112, v112
MA a,88,v,176,v,128
v_exp_f32 v113, v113
MA a,108,v,180,v,128
v_exp_f32 v114, v114
MA a,128,v,184,v,128
s_add_u32 m0, s64, 114688
s_mov_b32 s70, 319488
MA a,148,v,188,v,128
v_lshlrev_b32 v233, 11, v233
CV 132,104,105
MA v,220,v,216,v,128
s_waitcnt lgkmcnt(0)
v_lshl_add_u32 v253, v232, 4, v233
MC v,80,v,200,v,0,140
v_lshlrev_b32 v254, 24, v255
CV 133,106,107
MA v,80,v,204,v,20
buffer_load_dwordx4 v241, s[44:47], s70 offen offset:0 nt lds
MA v,80,v,208,v,40
ds_write_b128 v251, a[160:163] offset:0
CV 134,108,109
MA v,80,v,212,v,60
v_exp_f32 v115, v115
MC v,84,a,252,v,60,140
CV 135,110,111
CV 136,112,113
MA v,84,a,248,v,40
buffer_load_dwordx4 v242, s[44:47], s70 offen offset:1024 nt lds
MA v,84,a,244,v,20
v_exp_f32 v116, v116
MA v,84,a,240,v,0
v_exp_f32 v117, v117
MA a,12,v,160,v,132
v_exp_f32 v118, v118
MA a,32,v,164,v,132
buffer_load_dwordx4 v241, s[44:47], s70 offen offset:2048 nt lds
MA a,52,v,168,v,132
v_exp_f32 v119, v119
MA a,72,v,172,v,132
v_exp_f32 v80, v80
MA a,92,v,176,v,132
v_exp_f32 v81, v81
MA a,112,v,180,v,132
buffer_load_dwordx4 v242, s[44:47], s70 offen offset:3072 nt lds
MA a,132,v,184,v,132
v_exp_f32 v82, v82
MA a,152,v,188,v,132
v_exp_f32 v83, v83
MA v,224,v,216,v,132
v_exp_f32 v84, v84
MC v,88,v,200,v,4,144
v_exp_f32 v85, v85
MA v,88,v,204,v,24
s_memrealtime s[98:99]
CV 137,114,115
MA v,88,v,208,v,44
v_exp_f32 v86, v86
MA v,88,v,212,v,64
v_exp_f32 v87, v87
MC v,92,a,252,v,64,144
s_waitcnt lgkmcnt(0)
s_sub_i32 s98, s98, s76
MA v,92,a,248,v,44
s_cmp_lt_i32 s98, 0
s_cbranch_scc1 PACE1_5%=
PACED1_5%=:
CV 138,116,117
CV 139,118,119
MA v,92,a,244,v,24
)ASM"
R"ASM(v_exp_f32 v88, v88
MA v,92,a,240,v,4
v_exp_f32 v89, v89
MA a,16,v,160,v,136
s_waitcnt vmcnt(17)
s_barrier
MA a,36,v,164,v,136
TR v,160,239,16384
CV 120,80,81
MA a,56,v,168,v,136
TR v,162,239,20480
CV 121,82,83
MA a,76,v,172,v,136
TR v,164,240,16384
CV 122,84,85
MA a,96,v,176,v,136
TR v,166,240,20480
CV 123,86,87
MA a,116,v,180,v,136
TR v,168,239,16896
s_add_u32 s76, s76, s29
MA a,136,v,184,v,136
TR v,170,239,20992
CV 124,88,89
MA a,156,v,188,v,136
TR v,172,240,16896
MA v,228,v,216,v,136
TR v,174,240,20992
MC v,96,v,200,v,8,148
TR v,176,239,17408
MA v,96,v,204,v,28
TR v,178,239,21504
MA v,96,v,208,v,48
TR v,180,240,17408
MA v,96,v,212,v,68
TR v,182,240,21504
MC v,100,a,252,v,68,148
TR v,184,239,17920
MA v,100,a,248,v,48
TR v,186,239,22016
s_waitcnt lgkmcnt(6)
MA v,100,a,244,v,28
TR v,188,240,17920
MA v,100,a,240,v,8
TR v,190,240,22016
MA a,0,v,160,v,120
v_exp_f32 v90, v90
MA a,20,v,164,v,120
v_exp_f32 v91, v91
MA a,40,v,168,v,120
v_exp_f32 v92, v92
MA a,60,v,172,v,120
s_waitcnt lgkmcnt(0)
CV 125,90,91
MA a,80,v,176,v,120
v_exp_f32 v93, v93
MA a,100,v,180,v,120
v_exp_f32 v94, v94
MA a,120,v,184,v,120
v_exp_f32 v95, v95
MA a,140,v,188,v,120
v_exp_f32 v96, v96
MA v,192,v,216,v,120
s_add_u32 m0, s64, 16384
s_mov_b32 s67, 335872
MC v,104,v,200,v,12,152
v_exp_f32 v97, v97
MA v,104,v,204,v,32
v_exp_f32 v98, v98
MA v,104,v,208,v,52
v_exp_f32 v99, v99
MA v,104,v,212,v,72
buffer_load_dwordx4 v241, s[40:43], s67 offen offset:0 nt lds
MC v,108,a,252,v,72,152
CV 126,92,93
CV 127,94,95
MA v,108,a,248,v,52
v_exp_f32 v100, v100
MA v,108,a,244,v,32
v_exp_f32 v101, v101
MA v,108,a,240,v,12
buffer_load_dwordx4 v242, s[40:43], s67 offen offset:1024 nt lds
MA a,4,v,160,v,124
v_exp_f32 v102, v102
MA a,24,v,164,v,124
v_exp_f32 v103, v103
MA a,44,v,168,v,124
v_exp_f32 v104, v104
MA a,64,v,172,v,124
buffer_load_dwordx4 v241, s[40:43], s67 offen offset:2048 nt lds
MA a,84,v,176,v,124
v_exp_f32 v105, v105
MA a,104,v,180,v,124
v_exp_f32 v106, v106
MA a,124,v,184,v,124
v_exp_f32 v107, v107
MA a,144,v,188,v,124
buffer_load_dwordx4 v242, s[40:43], s67 offen offset:3072 nt lds
MA v,196,v,216,v,124
v_exp_f32 v108, v108
MC v,112,v,200,v,16,156
R4 v,200,238,32768
CV 128,96,97
MA v,112,v,204,v,36
R4 v,204,238,33280
CV 129,98,99
MA v,112,v,208,v,56
R4 v,208,238,33792
CV 130,100,101
MA v,112,v,212,v,76
R4 v,212,238,34304
CV 131,102,103
MC v,116,a,252,v,76,156
R4 a,252,238,38400
CV 132,104,105
MA v,116,a,248,v,56
R4 a,248,238,37888
CV 133,106,107
MA v,116,a,244,v,36
R4 a,244,238,37376
MA v,116,a,240,v,16
R4 a,240,238,36864
MA a,8,v,160,v,128
v_exp_f32 v109, v109
MA a,28,v,164,v,128
v_exp_f32 v110, v110
MA a,48,v,168,v,128
v_exp_f32 v111, v111
MA a,68,v,172,v,128
v_exp_f32 v112, v112
MA a,88,v,176,v,128
v_exp_f32 v113, v113
MA a,108,v,180,v,128
v_exp_f32 v114, v114
MA a,128,v,184,v,128
s_add_u32 m0, s64, 65536
s_mov_b32 s68, 327680
MA a,148,v,188,v,128
v_exp_f32 v115, v115
MA v,220,v,216,v,128
s_waitcnt lgkmcnt(0)
CV 134,108,109
MC v,80,v,200,v,0,140
ds_write_b128 v251, a[180:183] offset:4096
CV 135,110,111
MA v,80,v,204,v,20
buffer_load_dwordx4 v241, s[44:47], s68 offen offset:0 nt lds
MA v,80,v,208,v,40
v_exp_f32 v116, v116
MA v,80,v,212,v,60
v_exp_f32 v117, v117
MC v,84,a,252,v,60,140
v_exp_f32 v118, v118
MA v,84,a,248,v,40
buffer_load_dwordx4 v242, s[44:47], s68 offen offset:1024 nt lds
MA v,84,a,244,v,20
v_exp_f32 v119, v119
MA v,84,a,240,v,0
v_exp_f32 v80, v80
MA a,12,v,160,v,132
v_exp_f32 v81, v81
MA a,32,v,164,v,132
buffer_load_dwordx4 v241, s[44:47], s68 offen offset:2048 nt lds
MA a,52,v,168,v,132
v_exp_f32 v82, v82
MA a,72,v,172,v,132
v_exp_f32 v83, v83
MA a,92,v,176,v,132
v_exp_f32 v84, v84
MA a,112,v,180,v,132
buffer_load_dwordx4 v242, s[44:47], s68 offen offset:3072 nt lds
MA a,132,v,184,v,132
v_exp_f32 v85, v85
MA a,152,v,188,v,132
v_exp_f32 v86, v86
MA v,224,v,216,v,132
v_exp_f32 v87, v87
MC v,88,v,200,v,4,144
CV 136,112,113
CV 137,114,115
MA v,88,v,204,v,24
s_memrealtime s[98:99]
CV 138,116,117
MA v,88,v,208,v,44
CV 139,118,119
CV 120,80,81
MA v,88,v,212,v,64
CV 121,82,83
CV 122,84,85
MC v,92,a,252,v,64,144
s_waitcnt lgkmcnt(0)
s_sub_i32 s98, s98, s76
MA v,92,a,248,v,44
s_cmp_lt_i32 s98, 0
s_cbranch_scc1 PACE1_6%=
PACED1_6%=:
MA v,92,a,244,v,24
v_exp_f32 v88, v88
MA v,92,a,240,v,4
v_exp_f32 v89, v89
MA a,16,v,160,v,136
s_waitcnt vmcnt(16)
s_barrier
MA a,36,v,164,v,136
TR v,160,239,32768
CV 123,86,87
MA a,56,v,168,v,136
TR v,162,239,36864
s_add_u32 s76, s76, s29
MA a,76,v,172,v,136
TR v,164,240,32768
CV 124,88,89
MA a,96,v,176,v,136
TR v,166,240,36864
MA a,116,v,180,v,136
TR v,168,239,33280
MA a,136,v,184,v,136
TR v,170,239,37376
MA a,156,v,188,v,136
TR v,172,240,33280
MA v,228,v,216,v,136
TR v,174,240,37376
MC v,96,v,200,v,8,148
TR v,176,239,33792
MA v,96,v,204,v,28
TR v,178,239,37888
MA v,96,v,208,v,48
TR v,180,240,33792
MA v,96,v,212,v,68
TR v,182,240,37888
MC v,100,a,252,v,68,148
TR v,184,239,34304
MA v,100,a,248,v,48
TR v,186,239,38400
s_waitcnt lgkmcnt(6)
MA v,100,a,244,v,28
TR v,188,240,34304
MA v,100,a,240,v,8
TR v,190,240,38400
MA a,0,v,160,v,120
v_exp_f32 v90, v90
MA a,20,v,164,v,120
v_exp_f32 v91, v91
MA a,40,v,168,v,120
v_exp_f32 v92, v92
MA a,60,v,172,v,120
s_waitcnt lgkmcnt(0)
CV 125,90,91
MA a,80,v,176,v,120
v_exp_f32 v93, v93
MA a,100,v,180,v,120
v_exp_f32 v94, v94
MA a,120,v,184,v,120
v_exp_f32 v95, v95
MA a,140,v,188,v,120
v_exp_f32 v96, v96
MA v,192,v,216,v,120
s_add_u32 m0, s64, 32768
s_mov_b32 s69, 344064
MC v,104,v,200,v,12,152
v_exp_f32 v97, v97
MA v,104,v,204,v,32
v_exp_f32 v98, v98
MA v,104,v,208,v,52
v_exp_f32 v99, v99
MA v,104,v,212,v,72
buffer_load_dwordx4 v241, s[40:43], s69 offen offset:0 nt lds
MC v,108,a,252,v,72,152
CV 126,92,93
CV 127,94,95
MA v,108,a,248,v,52
v_exp_f32 v100, v100
MA v,108,a,244,v,32
v_exp_f32 v101, v101
MA v,108,a,240,v,12
buffer_load_dwordx4 v242, s[40:43], s69 offen offset:1024 nt lds
MA a,4,v,160,v,124
v_exp_f32 v102, v102
MA a,24,v,164,v,124
v_exp_f32 v103, v103
MA a,44,v,168,v,124
v_exp_f32 v104, v104
MA a,64,v,172,v,124
buffer_load_dwordx4 v241, s[40:43], s69 offen offset:2048 nt lds
MA a,84,v,176,v,124
v_exp_f32 v105, v105
MA a,104,v,180,v,124
v_exp_f32 v106, v106
MA a,124,v,184,v,124
v_exp_f32 v107, v107
MA a,144,v,188,v,124
buffer_load_dwordx4 v242, s[40:43], s69 offen offset:3072 nt lds
MA v,196,v,216,v,124
v_exp_f32 v108, v108
MC v,112,v,200,v,16,156
R4 v,200,238,49152
CV 128,96,97
MA v,112,v,204,v,36
R4 v,204,238,49664
CV 129,98,99
MA v,112,v,208,v,56
R4 v,208,238,50176
CV 130,100,101
MA v,112,v,212,v,76
R4 v,212,238,50688
CV 131,102,103
MC v,116,a,252,v,76,156
R4 a,252,238,54784
CV 132,104,105
MA v,116,a,248,v,56
R4 a,248,238,54272
CV 133,106,107
MA v,116,a,244,v,36
R4 a,244,238,53760
MA v,116,a,240,v,16
R4 a,240,238,53248
MA a,8,v,160,v,128
v_exp_f32 v109, v109
MA a,28,v,164,v,128
v_exp_f32 v110, v110
MA a,48,v,168,v,128
v_exp_f32 v111, v111
MA a,68,v,172,v,128
v_exp_f32 v112, v112
MA a,88,v,176,v,128
v_exp_f32 v113, v113
MA a,108,v,180,v,128
v_exp_f32 v114, v114
MA a,128,v,184,v,128
v_exp_f32 v115, v115
MA a,148,v,188,v,128
v_exp_f32 v116, v116
MA v,220,v,216,v,128
s_waitcnt lgkmcnt(0)
CV 134,108,109
MC v,80,v,200,v,0,140
ds_write_b128 v251, a[200:203] offset:8192
CV 135,110,111
MA v,80,v,204,v,20
v_exp_f32 v117, v117
MA v,80,v,208,v,40
v_exp_f32 v118, v118
MA v,80,v,212,v,60
v_exp_f32 v119, v119
MC v,84,a,252,v,60,140
CV 136,112,113
CV 137,114,115
MA v,84,a,248,v,40
CV 138,116,117
CV 139,118,119
MA v,84,a,244,v,20
v_exp_f32 v80, v80
MA v,84,a,240,v,0
v_exp_f32 v81, v81
MA a,12,v,160,v,132
v_exp_f32 v82, v82
MA a,32,v,164,v,132
v_exp_f32 v83, v83
MA a,52,v,168,v,132
v_exp_f32 v84, v84
MA a,72,v,172,v,132
v_exp_f32 v85, v85
MA a,92,v,176,v,132
v_exp_f32 v86, v86
MA a,112,v,180,v,132
v_exp_f32 v87, v87
MA a,132,v,184,v,132
CV 120,80,81
CV 121,82,83
MA a,152,v,188,v,132
CV 122,84,85
CV 123,86,87
MA v,224,v,216,v,132
MC v,88,v,200,v,4,144
MA v,88,v,204,v,24
s_memrealtime s[98:99]
MA v,88,v,208,v,44
MA v,88,v,212,v,64
MC v,92,a,252,v,64,144
s_waitcnt lgkmcnt(0)
s_sub_i32 s98, s98, s76
MA v,92,a,248,v,44
s_cmp_lt_i32 s98, 0
s_cbranch_scc1 PACE1_7%=
PACED1_7%=:
MA v,92,a,244,v,24
v_exp_f32 v88, v88
MA v,92,a,240,v,4
v_exp_f32 v89, v89
MA a,16,v,160,v,136
s_waitcnt vmcnt(12)
s_barrier
MA a,36,v,164,v,136
TR v,160,239,49152
s_add_u32 s76, s76, s29
MA a,56,v,168,v,136
TR v,162,239,53248
CV 124,88,89
MA a,76,v,172,v,136
TR v,164,240,49152
MA a,96,v,176,v,136
TR v,166,240,53248
MA a,116,v,180,v,136
TR v,168,239,49664
MA a,136,v,184,v,136
TR v,170,239,53760
MA a,156,v,188,v,136
TR v,172,240,49664
MA v,228,v,216,v,136
TR v,174,240,53760
MC v,96,v,200,v,8,148
TR v,176,239,50176
MA v,96,v,204,v,28
TR v,178,239,54272
MA v,96,v,208,v,48
TR v,180,240,50176
MA v,96,v,212,v,68
TR v,182,240,54272
MC v,100,a,252,v,68,148
TR v,184,239,50688
MA v,100,a,248,v,48
TR v,186,239,54784
s_waitcnt lgkmcnt(6)
MA v,100,a,244,v,28
TR v,188,240,50688
MA v,100,a,240,v,8
TR v,190,240,54784
MA a,0,v,160,v,120
v_exp_f32 v90, v90
MA a,20,v,164,v,120
v_exp_f32 v91, v91
MA a,40,v,168,v,120
v_exp_f32 v92, v92
MA a,60,v,172,v,120
s_waitcnt lgkmcnt(0)
CV 125,90,91
MA a,80,v,176,v,120
v_exp_f32 v93, v93
MA a,100,v,180,v,120
v_exp_f32 v94, v94
MA a,120,v,184,v,120
v_exp_f32 v95, v95
MA a,140,v,188,v,120
v_exp_f32 v96, v96
MA v,192,v,216,v,120
s_add_u32 m0, s64, 81920
s_mov_b32 s70, 335872
MC v,104,v,200,v,12,152
v_exp_f32 v97, v97
MA v,104,v,204,v,32
v_exp_f32 v98, v98
MA v,104,v,208,v,52
v_exp_f32 v99, v99
MA v,104,v,212,v,72
buffer_load_dwordx4 v241, s[44:47], s70 offen offset:0 nt lds
MC v,108,a,252,v,72,152
CV 126,92,93
CV 127,94,95
MA v,108,a,248,v,52
v_exp_f32 v100, v100
MA v,108,a,244,v,32
v_exp_f32 v101, v101
MA v,108,a,240,v,12
buffer_load_dwordx4 v242, s[44:47], s70 offen offset:1024 nt lds
MA a,4,v,160,v,124
v_exp_f32 v102, v102
MA a,24,v,164,v,124
v_exp_f32 v103, v103
MA a,44,v,168,v,124
v_exp_f32 v104, v104
MA a,64,v,172,v,124
buffer_load_dwordx4 v241, s[44:47], s70 offen offset:2048 nt lds
MA a,84,v,176,v,124
v_exp_f32 v105, v105
MA a,104,v,180,v,124
v_exp_f32 v106, v106
MA a,124,v,184,v,124
v_exp_f32 v107, v107
MA a,144,v,188,v,124
buffer_load_dwordx4 v242, s[44:47], s70 offen offset:3072 nt lds
MA v,196,v,216,v,124
v_exp_f32 v108, v108
MC v,112,v,200,v,16,156
R4 v,200,238,0
CV 128,96,97
MA v,112,v,204,v,36
R4 v,204,238,512
CV 129,98,99
MA v,112,v,208,v,56
R4 v,208,238,1024
CV 130,100,101
MA v,112,v,212,v,76
R4 v,212,238,1536
CV 131,102,103
MC v,116,a,252,v,76,156
R4 a,252,238,5632
CV 132,104,105
MA v,116,a,248,v,56
R4 a,248,238,5120
CV 133,106,107
MA v,116,a,244,v,36
R4 a,244,238,4608
MA v,116,a,240,v,16
R4 a,240,238,4096
MA a,8,v,160,v,128
v_exp_f32 v109, v109
MA a,28,v,164,v,128
v_exp_f32 v110, v110
MA a,48,v,168,v,128
v_exp_f32 v111, v111
MA a,68,v,172,v,128
v_exp_f32 v112, v112
MA a,88,v,176,v,128
v_exp_f32 v113, v113
MA a,108,v,180,v,128
v_exp_f32 v114, v114
MA a,128,v,184,v,128
s_add_u32 m0, s64, 49152
s_mov_b32 s67, 352256
MA a,148,v,188,v,128
v_exp_f32 v115, v115
MA v,220,v,216,v,128
s_waitcnt lgkmcnt(0)
CV 134,108,109
MC v,80,v,200,v,0,140
ds_write_b128 v251, a[220:223] offset:12288
CV 135,110,111
MA v,80,v,204,v,20
buffer_load_dwordx4 v241, s[40:43], s67 offen offset:0 nt lds
MA v,80,v,208,v,40
v_exp_f32 v116, v116
MA v,80,v,212,v,60
v_exp_f32 v117, v117
MC v,84,a,252,v,60,140
v_exp_f32 v118, v118
MA v,84,a,248,v,40
buffer_load_dwordx4 v242, s[40:43], s67 offen offset:1024 nt lds
MA v,84,a,244,v,20
v_exp_f32 v119, v119
MA v,84,a,240,v,0
v_exp_f32 v80, v80
MA a,12,v,160,v,132
v_exp_f32 v81, v81
MA a,32,v,164,v,132
buffer_load_dwordx4 v241, s[40:43], s67 offen offset:2048 nt lds
MA a,52,v,168,v,132
v_exp_f32 v82, v82
MA a,72,v,172,v,132
v_exp_f32 v83, v83
MA a,92,v,176,v,132
v_exp_f32 v84, v84
MA a,112,v,180,v,132
buffer_load_dwordx4 v242, s[40:43], s67 offen offset:3072 nt lds
MA a,132,v,184,v,132
v_exp_f32 v85, v85
MA a,152,v,188,v,132
v_exp_f32 v86, v86
MA v,224,v,216,v,132
v_exp_f32 v87, v87
MC v,88,v,200,v,4,144
CV 136,112,113
CV 137,114,115
MA v,88,v,204,v,24
s_memrealtime s[98:99]
CV 138,116,117
MA v,88,v,208,v,44
CV 139,118,119
CV 120,80,81
MA v,88,v,212,v,64
)ASM"
R"ASM(CV 121,82,83
CV 122,84,85
MC v,92,a,252,v,64,144
s_waitcnt lgkmcnt(0)
s_sub_i32 s98, s98, s76
MA v,92,a,248,v,44
s_cmp_lt_i32 s98, 0
s_cbranch_scc1 PACE1_8%=
PACED1_8%=:
MA v,92,a,244,v,24
v_exp_f32 v88, v88
MA v,92,a,240,v,4
v_exp_f32 v89, v89
MA a,16,v,160,v,136
s_waitcnt vmcnt(12)
s_barrier
MA a,36,v,164,v,136
TR v,160,239,0
CV 123,86,87
MA a,56,v,168,v,136
TR v,162,239,4096
s_add_u32 s76, s76, s29
MA a,76,v,172,v,136
TR v,164,240,0
CV 124,88,89
MA a,96,v,176,v,136
TR v,166,240,4096
MA a,116,v,180,v,136
TR v,168,239,512
MA a,136,v,184,v,136
TR v,170,239,4608
MA a,156,v,188,v,136
TR v,172,240,512
MA v,228,v,216,v,136
TR v,174,240,4608
MC v,96,v,200,v,8,148
TR v,176,239,1024
MA v,96,v,204,v,28
TR v,178,239,5120
MA v,96,v,208,v,48
TR v,180,240,1024
MA v,96,v,212,v,68
TR v,182,240,5120
MC v,100,a,252,v,68,148
TR v,184,239,1536
MA v,100,a,248,v,48
TR v,186,239,5632
s_waitcnt lgkmcnt(6)
MA v,100,a,244,v,28
TR v,188,240,1536
MA v,100,a,240,v,8
TR v,190,240,5632
MA a,0,v,160,v,120
v_exp_f32 v90, v90
MA a,20,v,164,v,120
v_exp_f32 v91, v91
MA a,40,v,168,v,120
v_exp_f32 v92, v92
MA a,60,v,172,v,120
s_waitcnt lgkmcnt(0)
CV 125,90,91
MA a,80,v,176,v,120
v_exp_f32 v93, v93
MA a,100,v,180,v,120
v_exp_f32 v94, v94
MA a,120,v,184,v,120
v_exp_f32 v95, v95
MA a,140,v,188,v,120
v_exp_f32 v96, v96
MA v,192,v,216,v,120
s_add_u32 m0, s64, 98304
s_mov_b32 s68, 344064
MC v,104,v,200,v,12,152
v_exp_f32 v97, v97
MA v,104,v,204,v,32
v_exp_f32 v98, v98
MA v,104,v,208,v,52
v_exp_f32 v99, v99
MA v,104,v,212,v,72
buffer_load_dwordx4 v241, s[44:47], s68 offen offset:0 nt lds
MC v,108,a,252,v,72,152
CV 126,92,93
CV 127,94,95
MA v,108,a,248,v,52
v_exp_f32 v100, v100
MA v,108,a,244,v,32
v_exp_f32 v101, v101
MA v,108,a,240,v,12
buffer_load_dwordx4 v242, s[44:47], s68 offen offset:1024 nt lds
MA a,4,v,160,v,124
v_exp_f32 v102, v102
MA a,24,v,164,v,124
v_exp_f32 v103, v103
MA a,44,v,168,v,124
v_exp_f32 v104, v104
MA a,64,v,172,v,124
buffer_load_dwordx4 v241, s[44:47], s68 offen offset:2048 nt lds
MA a,84,v,176,v,124
v_exp_f32 v105, v105
MA a,104,v,180,v,124
v_exp_f32 v106, v106
MA a,124,v,184,v,124
v_exp_f32 v107, v107
MA a,144,v,188,v,124
buffer_load_dwordx4 v242, s[44:47], s68 offen offset:3072 nt lds
MA v,196,v,216,v,124
v_exp_f32 v108, v108
MC v,112,v,200,v,16,156
R4 v,200,238,16384
CV 128,96,97
MA v,112,v,204,v,36
R4 v,204,238,16896
CV 129,98,99
MA v,112,v,208,v,56
R4 v,208,238,17408
CV 130,100,101
MA v,112,v,212,v,76
R4 v,212,238,17920
CV 131,102,103
MC v,116,a,252,v,76,156
R4 a,252,238,22016
CV 132,104,105
MA v,116,a,248,v,56
R4 a,248,238,21504
CV 133,106,107
MA v,116,a,244,v,36
R4 a,244,238,20992
MA v,116,a,240,v,16
R4 a,240,238,20480
MA a,8,v,160,v,128
v_exp_f32 v109, v109
MA a,28,v,164,v,128
v_exp_f32 v110, v110
MA a,48,v,168,v,128
v_exp_f32 v111, v111
MA a,68,v,172,v,128
v_exp_f32 v112, v112
MA a,88,v,176,v,128
v_exp_f32 v113, v113
MA a,108,v,180,v,128
v_exp_f32 v114, v114
MA a,128,v,184,v,128
s_add_u32 m0, s64, 0
s_mov_b32 s69, 360448
MA a,148,v,188,v,128
v_exp_f32 v115, v115
MA v,220,v,216,v,128
s_waitcnt lgkmcnt(0)
CV 134,108,109
MC v,80,v,200,v,0,140
ds_write_b128 v251, a[164:167] offset:16384
CV 135,110,111
MA v,80,v,204,v,20
buffer_load_dwordx4 v241, s[40:43], s69 offen offset:0 nt lds
MA v,80,v,208,v,40
v_exp_f32 v116, v116
MA v,80,v,212,v,60
v_exp_f32 v117, v117
MC v,84,a,252,v,60,140
v_exp_f32 v118, v118
MA v,84,a,248,v,40
buffer_load_dwordx4 v242, s[40:43], s69 offen offset:1024 nt lds
MA v,84,a,244,v,20
v_exp_f32 v119, v119
MA v,84,a,240,v,0
v_exp_f32 v80, v80
MA a,12,v,160,v,132
v_exp_f32 v81, v81
MA a,32,v,164,v,132
buffer_load_dwordx4 v241, s[40:43], s69 offen offset:2048 nt lds
MA a,52,v,168,v,132
v_exp_f32 v82, v82
MA a,72,v,172,v,132
v_exp_f32 v83, v83
MA a,92,v,176,v,132
v_exp_f32 v84, v84
MA a,112,v,180,v,132
buffer_load_dwordx4 v242, s[40:43], s69 offen offset:3072 nt lds
MA a,132,v,184,v,132
v_exp_f32 v85, v85
MA a,152,v,188,v,132
v_exp_f32 v86, v86
MA v,224,v,216,v,132
v_exp_f32 v87, v87
MC v,88,v,200,v,4,144
CV 136,112,113
CV 137,114,115
MA v,88,v,204,v,24
s_memrealtime s[98:99]
CV 138,116,117
MA v,88,v,208,v,44
CV 139,118,119
CV 120,80,81
MA v,88,v,212,v,64
CV 121,82,83
CV 122,84,85
MC v,92,a,252,v,64,144
s_waitcnt lgkmcnt(0)
s_sub_i32 s98, s98, s76
MA v,92,a,248,v,44
s_cmp_lt_i32 s98, 0
s_cbranch_scc1 PACE1_9%=
PACED1_9%=:
MA v,92,a,244,v,24
v_exp_f32 v88, v88
MA v,92,a,240,v,4
v_exp_f32 v89, v89
MA a,16,v,160,v,136
s_waitcnt vmcnt(12)
s_barrier
MA a,36,v,164,v,136
TR v,160,239,16384
CV 123,86,87
MA a,56,v,168,v,136
TR v,162,239,20480
s_add_u32 s76, s76, s29
MA a,76,v,172,v,136
TR v,164,240,16384
CV 124,88,89
MA a,96,v,176,v,136
TR v,166,240,20480
MA a,116,v,180,v,136
TR v,168,239,16896
MA a,136,v,184,v,136
TR v,170,239,20992
MA a,156,v,188,v,136
TR v,172,240,16896
MA v,228,v,216,v,136
TR v,174,240,20992
MC v,96,v,200,v,8,148
TR v,176,239,17408
MA v,96,v,204,v,28
TR v,178,239,21504
MA v,96,v,208,v,48
TR v,180,240,17408
MA v,96,v,212,v,68
TR v,182,240,21504
MC v,100,a,252,v,68,148
TR v,184,239,17920
MA v,100,a,248,v,48
TR v,186,239,22016
s_waitcnt lgkmcnt(6)
MA v,100,a,244,v,28
TR v,188,240,17920
MA v,100,a,240,v,8
TR v,190,240,22016
MA a,0,v,160,v,120
v_exp_f32 v90, v90
MA a,20,v,164,v,120
v_exp_f32 v91, v91
MA a,40,v,168,v,120
v_exp_f32 v92, v92
MA a,60,v,172,v,120
s_waitcnt lgkmcnt(0)
CV 125,90,91
MA a,80,v,176,v,120
v_exp_f32 v93, v93
MA a,100,v,180,v,120
v_exp_f32 v94, v94
MA a,120,v,184,v,120
v_exp_f32 v95, v95
MA a,140,v,188,v,120
v_exp_f32 v96, v96
MA v,192,v,216,v,120
s_add_u32 m0, s64, 114688
s_mov_b32 s70, 352256
MC v,104,v,200,v,12,152
v_exp_f32 v97, v97
MA v,104,v,204,v,32
v_exp_f32 v98, v98
MA v,104,v,208,v,52
v_exp_f32 v99, v99
MA v,104,v,212,v,72
buffer_load_dwordx4 v241, s[44:47], s70 offen offset:0 nt lds
MC v,108,a,252,v,72,152
CV 126,92,93
CV 127,94,95
MA v,108,a,248,v,52
v_exp_f32 v100, v100
MA v,108,a,244,v,32
v_exp_f32 v101, v101
MA v,108,a,240,v,12
buffer_load_dwordx4 v242, s[44:47], s70 offen offset:1024 nt lds
MA a,4,v,160,v,124
v_exp_f32 v102, v102
MA a,24,v,164,v,124
v_exp_f32 v103, v103
MA a,44,v,168,v,124
v_exp_f32 v104, v104
MA a,64,v,172,v,124
buffer_load_dwordx4 v241, s[44:47], s70 offen offset:2048 nt lds
MA a,84,v,176,v,124
v_exp_f32 v105, v105
MA a,104,v,180,v,124
v_exp_f32 v106, v106
MA a,124,v,184,v,124
v_exp_f32 v107, v107
MA a,144,v,188,v,124
buffer_load_dwordx4 v242, s[44:47], s70 offen offset:3072 nt lds
MA v,196,v,216,v,124
v_exp_f32 v108, v108
MC v,112,v,200,v,16,156
R4 v,200,238,32768
CV 128,96,97
MA v,112,v,204,v,36
R4 v,204,238,33280
CV 129,98,99
MA v,112,v,208,v,56
R4 v,208,238,33792
CV 130,100,101
MA v,112,v,212,v,76
R4 v,212,238,34304
CV 131,102,103
MC v,116,a,252,v,76,156
R4 a,252,238,38400
CV 132,104,105
MA v,116,a,248,v,56
R4 a,248,238,37888
CV 133,106,107
MA v,116,a,244,v,36
R4 a,244,238,37376
MA v,116,a,240,v,16
R4 a,240,238,36864
MA a,8,v,160,v,128
v_exp_f32 v109, v109
MA a,28,v,164,v,128
v_exp_f32 v110, v110
MA a,48,v,168,v,128
v_exp_f32 v111, v111
MA a,68,v,172,v,128
v_exp_f32 v112, v112
MA a,88,v,176,v,128
v_exp_f32 v113, v113
MA a,108,v,180,v,128
v_exp_f32 v114, v114
MA a,128,v,184,v,128
v_exp_f32 v115, v115
MA a,148,v,188,v,128
v_exp_f32 v116, v116
MA v,220,v,216,v,128
s_waitcnt lgkmcnt(0)
CV 134,108,109
MC v,80,v,200,v,0,140
ds_write_b128 v251, a[184:187] offset:20480
CV 135,110,111
MA v,80,v,204,v,20
v_exp_f32 v117, v117
MA v,80,v,208,v,40
v_exp_f32 v118, v118
MA v,80,v,212,v,60
v_exp_f32 v119, v119
MC v,84,a,252,v,60,140
CV 136,112,113
CV 137,114,115
MA v,84,a,248,v,40
CV 138,116,117
CV 139,118,119
MA v,84,a,244,v,20
v_exp_f32 v80, v80
MA v,84,a,240,v,0
v_exp_f32 v81, v81
MA a,12,v,160,v,132
v_exp_f32 v82, v82
MA a,32,v,164,v,132
v_exp_f32 v83, v83
MA a,52,v,168,v,132
v_exp_f32 v84, v84
MA a,72,v,172,v,132
v_exp_f32 v85, v85
MA a,92,v,176,v,132
v_exp_f32 v86, v86
MA a,112,v,180,v,132
v_exp_f32 v87, v87
MA a,132,v,184,v,132
CV 120,80,81
CV 121,82,83
MA a,152,v,188,v,132
CV 122,84,85
CV 123,86,87
MA v,224,v,216,v,132
MC v,88,v,200,v,4,144
MA v,88,v,204,v,24
s_memrealtime s[98:99]
MA v,88,v,208,v,44
MA v,88,v,212,v,64
MC v,92,a,252,v,64,144
s_waitcnt lgkmcnt(0)
s_sub_i32 s98, s98, s76
MA v,92,a,248,v,44
s_cmp_lt_i32 s98, 0
s_cbranch_scc1 PACE1_10%=
PACED1_10%=:
MA v,92,a,244,v,24
v_exp_f32 v88, v88
MA v,92,a,240,v,4
v_exp_f32 v89, v89
MA a,16,v,160,v,136
s_waitcnt vmcnt(8)
s_barrier
MA a,36,v,164,v,136
TR v,160,239,32768
s_add_u32 s76, s76, s29
MA a,56,v,168,v,136
TR v,162,239,36864
CV 124,88,89
MA a,76,v,172,v,136
TR v,164,240,32768
MA a,96,v,176,v,136
TR v,166,240,36864
MA a,116,v,180,v,136
TR v,168,239,33280
MA a,136,v,184,v,136
TR v,170,239,37376
MA a,156,v,188,v,136
TR v,172,240,33280
MA v,228,v,216,v,136
TR v,174,240,37376
MC v,96,v,200,v,8,148
TR v,176,239,33792
MA v,96,v,204,v,28
TR v,178,239,37888
MA v,96,v,208,v,48
TR v,180,240,33792
MA v,96,v,212,v,68
TR v,182,240,37888
MC v,100,a,252,v,68,148
TR v,184,239,34304
MA v,100,a,248,v,48
TR v,186,239,38400
s_waitcnt lgkmcnt(6)
MA v,100,a,244,v,28
TR v,188,240,34304
MA v,100,a,240,v,8
TR v,190,240,38400
MA a,0,v,160,v,120
v_exp_f32 v90, v90
MA a,20,v,164,v,120
v_exp_f32 v91, v91
MA a,40,v,168,v,120
v_exp_f32 v92, v92
MA a,60,v,172,v,120
s_waitcnt lgkmcnt(0)
CV 125,90,91
MA a,80,v,176,v,120
v_exp_f32 v93, v93
MA a,100,v,180,v,120
v_exp_f32 v94, v94
MA a,120,v,184,v,120
v_exp_f32 v95, v95
MA a,140,v,188,v,120
v_exp_f32 v96, v96
MA v,192,v,216,v,120
s_add_u32 m0, s64, 16384
s_mov_b32 s67, 368640
MC v,104,v,200,v,12,152
v_exp_f32 v97, v97
MA v,104,v,204,v,32
v_exp_f32 v98, v98
MA v,104,v,208,v,52
v_exp_f32 v99, v99
MA v,104,v,212,v,72
buffer_load_dwordx4 v241, s[40:43], s67 offen offset:0 nt lds
MC v,108,a,252,v,72,152
CV 126,92,93
CV 127,94,95
MA v,108,a,248,v,52
v_exp_f32 v100, v100
MA v,108,a,244,v,32
v_exp_f32 v101, v101
MA v,108,a,240,v,12
buffer_load_dwordx4 v242, s[40:43], s67 offen offset:1024 nt lds
MA a,4,v,160,v,124
v_exp_f32 v102, v102
MA a,24,v,164,v,124
v_exp_f32 v103, v103
MA a,44,v,168,v,124
v_exp_f32 v104, v104
MA a,64,v,172,v,124
buffer_load_dwordx4 v241, s[40:43], s67 offen offset:2048 nt lds
MA a,84,v,176,v,124
v_exp_f32 v105, v105
MA a,104,v,180,v,124
v_exp_f32 v106, v106
MA a,124,v,184,v,124
v_exp_f32 v107, v107
MA a,144,v,188,v,124
buffer_load_dwordx4 v242, s[40:43], s67 offen offset:3072 nt lds
MA v,196,v,216,v,124
v_exp_f32 v108, v108
MC v,112,v,200,v,16,156
R4 v,200,238,49152
CV 128,96,97
MA v,112,v,204,v,36
R4 v,204,238,49664
CV 129,98,99
MA v,112,v,208,v,56
R4 v,208,238,50176
CV 130,100,101
MA v,112,v,212,v,76
R4 v,212,238,50688
CV 131,102,103
MC v,116,a,252,v,76,156
R4 a,252,238,54784
CV 132,104,105
MA v,116,a,248,v,56
R4 a,248,238,54272
CV 133,106,107
MA v,116,a,244,v,36
R4 a,244,238,53760
MA v,116,a,240,v,16
R4 a,240,238,53248
MA a,8,v,160,v,128
v_exp_f32 v109, v109
MA a,28,v,164,v,128
v_exp_f32 v110, v110
MA a,48,v,168,v,128
v_exp_f32 v111, v111
MA a,68,v,172,v,128
v_exp_f32 v112, v112
MA a,88,v,176,v,128
v_exp_f32 v113, v113
MA a,108,v,180,v,128
v_exp_f32 v114, v114
MA a,128,v,184,v,128
s_add_u32 m0, s64, 65536
s_mov_b32 s68, 360448
MA a,148,v,188,v,128
v_exp_f32 v115, v115
MA v,220,v,216,v,128
s_waitcnt lgkmcnt(0)
CV 134,108,109
MC v,80,v,200,v,0,140
ds_write_b128 v251, a[204:207] offset:24576
CV 135,110,111
MA v,80,v,204,v,20
buffer_load_dwordx4 v241, s[44:47], s68 offen offset:0 nt lds
MA v,80,v,208,v,40
v_exp_f32 v116, v116
MA v,80,v,212,v,60
v_exp_f32 v117, v117
MC v,84,a,252,v,60,140
v_exp_f32 v118, v118
MA v,84,a,248,v,40
buffer_load_dwordx4 v242, s[44:47], s68 offen offset:1024 nt lds
MA v,84,a,244,v,20
v_exp_f32 v119, v119
MA v,84,a,240,v,0
v_exp_f32 v80, v80
MA a,12,v,160,v,132
v_exp_f32 v81, v81
MA a,32,v,164,v,132
buffer_load_dwordx4 v241, s[44:47], s68 offen offset:2048 nt lds
MA a,52,v,168,v,132
v_exp_f32 v82, v82
MA a,72,v,172,v,132
v_exp_f32 v83, v83
MA a,92,v,176,v,132
v_exp_f32 v84, v84
MA a,112,v,180,v,132
buffer_load_dwordx4 v242, s[44:47], s68 offen offset:3072 nt lds
MA a,132,v,184,v,132
v_exp_f32 v85, v85
MA a,152,v,188,v,132
v_exp_f32 v86, v86
MA v,224,v,216,v,132
v_exp_f32 v87, v87
MC v,88,v,200,v,4,144
CV 136,112,113
)ASM"
R"ASM(CV 137,114,115
MA v,88,v,204,v,24
s_memrealtime s[98:99]
CV 138,116,117
MA v,88,v,208,v,44
CV 139,118,119
CV 120,80,81
MA v,88,v,212,v,64
CV 121,82,83
CV 122,84,85
MC v,92,a,252,v,64,144
s_waitcnt lgkmcnt(0)
s_sub_i32 s98, s98, s76
MA v,92,a,248,v,44
s_cmp_lt_i32 s98, 0
s_cbranch_scc1 PACE1_11%=
PACED1_11%=:
MA v,92,a,244,v,24
v_exp_f32 v88, v88
MA v,92,a,240,v,4
v_exp_f32 v89, v89
MA a,16,v,160,v,136
s_waitcnt vmcnt(8)
s_barrier
MA a,36,v,164,v,136
TR v,160,239,49152
CV 123,86,87
MA a,56,v,168,v,136
TR v,162,239,53248
s_add_u32 s76, s76, s29
MA a,76,v,172,v,136
TR v,164,240,49152
CV 124,88,89
MA a,96,v,176,v,136
TR v,166,240,53248
v_xor_b32 v251, 131072, v251
MA a,116,v,180,v,136
TR v,168,239,49664
MA a,136,v,184,v,136
TR v,170,239,53760
MA a,156,v,188,v,136
TR v,172,240,49664
MA v,228,v,216,v,136
TR v,174,240,53760
MC v,96,v,200,v,8,148
TR v,176,239,50176
MA v,96,v,204,v,28
TR v,178,239,54272
MA v,96,v,208,v,48
TR v,180,240,50176
MA v,96,v,212,v,68
TR v,182,240,54272
MC v,100,a,252,v,68,148
TR v,184,239,50688
MA v,100,a,248,v,48
TR v,186,239,54784
s_waitcnt lgkmcnt(6)
MA v,100,a,244,v,28
TR v,188,240,50688
MA v,100,a,240,v,8
TR v,190,240,54784
MA a,0,v,160,v,120
v_exp_f32 v90, v90
MA a,20,v,164,v,120
v_exp_f32 v91, v91
MA a,40,v,168,v,120
v_exp_f32 v92, v92
MA a,60,v,172,v,120
s_waitcnt lgkmcnt(0)
CV 125,90,91
MA a,80,v,176,v,120
v_exp_f32 v93, v93
MA a,100,v,180,v,120
s_mov_b32 m0, 161792
s_min_u32 s94, s30, 3
MA a,120,v,184,v,120
s_sub_u32 s30, s30, s94
s_mov_b32 s70, 81920
MA a,140,v,188,v,120
v_exp_f32 v94, v94
MA v,192,v,216,v,120
v_exp_f32 v95, v95
MC v,104,v,200,v,12,152
buffer_load_dword v255, s[92:95], s70 idxen lds
MA v,104,v,204,v,32
s_add_u32 m0, s64, 32768
s_mov_b32 s69, 376832
MA v,104,v,208,v,52
v_exp_f32 v96, v96
MA v,104,v,212,v,72
v_exp_f32 v97, v97
MC v,108,a,252,v,72,152
buffer_load_dwordx4 v241, s[40:43], s69 offen offset:0 nt lds
MA v,108,a,248,v,52
CV 126,92,93
CV 127,94,95
MA v,108,a,244,v,32
v_exp_f32 v98, v98
MA v,108,a,240,v,12
v_exp_f32 v99, v99
MA a,4,v,160,v,124
buffer_load_dwordx4 v242, s[40:43], s69 offen offset:1024 nt lds
MA a,24,v,164,v,124
v_exp_f32 v100, v100
MA a,44,v,168,v,124
v_exp_f32 v101, v101
MA a,64,v,172,v,124
v_exp_f32 v102, v102
MA a,84,v,176,v,124
buffer_load_dwordx4 v241, s[40:43], s69 offen offset:2048 nt lds
MA a,104,v,180,v,124
v_exp_f32 v103, v103
MA a,124,v,184,v,124
v_exp_f32 v104, v104
MA a,144,v,188,v,124
v_exp_f32 v105, v105
MA v,196,v,216,v,124
buffer_load_dwordx4 v242, s[40:43], s69 offen offset:3072 nt lds
MC v,112,v,200,v,16,156
R4 v,200,238,0
CV 128,96,97
MA v,112,v,204,v,36
R4 v,204,238,512
CV 129,98,99
MA v,112,v,208,v,56
R4 v,208,238,1024
CV 130,100,101
MA v,112,v,212,v,76
R4 v,212,238,1536
CV 131,102,103
MC v,116,a,252,v,76,156
R4 a,252,238,5632
CV 132,104,105
MA v,116,a,248,v,56
R4 a,248,238,5120
MA v,116,a,244,v,36
R4 a,244,238,4608
MA v,116,a,240,v,16
R4 a,240,238,4096
MA a,8,v,160,v,128
v_exp_f32 v106, v106
MA a,28,v,164,v,128
v_exp_f32 v107, v107
MA a,48,v,168,v,128
v_exp_f32 v108, v108
MA a,68,v,172,v,128
v_exp_f32 v109, v109
MA a,88,v,176,v,128
v_exp_f32 v110, v110
MA a,108,v,180,v,128
v_exp_f32 v111, v111
MA a,128,v,184,v,128
s_add_u32 m0, s64, 81920
s_mov_b32 s70, 368640
MA a,148,v,188,v,128
v_exp_f32 v112, v112
MA v,220,v,216,v,128
s_waitcnt lgkmcnt(0)
CV 133,106,107
MC v,80,v,200,v,0,140
v_exp_f32 v113, v113
MA v,80,v,204,v,20
buffer_load_dwordx4 v241, s[44:47], s70 offen offset:0 nt lds
MA v,80,v,208,v,40
v_exp_f32 v114, v114
MA v,80,v,212,v,60
v_exp_f32 v115, v115
MC v,84,a,252,v,60,140
CV 134,108,109
CV 135,110,111
MA v,84,a,248,v,40
buffer_load_dwordx4 v242, s[44:47], s70 offen offset:1024 nt lds
MA v,84,a,244,v,20
v_exp_f32 v116, v116
MA v,84,a,240,v,0
v_exp_f32 v117, v117
MA a,12,v,160,v,132
v_exp_f32 v118, v118
MA a,32,v,164,v,132
buffer_load_dwordx4 v241, s[44:47], s70 offen offset:2048 nt lds
MA a,52,v,168,v,132
v_exp_f32 v119, v119
MA a,72,v,172,v,132
v_exp_f32 v80, v80
MA a,92,v,176,v,132
v_exp_f32 v81, v81
MA a,112,v,180,v,132
buffer_load_dwordx4 v242, s[44:47], s70 offen offset:3072 nt lds
MA a,132,v,184,v,132
v_exp_f32 v82, v82
MA a,152,v,188,v,132
v_exp_f32 v83, v83
MA v,224,v,216,v,132
v_exp_f32 v84, v84
MC v,88,v,200,v,4,144
v_exp_f32 v85, v85
MA v,88,v,204,v,24
s_memrealtime s[98:99]
CV 136,112,113
MA v,88,v,208,v,44
v_exp_f32 v86, v86
MA v,88,v,212,v,64
v_exp_f32 v87, v87
MC v,92,a,252,v,64,144
s_waitcnt lgkmcnt(0)
s_sub_i32 s98, s98, s76
MA v,92,a,248,v,44
s_cmp_lt_i32 s98, 0
s_cbranch_scc1 PACE1_12%=
PACED1_12%=:
CV 137,114,115
CV 138,116,117
CV 139,118,119
MA v,92,a,244,v,24
v_exp_f32 v88, v88
MA v,92,a,240,v,4
v_exp_f32 v89, v89
MA a,16,v,160,v,136
s_waitcnt vmcnt(9)
s_barrier
MA a,36,v,164,v,136
TR v,160,239,0
CV 120,80,81
MA a,56,v,168,v,136
TR v,162,239,4096
CV 121,82,83
MA a,76,v,172,v,136
TR v,164,240,0
CV 122,84,85
MA a,96,v,176,v,136
TR v,166,240,4096
CV 123,86,87
MA a,116,v,180,v,136
TR v,168,239,512
s_add_u32 s76, s76, s29
MA a,136,v,184,v,136
TR v,170,239,4608
CV 124,88,89
MA a,156,v,188,v,136
TR v,172,240,512
MA v,228,v,216,v,136
TR v,174,240,4608
MC v,96,v,200,v,8,148
TR v,176,239,1024
MA v,96,v,204,v,28
TR v,178,239,5120
MA v,96,v,208,v,48
TR v,180,240,1024
MA v,96,v,212,v,68
TR v,182,240,5120
MC v,100,a,252,v,68,148
TR v,184,239,1536
MA v,100,a,248,v,48
TR v,186,239,5632
s_waitcnt lgkmcnt(6)
MA v,100,a,244,v,28
TR v,188,240,1536
MA v,100,a,240,v,8
TR v,190,240,5632
MA a,0,v,160,v,120
ds_write_b128 v251, a[224:227] offset:0
MA a,20,v,164,v,120
v_exp_f32 v90, v90
MA a,40,v,168,v,120
v_exp_f32 v91, v91
MA a,60,v,172,v,120
s_waitcnt lgkmcnt(1)
CV 125,90,91
MA a,80,v,176,v,120
ds_write_b128 v251, a[168:171] offset:4096
MA a,100,v,180,v,120
v_exp_f32 v92, v92
MA a,120,v,184,v,120
v_exp_f32 v93, v93
MA a,140,v,188,v,120
v_exp_f32 v94, v94
MA v,192,v,216,v,120
s_add_u32 m0, s64, 49152
s_mov_b32 s67, 385024
MC v,104,v,200,v,12,152
ds_write_b128 v251, a[188:191] offset:8192
CV 126,92,93
MA v,104,v,204,v,32
v_exp_f32 v95, v95
MA v,104,v,208,v,52
v_exp_f32 v96, v96
MA v,104,v,212,v,72
buffer_load_dwordx4 v241, s[40:43], s67 offen offset:0 nt lds
MC v,108,a,252,v,72,152
ds_write_b128 v251, a[208:211] offset:12288
CV 127,94,95
MA v,108,a,248,v,52
v_exp_f32 v97, v97
MA v,108,a,244,v,32
v_exp_f32 v98, v98
MA v,108,a,240,v,12
buffer_load_dwordx4 v242, s[40:43], s67 offen offset:1024 nt lds
MA a,4,v,160,v,124
v_exp_f32 v99, v99
MA a,24,v,164,v,124
v_exp_f32 v100, v100
MA a,44,v,168,v,124
v_exp_f32 v101, v101
MA a,64,v,172,v,124
buffer_load_dwordx4 v241, s[40:43], s67 offen offset:2048 nt lds
MA a,84,v,176,v,124
v_exp_f32 v102, v102
MA a,104,v,180,v,124
v_exp_f32 v103, v103
MA a,124,v,184,v,124
v_exp_f32 v104, v104
MA a,144,v,188,v,124
buffer_load_dwordx4 v242, s[40:43], s67 offen offset:3072 nt lds
MA v,196,v,216,v,124
v_exp_f32 v105, v105
MC v,112,v,200,v,16,156
R4 v,200,238,16384
CV 128,96,97
MA v,112,v,204,v,36
R4 v,204,238,16896
CV 129,98,99
MA v,112,v,208,v,56
R4 v,208,238,17408
CV 130,100,101
MA v,112,v,212,v,76
R4 v,212,238,17920
CV 131,102,103
MC v,116,a,252,v,76,156
R4 a,252,238,22016
CV 132,104,105
MA v,116,a,248,v,56
R4 a,248,238,21504
MA v,116,a,244,v,36
R4 a,244,238,20992
MA v,116,a,240,v,16
R4 a,240,238,20480
MA a,8,v,160,v,128
v_exp_f32 v106, v106
MA a,28,v,164,v,128
v_exp_f32 v107, v107
MA a,48,v,168,v,128
v_exp_f32 v108, v108
MA a,68,v,172,v,128
v_exp_f32 v109, v109
MA a,88,v,176,v,128
v_exp_f32 v110, v110
MA a,108,v,180,v,128
v_exp_f32 v111, v111
MA a,128,v,184,v,128
s_add_u32 m0, s64, 98304
s_mov_b32 s68, 376832
MA a,148,v,188,v,128
v_exp_f32 v112, v112
MA v,220,v,216,v,128
s_waitcnt lgkmcnt(0)
CV 133,106,107
MC v,80,v,200,v,0,140
v_exp_f32 v113, v113
MA v,80,v,204,v,20
buffer_load_dwordx4 v241, s[44:47], s68 offen offset:0 nt lds
MA v,80,v,208,v,40
v_exp_f32 v114, v114
MA v,80,v,212,v,60
v_exp_f32 v115, v115
MC v,84,a,252,v,60,140
CV 134,108,109
CV 135,110,111
MA v,84,a,248,v,40
buffer_load_dwordx4 v242, s[44:47], s68 offen offset:1024 nt lds
MA v,84,a,244,v,20
v_exp_f32 v116, v116
MA v,84,a,240,v,0
v_exp_f32 v117, v117
MA a,12,v,160,v,132
v_exp_f32 v118, v118
MA a,32,v,164,v,132
buffer_load_dwordx4 v241, s[44:47], s68 offen offset:2048 nt lds
MA a,52,v,168,v,132
v_exp_f32 v119, v119
MA a,72,v,172,v,132
v_exp_f32 v80, v80
MA a,92,v,176,v,132
v_exp_f32 v81, v81
MA a,112,v,180,v,132
buffer_load_dwordx4 v242, s[44:47], s68 offen offset:3072 nt lds
MA a,132,v,184,v,132
v_exp_f32 v82, v82
MA a,152,v,188,v,132
v_exp_f32 v83, v83
MA v,224,v,216,v,132
v_exp_f32 v84, v84
MC v,88,v,200,v,4,144
v_exp_f32 v85, v85
MA v,88,v,204,v,24
s_memrealtime s[98:99]
CV 136,112,113
MA v,88,v,208,v,44
v_exp_f32 v86, v86
MA v,88,v,212,v,64
v_exp_f32 v87, v87
MC v,92,a,252,v,64,144
s_waitcnt lgkmcnt(0)
s_sub_i32 s98, s98, s76
MA v,92,a,248,v,44
s_cmp_lt_i32 s98, 0
s_cbranch_scc1 PACE1_13%=
PACED1_13%=:
CV 137,114,115
CV 138,116,117
CV 139,118,119
MA v,92,a,244,v,24
v_exp_f32 v88, v88
MA v,92,a,240,v,4
v_exp_f32 v89, v89
MA a,16,v,160,v,136
s_waitcnt vmcnt(8)
s_barrier
MA a,36,v,164,v,136
TR v,160,239,16384
CV 120,80,81
MA a,56,v,168,v,136
TR v,162,239,20480
CV 121,82,83
MA a,76,v,172,v,136
TR v,164,240,16384
CV 122,84,85
MA a,96,v,176,v,136
TR v,166,240,20480
CV 123,86,87
MA a,116,v,180,v,136
TR v,168,239,16896
s_add_u32 s76, s76, s29
MA a,136,v,184,v,136
TR v,170,239,20992
CV 124,88,89
MA a,156,v,188,v,136
TR v,172,240,16896
MA v,228,v,216,v,136
TR v,174,240,20992
MC v,96,v,200,v,8,148
TR v,176,239,17408
MA v,96,v,204,v,28
TR v,178,239,21504
MA v,96,v,208,v,48
TR v,180,240,17408
MA v,96,v,212,v,68
TR v,182,240,21504
MC v,100,a,252,v,68,148
TR v,184,239,17920
MA v,100,a,248,v,48
TR v,186,239,22016
s_waitcnt lgkmcnt(6)
MA v,100,a,244,v,28
TR v,188,240,17920
MA v,100,a,240,v,8
TR v,190,240,22016
MA a,0,v,160,v,120
ds_write_b128 v251, a[228:231] offset:16384
MA a,20,v,164,v,120
v_exp_f32 v90, v90
MA a,40,v,168,v,120
v_exp_f32 v91, v91
MA a,60,v,172,v,120
s_waitcnt lgkmcnt(1)
CV 125,90,91
MA a,80,v,176,v,120
ds_write_b128 v251, a[172:175] offset:20480
MA a,100,v,180,v,120
v_exp_f32 v92, v92
MA a,120,v,184,v,120
v_exp_f32 v93, v93
MA a,140,v,188,v,120
v_exp_f32 v94, v94
MA v,192,v,216,v,120
ds_write_b128 v251, a[192:195] offset:24576
CV 126,92,93
MC v,104,v,200,v,12,152
v_exp_f32 v95, v95
MA v,104,v,204,v,32
v_exp_f32 v96, v96
MA v,104,v,208,v,52
v_exp_f32 v97, v97
MA v,104,v,212,v,72
ds_write_b128 v251, a[212:215] offset:28672
CV 127,94,95
MC v,108,a,252,v,72,152
v_exp_f32 v98, v98
MA v,108,a,248,v,52
v_exp_f32 v99, v99
MA v,108,a,244,v,32
v_exp_f32 v100, v100
MA v,108,a,240,v,12
v_exp_f32 v101, v101
MA a,4,v,160,v,124
v_exp_f32 v102, v102
MA a,24,v,164,v,124
v_exp_f32 v103, v103
MA a,44,v,168,v,124
v_exp_f32 v104, v104
MA a,64,v,172,v,124
v_exp_f32 v105, v105
MA a,84,v,176,v,124
v_exp_f32 v106, v106
MA a,104,v,180,v,124
v_exp_f32 v107, v107
MA a,124,v,184,v,124
v_exp_f32 v108, v108
MA a,144,v,188,v,124
v_exp_f32 v109, v109
MA v,196,v,216,v,124
v_exp_f32 v110, v110
MC v,112,v,200,v,16,156
R4 v,200,238,32768
CV 128,96,97
MA v,112,v,204,v,36
R4 v,204,238,33280
CV 129,98,99
MA v,112,v,208,v,56
R4 v,208,238,33792
CV 130,100,101
MA v,112,v,212,v,76
R4 v,212,238,34304
CV 131,102,103
MC v,116,a,252,v,76,156
R4 a,252,238,38400
CV 132,104,105
MA v,116,a,248,v,56
R4 a,248,238,37888
CV 133,106,107
MA v,116,a,244,v,36
R4 a,244,238,37376
CV 134,108,109
MA v,116,a,240,v,16
R4 a,240,238,36864
MA a,8,v,160,v,128
v_exp_f32 v111, v111
MA a,28,v,164,v,128
v_exp_f32 v112, v112
MA a,48,v,168,v,128
v_exp_f32 v113, v113
MA a,68,v,172,v,128
v_exp_f32 v114, v114
MA a,88,v,176,v,128
v_exp_f32 v115, v115
MA a,108,v,180,v,128
v_exp_f32 v116, v116
MA a,128,v,184,v,128
s_add_u32 m0, s64, 114688
s_mov_b32 s69, 385024
MA a,148,v,188,v,128
v_exp_f32 v117, v117
MA v,220,v,216,v,128
s_waitcnt lgkmcnt(0)
CV 135,110,111
MC v,80,v,200,v,0,140
v_exp_f32 v118, v118
MA v,80,v,204,v,20
buffer_load_dwordx4 v241, s[44:47], s69 offen offset:0 nt lds
MA v,80,v,208,v,40
v_exp_f32 v119, v119
MA v,80,v,212,v,60
CV 136,112,113
CV 137,114,115
MC v,84,a,252,v,60,140
CV 138,116,117
CV 139,118,119
MA v,84,a,248,v,40
buffer_load_dwordx4 v242, s[44:47], s69 offen offset:1024 nt lds
)ASM"
R"ASM(MA v,84,a,244,v,20
v_exp_f32 v80, v80
MA v,84,a,240,v,0
v_exp_f32 v81, v81
MA a,12,v,160,v,132
v_exp_f32 v82, v82
MA a,32,v,164,v,132
buffer_load_dwordx4 v241, s[44:47], s69 offen offset:2048 nt lds
MA a,52,v,168,v,132
v_exp_f32 v83, v83
MA a,72,v,172,v,132
v_exp_f32 v84, v84
MA a,92,v,176,v,132
v_exp_f32 v85, v85
MA a,112,v,180,v,132
buffer_load_dwordx4 v242, s[44:47], s69 offen offset:3072 nt lds
MA a,132,v,184,v,132
v_exp_f32 v86, v86
MA a,152,v,188,v,132
v_exp_f32 v87, v87
MA v,224,v,216,v,132
CV 120,80,81
CV 121,82,83
MC v,88,v,200,v,4,144
CV 122,84,85
CV 123,86,87
MA v,88,v,204,v,24
s_memrealtime s[98:99]
MA v,88,v,208,v,44
MA v,88,v,212,v,64
MC v,92,a,252,v,64,144
s_waitcnt lgkmcnt(0)
s_sub_i32 s98, s98, s76
MA v,92,a,248,v,44
s_cmp_lt_i32 s98, 0
s_cbranch_scc1 PACE1_14%=
PACED1_14%=:
MA v,92,a,244,v,24
v_exp_f32 v88, v88
MA v,92,a,240,v,4
v_exp_f32 v89, v89
MA a,16,v,160,v,136
s_waitcnt vmcnt(4)
s_barrier
MA a,36,v,164,v,136
TR v,160,239,32768
s_add_u32 s76, s76, s29
MA a,56,v,168,v,136
TR v,162,239,36864
CV 124,88,89
MA a,76,v,172,v,136
TR v,164,240,32768
MA a,96,v,176,v,136
TR v,166,240,36864
MA a,116,v,180,v,136
TR v,168,239,33280
MA a,136,v,184,v,136
TR v,170,239,37376
MA a,156,v,188,v,136
TR v,172,240,33280
MA v,228,v,216,v,136
TR v,174,240,37376
MC v,96,v,200,v,8,148
TR v,176,239,33792
MA v,96,v,204,v,28
TR v,178,239,37888
MA v,96,v,208,v,48
TR v,180,240,33792
MA v,96,v,212,v,68
TR v,182,240,37888
MC v,100,a,252,v,68,148
TR v,184,239,34304
MA v,100,a,248,v,48
TR v,186,239,38400
s_waitcnt lgkmcnt(6)
MA v,100,a,244,v,28
TR v,188,240,34304
MA v,100,a,240,v,8
TR v,190,240,38400
MA a,0,v,160,v,120
ds_write_b128 v251, a[232:235] offset:32768
MA a,20,v,164,v,120
v_exp_f32 v90, v90
MA a,40,v,168,v,120
v_exp_f32 v91, v91
MA a,60,v,172,v,120
s_waitcnt lgkmcnt(1)
CV 125,90,91
MA a,80,v,176,v,120
ds_write_b128 v251, a[176:179] offset:36864
MA a,100,v,180,v,120
v_exp_f32 v92, v92
MA a,120,v,184,v,120
v_exp_f32 v93, v93
MA a,140,v,188,v,120
v_exp_f32 v94, v94
MA v,192,v,216,v,120
s_add_u32 m0, s64, 65536
s_add_u32 s70, s8, 0
MC v,104,v,200,v,12,152
ds_write_b128 v251, a[196:199] offset:40960
CV 126,92,93
MA v,104,v,204,v,32
v_exp_f32 v95, v95
MA v,104,v,208,v,52
v_exp_f32 v96, v96
MA v,104,v,212,v,72
buffer_load_dwordx4 v243, s[48:51], s70 offen offset:0 lds
MC v,108,a,252,v,72,152
ds_write_b128 v251, a[216:219] offset:45056
CV 127,94,95
MA v,108,a,248,v,52
v_exp_f32 v97, v97
MA v,108,a,244,v,32
v_exp_f32 v98, v98
MA v,108,a,240,v,12
buffer_load_dwordx4 v244, s[48:51], s70 offen offset:1024 lds
MA a,4,v,160,v,124
v_exp_f32 v99, v99
MA a,24,v,164,v,124
v_exp_f32 v100, v100
MA a,44,v,168,v,124
v_exp_f32 v101, v101
MA a,64,v,172,v,124
buffer_load_dwordx4 v245, s[48:51], s70 offen offset:2048 lds
MA a,84,v,176,v,124
buffer_load_dwordx4 v246, s[48:51], s70 offen offset:3072 lds
MA a,104,v,180,v,124
s_mov_b32 m0, 161792
s_mov_b32 s69, s8
MA a,124,v,184,v,124
buffer_load_dword v255, s[16:19], s69 idxen lds
MA a,144,v,188,v,124
s_mov_b32 m0, 161792
s_mov_b32 s70, s9
MA v,196,v,216,v,124
buffer_load_dword v255, s[16:19], s70 idxen lds
MC v,112,v,200,v,16,156
R4 v,200,238,49152
v_exp_f32 v102, v102
MA v,112,v,204,v,36
R4 v,204,238,49664
v_exp_f32 v103, v103
MA v,112,v,208,v,56
R4 v,208,238,50176
CV 128,96,97
MA v,112,v,212,v,76
R4 v,212,238,50688
CV 129,98,99
MC v,116,a,252,v,76,156
R4 a,252,238,54784
CV 130,100,101
MA v,116,a,248,v,56
R4 a,248,238,54272
CV 131,102,103
MA v,116,a,244,v,36
R4 a,244,238,53760
MA v,116,a,240,v,16
R4 a,240,238,53248
MA a,8,v,160,v,128
v_exp_f32 v104, v104
MA a,28,v,164,v,128
v_exp_f32 v105, v105
MA a,48,v,168,v,128
v_exp_f32 v106, v106
MA a,68,v,172,v,128
v_exp_f32 v107, v107
MA a,88,v,176,v,128
v_exp_f32 v108, v108
MA a,108,v,180,v,128
v_exp_f32 v109, v109
MA a,128,v,184,v,128
s_add_u32 m0, s64, 81920
s_add_u32 s67, s8, 131072
MA a,148,v,188,v,128
v_exp_f32 v110, v110
MA v,220,v,216,v,128
s_waitcnt lgkmcnt(0)
CV 132,104,105
MC v,80,v,200,v,0,140
v_exp_f32 v111, v111
MA v,80,v,204,v,20
buffer_load_dwordx4 v243, s[48:51], s67 offen offset:0 lds
MA v,80,v,208,v,40
v_exp_f32 v112, v112
MA v,80,v,212,v,60
v_exp_f32 v113, v113
MC v,84,a,252,v,60,140
CV 133,106,107
CV 134,108,109
MA v,84,a,248,v,40
buffer_load_dwordx4 v244, s[48:51], s67 offen offset:1024 lds
CV 135,110,111
MA v,84,a,244,v,20
v_exp_f32 v114, v114
MA v,84,a,240,v,0
v_exp_f32 v115, v115
MA a,12,v,160,v,132
v_exp_f32 v116, v116
MA a,32,v,164,v,132
buffer_load_dwordx4 v245, s[48:51], s67 offen offset:2048 lds
MA a,52,v,168,v,132
v_exp_f32 v117, v117
MA a,72,v,172,v,132
v_exp_f32 v118, v118
MA a,92,v,176,v,132
v_exp_f32 v119, v119
MA a,112,v,180,v,132
buffer_load_dwordx4 v246, s[48:51], s67 offen offset:3072 lds
MA a,132,v,184,v,132
v_exp_f32 v80, v80
MA a,152,v,188,v,132
v_exp_f32 v81, v81
MA v,224,v,216,v,132
v_exp_f32 v82, v82
MC v,88,v,200,v,4,144
v_exp_f32 v83, v83
MA v,88,v,204,v,24
v_exp_f32 v84, v84
MA v,88,v,208,v,44
v_exp_f32 v85, v85
MA v,88,v,212,v,64
v_exp_f32 v86, v86
MC v,92,a,252,v,64,144
CV 136,112,113
CV 137,114,115
MA v,92,a,248,v,44
CV 138,116,117
CV 139,118,119
MA v,92,a,244,v,24
v_exp_f32 v87, v87
MA v,92,a,240,v,4
v_exp_f32 v88, v88
MA a,16,v,160,v,136
s_waitcnt vmcnt(10)
s_barrier
MA a,36,v,164,v,136
TR v,160,239,49152
CV 120,80,81
MA a,56,v,168,v,136
TR v,162,239,53248
CV 121,82,83
MA a,76,v,172,v,136
TR v,164,240,49152
CV 122,84,85
MA a,96,v,176,v,136
TR v,166,240,53248
CV 123,86,87
MA a,116,v,180,v,136
TR v,168,239,49664
MA a,136,v,184,v,136
TR v,170,239,53760
MA a,156,v,188,v,136
TR v,172,240,49664
MA v,228,v,216,v,136
TR v,174,240,53760
MC v,96,v,200,v,8,148
TR v,176,239,50176
MA v,96,v,204,v,28
TR v,178,239,54272
MA v,96,v,208,v,48
TR v,180,240,50176
MA v,96,v,212,v,68
TR v,182,240,54272
MC v,100,a,252,v,68,148
TR v,184,239,50688
MA v,100,a,248,v,48
TR v,186,239,54784
s_waitcnt lgkmcnt(6)
MA v,100,a,244,v,28
TR v,188,240,50688
MA v,100,a,240,v,8
TR v,190,240,54784
MA a,0,v,160,v,120
ds_write_b128 v251, a[236:239] offset:49152
MA a,20,v,164,v,120
v_exp_f32 v89, v89
MA a,40,v,168,v,120
v_exp_f32 v90, v90
MA a,60,v,172,v,120
s_waitcnt lgkmcnt(1)
CV 124,88,89
MA a,80,v,176,v,120
v_exp_f32 v91, v91
MA a,100,v,180,v,120
v_exp_f32 v92, v92
MA a,120,v,184,v,120
v_exp_f32 v93, v93
MA a,140,v,188,v,120
v_exp_f32 v94, v94
MA v,192,v,216,v,120
s_add_u32 m0, s64, 98304
s_add_u32 s68, s9, 0
MC v,104,v,200,v,12,152
v_exp_f32 v95, v95
MA v,104,v,204,v,32
v_exp_f32 v96, v96
MA v,104,v,208,v,52
v_exp_f32 v97, v97
MA v,104,v,212,v,72
buffer_load_dwordx4 v243, s[48:51], s68 offen offset:0 lds
MC v,108,a,252,v,72,152
CV 125,90,91
CV 126,92,93
MA v,108,a,248,v,52
CV 127,94,95
CV 128,96,97
MA v,108,a,244,v,32
v_exp_f32 v98, v98
MA v,108,a,240,v,12
buffer_load_dwordx4 v244, s[48:51], s68 offen offset:1024 lds
MA a,4,v,160,v,124
v_exp_f32 v99, v99
MA a,24,v,164,v,124
v_exp_f32 v100, v100
MA a,44,v,168,v,124
v_exp_f32 v101, v101
MA a,64,v,172,v,124
buffer_load_dwordx4 v245, s[48:51], s68 offen offset:2048 lds
MA a,84,v,176,v,124
v_exp_f32 v102, v102
MA a,104,v,180,v,124
v_exp_f32 v103, v103
MA a,124,v,184,v,124
v_exp_f32 v104, v104
MA a,144,v,188,v,124
buffer_load_dwordx4 v246, s[48:51], s68 offen offset:3072 lds
MA v,196,v,216,v,124
s_mov_b32 m0, 161792
s_mov_b32 s67, s10
MC v,112,v,200,v,16,156
v_exp_f32 v105, v105
MA v,112,v,204,v,36
v_exp_f32 v106, v106
MA v,112,v,208,v,56
buffer_load_dword v255, s[16:19], s67 idxen lds
MA v,112,v,212,v,76
s_mov_b32 m0, 161792
s_mov_b32 s68, s11
MC v,116,a,252,v,76,156
CV 129,98,99
CV 130,100,101
MA v,116,a,248,v,56
CV 131,102,103
CV 132,104,105
MA v,116,a,244,v,36
buffer_load_dword v255, s[16:19], s68 idxen lds
v_exp_f32 v107, v107
MA v,116,a,240,v,16
s_mov_b32 m0, 161792
s_mov_b32 s69, s12
v_exp_f32 v108, v108
MA a,8,v,160,v,128
v_exp_f32 v109, v109
MA a,28,v,164,v,128
v_exp_f32 v110, v110
MA a,48,v,168,v,128
buffer_load_dword v255, s[16:19], s69 idxen lds
v_exp_f32 v111, v111
MA a,68,v,172,v,128
s_mov_b32 m0, 161792
s_mov_b32 s70, s13
v_rcp_f32 v232, v192
MA a,88,v,176,v,128
v_exp_f32 v112, v112
MA a,108,v,180,v,128
v_exp_f32 v113, v113
v_rcp_f32 v233, v196
MA a,128,v,184,v,128
buffer_load_dword v255, s[16:19], s70 idxen lds
CV 133,106,107
CV 134,108,109
CV 135,110,111
v_exp_f32 v114, v114
MA a,148,v,188,v,128
v_exp_f32 v115, v115
MA v,220,v,216,v,128
v_exp_f32 v116, v116
MA a,12,v,160,v,132
v_exp_f32 v117, v117
MA a,32,v,164,v,132
v_exp_f32 v118, v118
MA a,52,v,168,v,132
v_exp_f32 v119, v119
v_rcp_f32 v234, v220
MA a,72,v,172,v,132
CV 136,112,113
CV 137,114,115
MA a,92,v,176,v,132
s_waitcnt vmcnt(8) lgkmcnt(0)
s_barrier
MA a,112,v,180,v,132
TR a,240,247,0
CV 138,116,117
MA a,132,v,184,v,132
TR a,242,247,2048
CV 139,118,119
MA a,152,v,188,v,132
TR a,244,247,4096
v_add_f32 v80, v192, v196
MA v,224,v,216,v,132
TR a,246,247,6144
v_xor_b32 v251, 131072, v251
MA v,228,v,216,v,136
TR a,248,247,8192
s_add_u32 m0, s64, 114688
MA a,16,v,160,v,136
TR a,250,247,10240
s_add_u32 s67, s9, 131072
MA a,36,v,164,v,136
TR a,252,247,12288
v_add_f32 v81, v220, v224
MA a,56,v,168,v,136
TR a,254,247,14336
v_add_f32 v80, v80, v81
MA a,76,v,172,v,136
buffer_load_dwordx4 v243, s[48:51], s67 offen offset:0 lds
MA a,96,v,176,v,136
v_add_f32 v80, v80, v228
v_cmp_nge_f32 vcc, s83, v80
MA a,116,v,180,v,136
v_rcp_f32 v235, v224
MA a,136,v,184,v,136
s_waitcnt lgkmcnt(0)
v_rcp_f32 v236, v228
MA a,156,v,188,v,136
s_cbranch_vccnz SLOWCALL1%=
SLOWRET1%=:
MZ v,80,a,240,a,160
TR v,180,248,0
AR 224,0
MA v,80,a,244,a,180
TR v,182,248,2048
AR 225,1
MA v,80,a,248,a,200
TR v,184,248,4096
AR 226,2
MA v,80,a,252,a,220
TR v,186,248,6144
AR 227,3
MZ v,84,a,252,a,224
TR v,188,248,8192
AR 228,20
MA v,84,a,248,a,204
TR v,190,248,10240
AR 229,21
AR 230,22
AR 231,23
v_mul_f32 v224, v232, v224
v_mul_f32 v225, v232, v225
v_mul_f32 v226, v232, v226
v_mul_f32 v227, v232, v227
v_mul_f32 v228, v232, v228
v_mul_f32 v229, v232, v229
v_mul_f32 v230, v232, v230
v_mul_f32 v231, v232, v231
CV 0,224,225
CV 1,226,227
CV 2,228,229
CV 3,230,231
MA v,84,a,244,a,184
TR v,192,248,12288
AR 224,40
MA v,84,a,240,a,164
TR v,194,248,14336
AR 225,41
MZ v,88,a,240,a,168
buffer_load_dwordx4 v244, s[48:51], s67 offen offset:1024 lds
MA v,88,a,244,a,188
R4 a,0,252,0
AR 226,42
MA v,88,a,248,a,208
AR 227,43
AR 228,60
AR 229,61
AR 230,62
AR 231,63
v_mul_f32 v224, v232, v224
v_mul_f32 v225, v232, v225
v_mul_f32 v226, v232, v226
v_mul_f32 v227, v232, v227
v_mul_f32 v228, v232, v228
v_mul_f32 v229, v232, v229
v_mul_f32 v230, v232, v230
v_mul_f32 v231, v232, v231
CV 20,224,225
CV 21,226,227
CV 22,228,229
CV 23,230,231
MA v,88,a,252,a,228
buffer_load_dwordx4 v245, s[48:51], s67 offen offset:2048 lds
MZ v,92,a,252,a,232
R4 a,20,252,4096
AR 224,4
MA v,92,a,248,a,212
R4 a,40,252,8192
AR 225,5
MA v,92,a,244,a,192
buffer_load_dwordx4 v246, s[48:51], s67 offen offset:3072 lds
MA v,92,a,240,a,172
AR 226,6
AR 227,7
AR 228,24
AR 229,25
AR 230,26
AR 231,27
v_mul_f32 v224, v233, v224
v_mul_f32 v225, v233, v225
v_mul_f32 v226, v233, v226
v_mul_f32 v227, v233, v227
v_mul_f32 v228, v233, v228
v_mul_f32 v229, v233, v229
v_mul_f32 v230, v233, v230
v_mul_f32 v231, v233, v231
CV 4,224,225
CV 5,226,227
CV 6,228,229
CV 7,230,231
MZ v,96,a,240,a,176
R4 a,60,252,12288
v_xor_b32 v247, 0x4000, v247
MA v,96,a,244,a,196
R4 a,4,252,16384
AR 224,44
MA v,96,a,248,a,216
R4 a,24,252,20480
AR 225,45
MA v,96,a,252,a,236
s_waitcnt lgkmcnt(6)
AR 226,46
MZ v,100,v,180,a,160
TR a,240,247,0
AR 227,47
AR 228,64
AR 229,65
AR 230,66
AR 231,67
v_mul_f32 v224, v233, v224
v_mul_f32 v225, v233, v225
v_mul_f32 v226, v233, v226
v_mul_f32 v227, v233, v227
v_mul_f32 v228, v233, v228
v_mul_f32 v229, v233, v229
v_mul_f32 v230, v233, v230
v_mul_f32 v231, v233, v231
CV 24,224,225
CV 25,226,227
CV 26,228,229
CV 27,230,231
MA v,100,v,184,a,180
TR a,242,247,2048
AR 224,8
MA v,100,v,188,a,200
TR a,244,247,4096
AR 225,9
MA v,100,v,192,a,220
TR a,246,247,6144
AR 226,10
MZ v,104,v,192,a,224
TR a,248,247,8192
AR 227,11
MA v,104,v,188,a,204
TR a,250,247,10240
AR 228,28
AR 229,29
AR 230,30
AR 231,31
v_mul_f32 v224, v234, v224
v_mul_f32 v225, v234, v225
v_mul_f32 v226, v234, v226
v_mul_f32 v227, v234, v227
)ASM"
R"ASM(v_mul_f32 v228, v234, v228
v_mul_f32 v229, v234, v229
v_mul_f32 v230, v234, v230
v_mul_f32 v231, v234, v231
CV 8,224,225
CV 9,226,227
CV 10,228,229
CV 11,230,231
MA v,104,v,184,a,184
TR a,252,247,12288
AR 224,48
MA v,104,v,180,a,164
TR a,254,247,14336
AR 225,49
MZ v,108,v,180,a,168
R4 a,44,252,24576
AR 226,50
MA v,108,v,184,a,188
AR 227,51
AR 228,68
MA v,108,v,188,a,208
AR 229,69
AR 230,70
AR 231,71
v_mul_f32 v224, v234, v224
v_mul_f32 v225, v234, v225
v_mul_f32 v226, v234, v226
v_mul_f32 v227, v234, v227
v_mul_f32 v228, v234, v228
v_mul_f32 v229, v234, v229
v_mul_f32 v230, v234, v230
v_mul_f32 v231, v234, v231
CV 28,224,225
CV 29,226,227
CV 30,228,229
CV 31,230,231
MA v,108,v,192,a,228
AR 224,12
AR 225,13
MZ v,112,v,192,a,232
AR 226,14
AR 227,15
MA v,112,v,188,a,212
AR 228,32
AR 229,33
MA v,112,v,184,a,192
AR 230,34
AR 231,35
MA v,112,v,180,a,172
v_mul_f32 v224, v235, v224
v_mul_f32 v225, v235, v225
v_mul_f32 v226, v235, v226
v_mul_f32 v227, v235, v227
v_mul_f32 v228, v235, v228
v_mul_f32 v229, v235, v229
v_mul_f32 v230, v235, v230
v_mul_f32 v231, v235, v231
CV 12,224,225
CV 13,226,227
CV 14,228,229
CV 15,230,231
MZ v,116,v,180,a,176
v_xor_b32 v248, 0x4000, v248
v_xor_b32 v252, 131072, v252
MA v,116,v,184,a,196
s_waitcnt lgkmcnt(0)
R4 a,64,252,0
AR 224,52
MA v,116,v,188,a,216
R4 a,8,252,4096
AR 225,53
MA v,116,v,192,a,236
AR 226,54
MA v,80,a,240,a,0
TR v,180,248,0
AR 227,55
AR 228,72
AR 229,73
AR 230,74
AR 231,75
v_mul_f32 v224, v235, v224
v_mul_f32 v225, v235, v225
v_mul_f32 v226, v235, v226
v_mul_f32 v227, v235, v227
v_mul_f32 v228, v235, v228
v_mul_f32 v229, v235, v229
v_mul_f32 v230, v235, v230
v_mul_f32 v231, v235, v231
CV 32,224,225
CV 33,226,227
CV 34,228,229
CV 35,230,231
MA v,80,a,244,a,20
TR v,182,248,2048
AR 224,16
MA v,80,a,248,a,40
TR v,184,248,4096
R4 a,28,252,8192
MA v,80,a,252,a,60
TR v,186,248,6144
R4 a,48,252,12288
s_waitcnt lgkmcnt(6)
MA v,84,a,252,a,64
TR v,188,248,8192
R4 a,68,252,16384
MA v,84,a,248,a,44
TR v,190,248,10240
AR 225,17
R4 a,12,252,20480
AR 226,18
AR 227,19
AR 228,36
AR 229,37
AR 230,38
AR 231,39
v_mul_f32 v224, v236, v224
v_mul_f32 v225, v236, v225
v_mul_f32 v226, v236, v226
v_mul_f32 v227, v236, v227
v_mul_f32 v228, v236, v228
v_mul_f32 v229, v236, v229
v_mul_f32 v230, v236, v230
v_mul_f32 v231, v236, v231
CV 16,224,225
CV 17,226,227
CV 18,228,229
CV 19,230,231
MA v,84,a,244,a,24
TR v,192,248,12288
R4 a,32,252,24576
MA v,84,a,240,a,4
TR v,194,248,14336
R4 a,52,252,28672
MA v,88,a,240,a,8
R4 a,72,252,32768
AR 224,56
s_waitcnt lgkmcnt(7)
MA v,88,a,244,a,28
R4 a,16,252,36864
AR 225,57
MA v,88,a,248,a,48
AR 226,58
AR 227,59
AR 228,76
AR 229,77
AR 230,78
AR 231,79
v_mul_f32 v224, v236, v224
v_mul_f32 v225, v236, v225
v_mul_f32 v226, v236, v226
v_mul_f32 v227, v236, v227
v_mul_f32 v228, v236, v228
v_mul_f32 v229, v236, v229
v_mul_f32 v230, v236, v230
v_mul_f32 v231, v236, v231
CV 36,224,225
CV 37,226,227
CV 38,228,229
CV 39,230,231
MA v,88,a,252,a,68
R4 a,36,252,40960
v_xor_b32 v247, 0x4000, v247
s_waitcnt lgkmcnt(2)
MA v,92,a,252,a,72
R4 a,56,252,45056
AR 224,80
MA v,92,a,248,a,52
R4 a,76,252,49152
AR 225,81
MA v,92,a,244,a,32
AR 226,82
AR 227,83
MA v,92,a,240,a,12
AR 228,100
AR 229,101
s_waitcnt lgkmcnt(2)
MA v,96,a,240,a,16
s_add_u32 m0, s64, 65536
s_add_u32 s68, s8, 262144
MA v,96,a,244,a,36
AR 230,102
AR 231,103
s_waitcnt lgkmcnt(0)
MA v,96,a,248,a,56
v_mul_f32 v224, v232, v224
v_mul_f32 v225, v232, v225
MA v,96,a,252,a,76
s_waitcnt vmcnt(0)
s_barrier
v_mul_f32 v226, v232, v226
v_mul_f32 v227, v232, v227
v_mul_f32 v228, v232, v228
v_mul_f32 v229, v232, v229
v_mul_f32 v230, v232, v230
v_mul_f32 v231, v232, v231
CV 40,224,225
CV 41,226,227
CV 42,228,229
CV 43,230,231
MA v,100,v,180,a,0
TR a,240,247,32768
AR 224,120
MA v,100,v,184,a,20
TR a,242,247,34816
AR 225,121
MA v,100,v,188,a,40
TR a,244,247,36864
AR 226,122
MA v,100,v,192,a,60
TR a,246,247,38912
AR 227,123
MA v,104,v,192,a,64
TR a,248,247,40960
AR 228,140
MA v,104,v,188,a,44
TR a,250,247,43008
AR 229,141
MA v,104,v,184,a,24
TR a,252,247,45056
AR 230,142
MA v,104,v,180,a,4
TR a,254,247,47104
AR 231,143
MA v,108,v,180,a,8
buffer_load_dwordx4 v243, s[48:51], s68 offen offset:0 lds
MA v,108,v,184,a,28
v_mul_f32 v224, v232, v224
v_mul_f32 v225, v232, v225
v_mul_f32 v226, v232, v226
v_mul_f32 v227, v232, v227
v_mul_f32 v228, v232, v228
v_mul_f32 v229, v232, v229
v_mul_f32 v230, v232, v230
v_mul_f32 v231, v232, v231
CV 60,224,225
CV 61,226,227
CV 62,228,229
CV 63,230,231
MA v,108,v,188,a,48
ds_write_b128 v251, v[0:3] offset:0
v_xor_b32 v248, 0x4000, v248
MA v,108,v,192,a,68
buffer_load_dwordx4 v244, s[48:51], s68 offen offset:1024 lds
MA v,112,v,192,a,72
AR 224,84
AR 225,85
MA v,112,v,188,a,52
AR 226,86
AR 227,87
MA v,112,v,184,a,32
buffer_load_dwordx4 v245, s[48:51], s68 offen offset:2048 lds
MA v,112,v,180,a,12
ds_write_b128 v251, v[20:23] offset:4096
AR 228,104
MA v,116,v,180,a,16
AR 229,105
AR 230,106
MA v,116,v,184,a,36
buffer_load_dwordx4 v246, s[48:51], s68 offen offset:3072 lds
MA v,116,v,188,a,56
s_add_u32 m0, s64, 81920
s_add_u32 s69, s8, 393216
AR 231,107
v_mul_f32 v224, v233, v224
v_mul_f32 v225, v233, v225
v_mul_f32 v226, v233, v226
v_mul_f32 v227, v233, v227
v_mul_f32 v228, v233, v228
v_mul_f32 v229, v233, v229
v_mul_f32 v230, v233, v230
v_mul_f32 v231, v233, v231
CV 44,224,225
CV 45,226,227
CV 46,228,229
CV 47,230,231
MA v,116,v,192,a,76
s_waitcnt lgkmcnt(2)
AR 224,124
MZ v,140,a,240,a,160
TR v,180,248,32768
AR 225,125
MA v,140,a,244,a,180
TR v,182,248,34816
AR 226,126
MA v,140,a,248,a,200
TR v,184,248,36864
AR 227,127
MA v,140,a,252,a,220
TR v,186,248,38912
AR 228,144
MZ v,144,a,252,a,224
TR v,188,248,40960
AR 229,145
MA v,144,a,248,a,204
TR v,190,248,43008
AR 230,146
MA v,144,a,244,a,184
TR v,192,248,45056
AR 231,147
MA v,144,a,240,a,164
TR v,194,248,47104
v_mul_f32 v224, v233, v224
MZ v,148,a,240,a,168
buffer_load_dwordx4 v243, s[48:51], s69 offen offset:0 lds
v_mul_f32 v225, v233, v225
v_mul_f32 v226, v233, v226
v_mul_f32 v227, v233, v227
v_mul_f32 v228, v233, v228
v_mul_f32 v229, v233, v229
v_mul_f32 v230, v233, v230
v_mul_f32 v231, v233, v231
CV 64,224,225
CV 65,226,227
CV 66,228,229
CV 67,230,231
MA v,148,a,244,a,188
ds_write_b128 v251, v[40:43] offset:8192
AR 224,88
MA v,148,a,248,a,208
AR 225,89
AR 226,90
MA v,148,a,252,a,228
buffer_load_dwordx4 v244, s[48:51], s69 offen offset:1024 lds
MZ v,152,a,252,a,232
AR 227,91
AR 228,108
MA v,152,a,248,a,212
ds_write_b128 v251, v[60:63] offset:12288
AR 229,109
MA v,152,a,244,a,192
buffer_load_dwordx4 v245, s[48:51], s69 offen offset:2048 lds
MA v,152,a,240,a,172
AR 230,110
AR 231,111
MZ v,156,a,240,a,176
v_mul_f32 v224, v234, v224
v_mul_f32 v225, v234, v225
MA v,156,a,244,a,196
buffer_load_dwordx4 v246, s[48:51], s69 offen offset:3072 lds
s_mov_b32 m0, 161792
s_mov_b32 s68, s14
v_mul_f32 v226, v234, v226
v_mul_f32 v227, v234, v227
v_mul_f32 v228, v234, v228
v_mul_f32 v229, v234, v229
v_mul_f32 v230, v234, v230
v_mul_f32 v231, v234, v231
CV 48,224,225
CV 49,226,227
CV 50,228,229
CV 51,230,231
MA v,156,a,248,a,216
buffer_load_dword v255, s[16:19], s68 idxen lds
s_mov_b32 m0, 161792
s_mov_b32 s69, s15
v_xor_b32 v247, 0x4000, v247
MA v,156,a,252,a,236
s_waitcnt lgkmcnt(2)
buffer_load_dword v255, s[16:19], s69 idxen lds
MZ v,160,v,180,a,160
TR a,240,247,32768
AR 224,128
MA v,160,v,184,a,180
TR a,242,247,34816
AR 225,129
MA v,160,v,188,a,200
TR a,244,247,36864
AR 226,130
MA v,160,v,192,a,220
TR a,246,247,38912
AR 227,131
MZ v,164,v,192,a,224
TR a,248,247,40960
AR 228,148
MA v,164,v,188,a,204
TR a,250,247,43008
AR 229,149
MA v,164,v,184,a,184
TR a,252,247,45056
AR 230,150
MA v,164,v,180,a,164
TR a,254,247,47104
AR 231,151
v_mul_f32 v224, v234, v224
v_mul_f32 v225, v234, v225
v_mul_f32 v226, v234, v226
v_mul_f32 v227, v234, v227
v_mul_f32 v228, v234, v228
v_mul_f32 v229, v234, v229
v_mul_f32 v230, v234, v230
v_mul_f32 v231, v234, v231
CV 68,224,225
CV 69,226,227
CV 70,228,229
CV 71,230,231
MZ v,168,v,180,a,168
ds_write_b128 v251, v[4:7] offset:16384
AR 224,92
MA v,168,v,184,a,188
AR 225,93
AR 226,94
MA v,168,v,188,a,208
AR 227,95
AR 228,112
MA v,168,v,192,a,228
AR 229,113
AR 230,114
MZ v,172,v,192,a,232
ds_write_b128 v251, v[24:27] offset:20480
AR 231,115
MA v,172,v,188,a,212
v_mul_f32 v224, v235, v224
v_mul_f32 v225, v235, v225
MA v,172,v,184,a,192
v_mul_f32 v226, v235, v226
v_mul_f32 v227, v235, v227
MA v,172,v,180,a,172
v_mul_f32 v228, v235, v228
v_mul_f32 v229, v235, v229
MZ v,176,v,180,a,176
v_mul_f32 v230, v235, v230
v_mul_f32 v231, v235, v231
CV 52,224,225
CV 53,226,227
CV 54,228,229
CV 55,230,231
MA v,176,v,184,a,196
ds_write_b128 v251, v[44:47] offset:24576
v_xor_b32 v248, 0x4000, v248
MA v,176,v,188,a,216
v_xor_b32 v252, 131072, v252
AR 224,132
MA v,176,v,192,a,236
s_waitcnt lgkmcnt(3)
AR 225,133
MA v,140,a,240,a,0
TR v,180,248,32768
AR 226,134
MA v,140,a,244,a,20
TR v,182,248,34816
AR 227,135
MA v,140,a,248,a,40
TR v,184,248,36864
AR 228,152
MA v,140,a,252,a,60
TR v,186,248,38912
AR 229,153
MA v,144,a,252,a,64
TR v,188,248,40960
AR 230,154
MA v,144,a,248,a,44
TR v,190,248,43008
AR 231,155
MA v,144,a,244,a,24
TR v,192,248,45056
v_mul_f32 v224, v235, v224
v_mul_f32 v225, v235, v225
v_mul_f32 v226, v235, v226
v_mul_f32 v227, v235, v227
v_mul_f32 v228, v235, v228
v_mul_f32 v229, v235, v229
v_mul_f32 v230, v235, v230
v_mul_f32 v231, v235, v231
CV 72,224,225
CV 73,226,227
CV 74,228,229
CV 75,230,231
MA v,144,a,240,a,4
TR v,194,248,47104
AR 224,96
MA v,148,a,240,a,8
AR 225,97
AR 226,98
MA v,148,a,244,a,28
AR 227,99
AR 228,116
MA v,148,a,248,a,48
AR 229,117
AR 230,118
MA v,148,a,252,a,68
AR 231,119
v_mul_f32 v224, v236, v224
MA v,152,a,252,a,72
v_mul_f32 v225, v236, v225
v_mul_f32 v226, v236, v226
MA v,152,a,248,a,52
v_mul_f32 v227, v236, v227
v_mul_f32 v228, v236, v228
MA v,152,a,244,a,32
v_mul_f32 v229, v236, v229
v_mul_f32 v230, v236, v230
MA v,152,a,240,a,12
v_mul_f32 v231, v236, v231
CV 56,224,225
CV 57,226,227
CV 58,228,229
CV 59,230,231
MA v,156,a,240,a,16
s_add_u32 m0, s64, 98304
s_add_u32 s70, s9, 262144
MA v,156,a,244,a,36
v_xor_b32 v247, 0x4000, v247
v_xor_b32 v251, 131072, v251
MA v,156,a,248,a,56
ds_write_b128 v251, v[64:67] offset:0
AR 224,136
MA v,156,a,252,a,76
s_waitcnt vmcnt(2) lgkmcnt(1)
s_barrier
MA v,160,v,180,a,0
TR a,240,247,0
AR 225,137
MA v,160,v,184,a,20
TR a,242,247,2048
AR 226,138
MA v,160,v,188,a,40
TR a,244,247,4096
AR 227,139
MA v,160,v,192,a,60
TR a,246,247,6144
AR 228,156
MA v,164,v,192,a,64
TR a,248,247,8192
ds_write_b128 v251, v[8:11] offset:4096
MA v,164,v,188,a,44
TR a,250,247,10240
AR 229,157
AR 230,158
AR 231,159
v_mul_f32 v224, v236, v224
v_mul_f32 v225, v236, v225
v_mul_f32 v226, v236, v226
v_mul_f32 v227, v236, v227
v_mul_f32 v228, v236, v228
v_mul_f32 v229, v236, v229
v_mul_f32 v230, v236, v230
v_mul_f32 v231, v236, v231
CV 76,224,225
CV 77,226,227
CV 78,228,229
CV 79,230,231
MA v,164,v,184,a,24
TR a,252,247,12288
ds_write_b128 v251, v[28:31] offset:8192
MA v,164,v,180,a,4
TR a,254,247,14336
v_xor_b32 v248, 0x4000, v248
MA v,168,v,180,a,8
buffer_load_dwordx4 v243, s[48:51], s70 offen offset:0 lds
ds_write_b128 v251, v[48:51] offset:12288
MA v,168,v,184,a,28
MA v,168,v,188,a,48
ds_write_b128 v251, v[68:71] offset:16384
v_xor_b32 v247, 0x4000, v247
MA v,168,v,192,a,68
buffer_load_dwordx4 v244, s[48:51], s70 offen offset:1024 lds
MA v,172,v,192,a,72
ds_write_b128 v251, v[12:15] offset:20480
MA v,172,v,188,a,52
MA v,172,v,184,a,32
buffer_load_dwordx4 v245, s[48:51], s70 offen offset:2048 lds
ds_write_b128 v251, v[32:35] offset:24576
MA v,172,v,180,a,12
MA v,176,v,180,a,16
s_waitcnt lgkmcnt(1)
ds_write_b128 v251, v[52:55] offset:28672
MA v,176,v,184,a,36
buffer_load_dwordx4 v246, s[48:51], s70 offen offset:3072 lds
MA v,176,v,188,a,56
ds_write_b128 v251, v[72:75] offset:32768
s_add_u32 m0, s64, 114688
s_add_u32 s67, s9, 393216
MA v,176,v,192,a,76
MA v,80,a,240,v,0
TR v,180,248,0
ds_write_b128 v251, v[16:19] offset:36864
MA v,80,a,244,v,20
TR v,182,248,2048
MA v,80,a,248,v,40
TR v,184,248,4096
ds_write_b128 v251, v[36:39] offset:40960
MA v,80,a,252,v,60
TR v,186,248,6144
MA v,84,a,252,v,64
TR v,188,248,8192
)ASM"
R"ASM(ds_write_b128 v251, v[56:59] offset:45056
MA v,84,a,248,v,44
TR v,190,248,10240
MA v,84,a,244,v,24
TR v,192,248,12288
ds_write_b128 v251, v[76:79] offset:49152
MA v,84,a,240,v,4
TR v,194,248,14336
MA v,88,a,240,v,8
buffer_load_dwordx4 v243, s[48:51], s67 offen offset:0 lds
MA v,88,a,244,v,28
s_waitcnt lgkmcnt(1)
s_barrier
MA v,88,a,248,v,48
R4 a,80,252,0
v_xor_b32 v248, 0x4000, v248
MA v,88,a,252,v,68
buffer_load_dwordx4 v244, s[48:51], s67 offen offset:1024 lds
MA v,92,a,252,v,72
R4 a,100,252,4096
MA v,92,a,248,v,52
R4 a,120,252,8192
MA v,92,a,244,v,32
buffer_load_dwordx4 v245, s[48:51], s67 offen offset:2048 lds
MA v,92,a,240,v,12
R4 a,140,252,12288
MA v,96,a,240,v,16
R4 a,84,252,16384
MA v,96,a,244,v,36
buffer_load_dwordx4 v246, s[48:51], s67 offen offset:3072 lds
MA v,96,a,248,v,56
R4 a,104,252,20480
MA v,96,a,252,v,76
s_waitcnt lgkmcnt(6)
MA v,100,v,180,v,0
TR a,240,247,0
MA v,100,v,184,v,20
TR a,242,247,2048
MA v,100,v,188,v,40
TR a,244,247,4096
MA v,100,v,192,v,60
TR a,246,247,6144
MA v,104,v,192,v,64
TR a,248,247,8192
MA v,104,v,188,v,44
TR a,250,247,10240
MA v,104,v,184,v,24
TR a,252,247,12288
MA v,104,v,180,v,4
TR a,254,247,14336
MA v,108,v,180,v,8
R4 a,124,252,24576
MA v,108,v,184,v,28
v_xor_b32 v252, 131072, v252
MA v,108,v,188,v,48
s_waitcnt lgkmcnt(2)
R4 a,144,252,0
v_xor_b32 v247, 0x4000, v247
MA v,108,v,192,v,68
R4 a,88,252,4096
MA v,112,v,192,v,72
R4 a,108,252,8192
MA v,112,v,188,v,52
R4 a,128,252,12288
MA v,112,v,184,v,32
R4 a,148,252,16384
MA v,112,v,180,v,12
R4 a,92,252,20480
MA v,116,v,180,v,16
R4 a,112,252,24576
MA v,116,v,184,v,36
R4 a,132,252,28672
MA v,116,v,188,v,56
R4 a,152,252,32768
MA v,116,v,192,v,76
s_waitcnt lgkmcnt(10)
MA v,80,a,240,a,80
TR v,180,248,0
MA v,80,a,244,a,100
TR v,182,248,2048
MA v,80,a,248,a,120
TR v,184,248,4096
MA v,80,a,252,a,140
TR v,186,248,6144
s_waitcnt lgkmcnt(4)
MA v,84,a,252,a,144
TR v,188,248,8192
MA v,84,a,248,a,124
TR v,190,248,10240
MA v,84,a,244,a,104
TR v,192,248,12288
CV 120,80,81
MA v,84,a,240,a,84
TR v,194,248,14336
CV 121,82,83
MA v,88,a,240,a,88
R4 a,96,252,36864
s_mul_i32 s72, s8, 1280
MA v,88,a,244,a,108
R4 a,116,252,40960
s_add_u32 s72, s72, s20
MA v,88,a,248,a,128
R4 a,136,252,45056
v_xor_b32 v248, 0x4000, v248
MA v,88,a,252,a,148
R4 a,156,252,49152
CV 124,84,85
MA v,92,a,252,a,152
CV 125,86,87
MA v,92,a,248,a,132
MA v,92,a,244,a,112
CV 128,88,89
CV 129,90,91
MA v,92,a,240,a,92
s_waitcnt lgkmcnt(0)
MA v,96,a,240,a,96
s_add_u32 m0, s64, 65536
s_add_u32 s68, s10, 0
MA v,96,a,244,a,116
MA v,96,a,248,a,136
CV 132,92,93
CV 133,94,95
MA v,96,a,252,a,156
s_waitcnt vmcnt(0)
s_barrier
MA v,100,v,180,a,80
TR a,240,247,32768
v_mov_b32 v222, 0x3f80
MA v,100,v,184,a,100
TR a,242,247,34816
v_mov_b32 v216, 0
MA v,100,v,188,a,120
buffer_load_dwordx4 v243, s[48:51], s68 offen offset:0 lds
MA v,100,v,192,a,140
TR a,244,247,36864
CV 136,96,97
MA v,104,v,192,a,144
TR a,246,247,38912
CV 137,98,99
MA v,104,v,188,a,124
TR a,248,247,40960
v_mov_b32 v217, 0
MA v,104,v,184,a,104
TR a,250,247,43008
CV 122,100,101
MA v,104,v,180,a,84
buffer_load_dwordx4 v244, s[48:51], s68 offen offset:1024 lds
MA v,108,v,180,a,88
TR a,252,247,45056
CV 123,102,103
MA v,108,v,184,a,108
TR a,254,247,47104
v_mov_b32 v220, 0
MA v,108,v,188,a,128
v_xor_b32 v247, 0x4000, v247
CV 126,104,105
MA v,108,v,192,a,148
CV 127,106,107
v_mov_b32 v221, 0
MA v,112,v,192,a,152
buffer_load_dwordx4 v245, s[48:51], s68 offen offset:2048 lds
MA v,112,v,188,a,132
v_bfe_u32 v218, v255, 2, 2
MA v,112,v,184,a,112
CV 130,108,109
CV 131,110,111
MA v,112,v,180,a,92
v_lshrrev_b32 v219, 4, v255
MA v,116,v,180,a,96
v_cmp_eq_u32 vcc, v218, v219
MA v,116,v,184,a,116
buffer_load_dwordx4 v246, s[48:51], s68 offen offset:3072 lds
MA v,116,v,188,a,136
s_add_u32 m0, s64, 81920
s_add_u32 s69, s10, 131072
MA v,116,v,192,a,156
s_waitcnt lgkmcnt(0)
CV 134,112,113
MA v,140,a,240,v,0
TR v,180,248,32768
CV 135,114,115
MA v,140,a,244,v,20
TR v,182,248,34816
v_and_b32 v218, 1, v255
MA v,140,a,248,v,40
buffer_load_dwordx4 v243, s[48:51], s69 offen offset:0 lds
MA v,140,a,252,v,60
TR v,184,248,36864
CV 138,116,117
MA v,144,a,252,v,64
TR v,186,248,38912
CV 139,118,119
MA v,144,a,248,v,44
TR v,188,248,40960
v_lshlrev_b32 v218, 4, v218
MA v,144,a,244,v,24
TR v,190,248,43008
v_lshlrev_b32 v218, v218, v222
MA v,144,a,240,v,4
buffer_load_dwordx4 v244, s[48:51], s69 offen offset:1024 lds
MA v,148,a,240,v,8
TR v,192,248,45056
v_cndmask_b32 v218, 0, v218, vcc
MA v,148,a,244,v,28
TR v,194,248,47104
v_and_b32 v219, 2, v255
MA v,148,a,248,v,48
v_xor_b32 v248, 0x4000, v248
v_cmp_eq_u32 vcc, 0, v219
MA v,148,a,252,v,68
s_nop 0
s_lshl1_add_u32 s74, s8, s21
MA v,152,a,252,v,72
buffer_load_dwordx4 v245, s[48:51], s69 offen offset:2048 lds
MA v,152,a,248,v,52
v_mov_b32 v224, s96
MA v,152,a,244,v,32
v_mov_b32 v225, s97
MA v,152,a,240,v,12
v_cndmask_b32 v219, v218, v216, vcc
MA v,156,a,240,v,16
buffer_store_dwordx4 v[120:123], v249, s[52:55], s72 offen offset:0 sc1
MA v,156,a,244,v,36
buffer_load_dwordx4 v246, s[48:51], s69 offen offset:3072 lds
MA v,156,a,248,v,56
s_add_u32 m0, s64, 0
s_add_u32 s70, s10, 262144
MA v,156,a,252,v,76
s_waitcnt lgkmcnt(0)
v_cndmask_b32 v218, v216, v218, vcc
MA v,160,v,180,v,0
TR a,240,247,32768
MA v,160,v,184,v,20
TR a,242,247,34816
MA v,160,v,188,v,40
buffer_load_dwordx4 v243, s[48:51], s70 offen offset:0 lds
MA v,160,v,192,v,60
TR a,244,247,36864
MA v,164,v,192,v,64
TR a,246,247,38912
MA v,164,v,188,v,44
TR a,248,247,40960
MA v,164,v,184,v,24
TR a,250,247,43008
MA v,164,v,180,v,4
buffer_load_dwordx4 v244, s[48:51], s70 offen offset:1024 lds
MA v,168,v,180,v,8
TR a,252,247,45056
MA v,168,v,184,v,28
TR a,254,247,47104
MA v,168,v,188,v,48
v_xor_b32 v247, 0x4000, v247
MA v,168,v,192,v,68
MA v,172,v,192,v,72
buffer_load_dwordx4 v245, s[48:51], s70 offen offset:2048 lds
MA v,172,v,188,v,52
MA v,172,v,184,v,32
buffer_store_dwordx4 v[124:127], v249, s[52:55], s72 offen offset:1024 sc1
MA v,172,v,180,v,12
MA v,176,v,180,v,16
buffer_store_dwordx4 v[128:131], v249, s[52:55], s72 offen offset:2048 sc1
MA v,176,v,184,v,36
buffer_load_dwordx4 v246, s[48:51], s70 offen offset:3072 lds
MA v,176,v,188,v,56
s_add_u32 m0, s64, 16384
s_add_u32 s67, s10, 393216
MA v,176,v,192,v,76
s_waitcnt lgkmcnt(0)
MA v,140,a,240,a,80
TR v,180,248,32768
MA v,140,a,244,a,100
TR v,182,248,34816
MA v,140,a,248,a,120
buffer_load_dwordx4 v243, s[48:51], s67 offen offset:0 lds
MA v,140,a,252,a,140
TR v,184,248,36864
MA v,144,a,252,a,144
TR v,186,248,38912
MA v,144,a,248,a,124
TR v,188,248,40960
MA v,144,a,244,a,104
TR v,190,248,43008
CV 120,140,141
MA v,144,a,240,a,84
buffer_load_dwordx4 v244, s[48:51], s67 offen offset:1024 lds
MA v,148,a,240,a,88
TR v,192,248,45056
CV 121,142,143
MA v,148,a,244,a,108
TR v,194,248,47104
MA v,148,a,248,a,128
v_xor_b32 v248, 0x4000, v248
CV 124,144,145
MA v,148,a,252,a,148
CV 125,146,147
MA v,152,a,252,a,152
buffer_load_dwordx4 v245, s[48:51], s67 offen offset:2048 lds
MA v,152,a,248,a,132
MA v,152,a,244,a,112
CV 128,148,149
CV 129,150,151
MA v,152,a,240,a,92
MA v,156,a,240,a,96
buffer_store_dwordx4 v[132:135], v249, s[52:55], s72 offen offset:3072 sc1
MA v,156,a,244,a,116
buffer_load_dwordx4 v246, s[48:51], s67 offen offset:3072 lds
MA v,156,a,248,a,136
s_add_u32 m0, s64, 32768
s_add_u32 s68, s11, 0
MA v,156,a,252,a,156
s_waitcnt vmcnt(11) lgkmcnt(0)
s_barrier
MA v,160,v,180,a,80
TR a,240,247,0
CV 132,152,153
MA v,160,v,184,a,100
TR a,242,247,2048
CV 133,154,155
MA v,160,v,188,a,120
buffer_load_dwordx4 v243, s[48:51], s68 offen offset:0 lds
MA v,160,v,192,a,140
TR a,244,247,4096
MA v,164,v,192,a,144
TR a,246,247,6144
MA v,164,v,188,a,124
TR a,248,247,8192
MA v,164,v,184,a,104
TR a,250,247,10240
CV 122,160,161
MA v,164,v,180,a,84
buffer_load_dwordx4 v244, s[48:51], s68 offen offset:1024 lds
MA v,168,v,180,a,88
TR a,252,247,12288
CV 123,162,163
MA v,168,v,184,a,108
TR a,254,247,14336
MA v,168,v,188,a,128
buffer_store_dwordx4 v[136:139], v250, s[52:55], s72 offen offset:0 sc1
MA v,168,v,192,a,148
s_mul_i32 s72, s9, 1280
s_add_u32 s72, s72, s20
MA v,172,v,192,a,152
buffer_load_dwordx4 v245, s[48:51], s68 offen offset:2048 lds
MA v,172,v,188,a,132
CV 136,156,157
CV 137,158,159
MA v,172,v,184,a,112
v_xor_b32 v247, 0x4000, v247
CV 126,164,165
MA v,172,v,180,a,92
CV 127,166,167
CV 130,168,169
MA v,176,v,180,a,96
CV 131,170,171
MA v,176,v,184,a,116
buffer_load_dwordx4 v246, s[48:51], s68 offen offset:3072 lds
MA v,176,v,188,a,136
s_add_u32 m0, s64, 49152
s_add_u32 s69, s11, 131072
MA v,176,v,192,a,156
s_waitcnt lgkmcnt(0)
CV 134,172,173
MZ v,80,a,240,a,160
TR v,180,248,0
CV 135,174,175
MA v,80,a,244,a,180
TR v,182,248,2048
MA v,80,a,248,a,200
buffer_load_dwordx4 v243, s[48:51], s69 offen offset:0 lds
MA v,80,a,252,a,220
TR v,184,248,4096
CV 138,176,177
MZ v,84,a,252,a,224
TR v,186,248,6144
CV 139,178,179
MA v,84,a,248,a,204
TR v,188,248,8192
MA v,84,a,244,a,184
TR v,190,248,10240
MA v,84,a,240,a,164
buffer_load_dwordx4 v244, s[48:51], s69 offen offset:1024 lds
MZ v,88,a,240,a,168
TR v,192,248,12288
MA v,88,a,244,a,188
TR v,194,248,14336
MA v,88,a,248,a,208
v_xor_b32 v248, 0x4000, v248
MA v,88,a,252,a,228
MZ v,92,a,252,a,232
buffer_load_dwordx4 v245, s[48:51], s69 offen offset:2048 lds
MA v,92,a,248,a,212
MA v,92,a,244,a,192
MA v,92,a,240,a,172
MZ v,96,a,240,a,176
MA v,96,a,244,a,196
buffer_load_dwordx4 v246, s[48:51], s69 offen offset:3072 lds
MA v,96,a,248,a,216
MA v,96,a,252,a,236
s_waitcnt lgkmcnt(0)
MZ v,100,v,180,a,160
TR a,240,247,0
MA v,100,v,184,a,180
TR a,242,247,2048
MA v,100,v,188,a,200
buffer_store_dwordx4 v[120:123], v249, s[52:55], s72 offen offset:0 sc1
MA v,100,v,192,a,220
TR a,244,247,4096
MZ v,104,v,192,a,224
TR a,246,247,6144
MA v,104,v,188,a,204
TR a,248,247,8192
MA v,104,v,184,a,184
TR a,250,247,10240
MA v,104,v,180,a,164
buffer_store_dwordx4 v[124:127], v249, s[52:55], s72 offen offset:1024 sc1
MZ v,108,v,180,a,168
TR a,252,247,12288
MA v,108,v,184,a,188
TR a,254,247,14336
MA v,108,v,188,a,208
v_xor_b32 v247, 0x14000, v247
MA v,108,v,192,a,228
MZ v,112,v,192,a,232
buffer_store_dwordx4 v[128:131], v249, s[52:55], s72 offen offset:2048 sc1
MA v,112,v,188,a,212
MA v,112,v,184,a,192
MA v,112,v,180,a,172
MZ v,116,v,180,a,176
MA v,116,v,184,a,196
MA v,116,v,188,a,216
MA v,116,v,192,a,236
s_waitcnt lgkmcnt(0)
MA v,80,a,240,a,0
TR v,180,248,0
MA v,80,a,244,a,20
TR v,182,248,2048
MA v,80,a,248,a,40
MA v,80,a,252,a,60
TR v,184,248,4096
MA v,84,a,252,a,64
TR v,186,248,6144
MA v,84,a,248,a,44
TR v,188,248,8192
MA v,84,a,244,a,24
TR v,190,248,10240
s_waitcnt vmcnt(9)
MA v,84,a,240,a,4
buffer_store_dwordx4 v[132:135], v249, s[52:55], s72 offen offset:3072 sc1
MA v,88,a,240,a,8
TR v,192,248,12288
MA v,88,a,244,a,28
TR v,194,248,14336
MA v,88,a,248,a,48
v_xor_b32 v248, 0x14000, v248
MA v,88,a,252,a,68
MA v,92,a,252,a,72
buffer_store_dwordx2 v[224:225], v254, s[60:63], s74 offen sc1
MA v,92,a,248,a,52
s_lshl1_add_u32 s74, s9, s21
MA v,92,a,244,a,32
MA v,92,a,240,a,12
MA v,96,a,240,a,16
s_add_u32 m0, s64, 65536
s_add_u32 s68, s12, 0
MA v,96,a,244,a,36
buffer_store_dwordx4 v[136:139], v250, s[52:55], s72 offen offset:0 sc1
MA v,96,a,248,a,56
MA v,96,a,252,a,76
s_waitcnt lgkmcnt(0)
s_barrier
MA v,100,v,180,a,0
TR a,240,247,0
MA v,100,v,184,a,20
TR a,242,247,2048
MA v,100,v,188,a,40
buffer_load_dwordx4 v243, s[48:51], s68 offen offset:0 lds
MA v,100,v,192,a,60
TR a,244,247,4096
MA v,104,v,192,a,64
TR a,246,247,6144
MA v,104,v,188,a,44
TR a,248,247,8192
MA v,104,v,184,a,24
TR a,250,247,10240
MA v,104,v,180,a,4
buffer_load_dwordx4 v244, s[48:51], s68 offen offset:1024 lds
MA v,108,v,180,a,8
TR a,252,247,12288
MA v,108,v,184,a,28
TR a,254,247,14336
MA v,108,v,188,a,48
v_xor_b32 v247, 0x4000, v247
MA v,108,v,192,a,68
MA v,112,v,192,a,72
buffer_load_dwordx4 v245, s[48:51], s68 offen offset:2048 lds
MA v,112,v,188,a,52
MA v,112,v,184,a,32
MA v,112,v,180,a,12
MA v,116,v,180,a,16
MA v,116,v,184,a,36
buffer_load_dwordx4 v246, s[48:51], s68 offen offset:3072 lds
MA v,116,v,188,a,56
s_add_u32 m0, s64, 81920
s_add_u32 s69, s12, 131072
MA v,116,v,192,a,76
s_waitcnt lgkmcnt(0)
MA v,80,a,240,v,0
TR v,180,248,0
)ASM"
R"ASM(MA v,80,a,244,v,20
TR v,182,248,2048
MA v,80,a,248,v,40
buffer_load_dwordx4 v243, s[48:51], s69 offen offset:0 lds
MA v,80,a,252,v,60
TR v,184,248,4096
MA v,84,a,252,v,64
TR v,186,248,6144
MA v,84,a,248,v,44
TR v,188,248,8192
MA v,84,a,244,v,24
TR v,190,248,10240
MA v,84,a,240,v,4
buffer_load_dwordx4 v244, s[48:51], s69 offen offset:1024 lds
MA v,88,a,240,v,8
TR v,192,248,12288
MA v,88,a,244,v,28
TR v,194,248,14336
MA v,88,a,248,v,48
v_xor_b32 v248, 0x4000, v248
MA v,88,a,252,v,68
MA v,92,a,252,v,72
buffer_load_dwordx4 v245, s[48:51], s69 offen offset:2048 lds
MA v,92,a,248,v,52
MA v,92,a,244,v,32
MA v,92,a,240,v,12
MA v,96,a,240,v,16
MA v,96,a,244,v,36
buffer_load_dwordx4 v246, s[48:51], s69 offen offset:3072 lds
MA v,96,a,248,v,56
s_add_u32 m0, s64, 98304
s_add_u32 s70, s11, 262144
MA v,96,a,252,v,76
s_waitcnt lgkmcnt(0)
MA v,100,v,180,v,0
TR a,240,247,0
MA v,100,v,184,v,20
TR a,242,247,2048
MA v,100,v,188,v,40
buffer_load_dwordx4 v243, s[48:51], s70 offen offset:0 lds
MA v,100,v,192,v,60
TR a,244,247,4096
MA v,104,v,192,v,64
TR a,246,247,6144
MA v,104,v,188,v,44
TR a,248,247,8192
MA v,104,v,184,v,24
TR a,250,247,10240
MA v,104,v,180,v,4
buffer_load_dwordx4 v244, s[48:51], s70 offen offset:1024 lds
MA v,108,v,180,v,8
TR a,252,247,12288
MA v,108,v,184,v,28
TR a,254,247,14336
MA v,108,v,188,v,48
v_xor_b32 v247, 0x4000, v247
MA v,108,v,192,v,68
MA v,112,v,192,v,72
buffer_load_dwordx4 v245, s[48:51], s70 offen offset:2048 lds
MA v,112,v,188,v,52
MA v,112,v,184,v,32
MA v,112,v,180,v,12
MA v,116,v,180,v,16
MA v,116,v,184,v,36
buffer_load_dwordx4 v246, s[48:51], s70 offen offset:3072 lds
MA v,116,v,188,v,56
s_add_u32 m0, s64, 114688
s_add_u32 s67, s11, 393216
MA v,116,v,192,v,76
s_waitcnt lgkmcnt(0)
MA v,80,a,240,a,80
TR v,180,248,0
MA v,80,a,244,a,100
TR v,182,248,2048
MA v,80,a,248,a,120
buffer_load_dwordx4 v243, s[48:51], s67 offen offset:0 lds
MA v,80,a,252,a,140
TR v,184,248,4096
MA v,84,a,252,a,144
TR v,186,248,6144
MA v,84,a,248,a,124
TR v,188,248,8192
MA v,84,a,244,a,104
TR v,190,248,10240
s_waitcnt vmcnt(13)
MA v,84,a,240,a,84
buffer_load_dwordx4 v244, s[48:51], s67 offen offset:1024 lds
MA v,88,a,240,a,88
TR v,192,248,12288
s_mul_i32 s73, s14, 160
MA v,88,a,244,a,108
TR v,194,248,14336
s_add_u32 s73, s73, s22
MA v,88,a,248,a,128
s_lshr_b32 s75, s14, 4
s_add_u32 s75, s75, s23
MA v,88,a,252,a,148
v_xor_b32 v248, 0x4000, v248
CV 120,80,81
MA v,92,a,252,a,152
buffer_load_dwordx4 v245, s[48:51], s67 offen offset:2048 lds
MA v,92,a,248,a,132
CV 121,82,83
CV 124,84,85
MA v,92,a,244,a,112
CV 125,86,87
s_mul_i32 s72, s10, 1280
MA v,92,a,240,a,92
s_add_u32 s72, s72, s20
CV 128,88,89
MA v,96,a,240,a,96
buffer_store_dwordx2 v[224:225], v254, s[60:63], s74 offen sc1
MA v,96,a,244,a,116
buffer_load_dwordx4 v246, s[48:51], s67 offen offset:3072 lds
MA v,96,a,248,a,136
s_add_u32 m0, s64, 0
s_add_u32 s70, s12, 262144
MA v,96,a,252,a,156
s_waitcnt lgkmcnt(0)
s_barrier
MA v,100,v,180,a,80
TR a,240,247,32768
CV 129,90,91
MA v,100,v,184,a,100
TR a,242,247,34816
CV 132,92,93
MA v,100,v,188,a,120
buffer_load_dwordx4 v243, s[48:51], s70 offen offset:0 lds
MA v,100,v,192,a,140
TR a,244,247,36864
CV 133,94,95
MA v,104,v,192,a,144
TR a,246,247,38912
CV 136,96,97
MA v,104,v,188,a,124
TR a,248,247,40960
CV 137,98,99
MA v,104,v,184,a,104
TR a,250,247,43008
CV 122,100,101
MA v,104,v,180,a,84
buffer_load_dwordx4 v244, s[48:51], s70 offen offset:1024 lds
MA v,108,v,180,a,88
TR a,252,247,45056
CV 123,102,103
MA v,108,v,184,a,108
TR a,254,247,47104
MA v,108,v,188,a,128
v_xor_b32 v247, 0x4000, v247
CV 126,104,105
MA v,108,v,192,a,148
buffer_load_dwordx2 v[222:223], off, s[60:63], s75 sc1
MA v,112,v,192,a,152
buffer_load_dwordx4 v245, s[48:51], s70 offen offset:2048 lds
MA v,112,v,188,a,132
CV 127,106,107
MA v,112,v,184,a,112
CV 130,108,109
CV 131,110,111
MA v,112,v,180,a,92
MA v,116,v,180,a,96
MA v,116,v,184,a,116
buffer_load_dwordx4 v246, s[48:51], s70 offen offset:3072 lds
MA v,116,v,188,a,136
s_add_u32 m0, s64, 16384
s_add_u32 s67, s12, 393216
MA v,116,v,192,a,156
s_waitcnt lgkmcnt(0)
CV 134,112,113
MZ v,80,a,240,a,160
TR v,180,248,32768
CV 135,114,115
MA v,80,a,244,a,180
TR v,182,248,34816
s_lshl1_add_u32 s74, s10, s21
MA v,80,a,248,a,200
buffer_load_dwordx4 v243, s[48:51], s67 offen offset:0 lds
MA v,80,a,252,a,220
TR v,184,248,36864
CV 138,116,117
MZ v,84,a,252,a,224
TR v,186,248,38912
CV 139,118,119
MA v,84,a,248,a,204
TR v,188,248,40960
MA v,84,a,244,a,184
TR v,190,248,43008
MA v,84,a,240,a,164
buffer_load_dwordx4 v244, s[48:51], s67 offen offset:1024 lds
MZ v,88,a,240,a,168
TR v,192,248,45056
MA v,88,a,244,a,188
TR v,194,248,47104
MA v,88,a,248,a,208
v_xor_b32 v248, 0x4000, v248
MA v,88,a,252,a,228
MZ v,92,a,252,a,232
buffer_load_dwordx4 v245, s[48:51], s67 offen offset:2048 lds
MA v,92,a,248,a,212
MA v,92,a,244,a,192
MA v,92,a,240,a,172
MZ v,96,a,240,a,176
MA v,96,a,244,a,196
buffer_load_dwordx4 v246, s[48:51], s67 offen offset:3072 lds
MA v,96,a,248,a,216
MA v,96,a,252,a,236
s_waitcnt lgkmcnt(0)
MZ v,100,v,180,a,160
TR a,240,247,32768
MA v,100,v,184,a,180
TR a,242,247,34816
MA v,100,v,188,a,200
buffer_store_dwordx4 v[120:123], v249, s[52:55], s72 offen offset:0 sc1
MA v,100,v,192,a,220
TR a,244,247,36864
MZ v,104,v,192,a,224
TR a,246,247,38912
MA v,104,v,188,a,204
TR a,248,247,40960
MA v,104,v,184,a,184
TR a,250,247,43008
MA v,104,v,180,a,164
buffer_store_dwordx4 v[124:127], v249, s[52:55], s72 offen offset:1024 sc1
MZ v,108,v,180,a,168
TR a,252,247,45056
MA v,108,v,184,a,188
TR a,254,247,47104
MA v,108,v,188,a,208
v_xor_b32 v247, 0x14000, v247
MA v,108,v,192,a,228
MZ v,112,v,192,a,232
buffer_store_dwordx4 v[128:131], v249, s[52:55], s72 offen offset:2048 sc1
MA v,112,v,188,a,212
MA v,112,v,184,a,192
MA v,112,v,180,a,172
MZ v,116,v,180,a,176
MA v,116,v,184,a,196
MA v,116,v,188,a,216
MA v,116,v,192,a,236
s_waitcnt lgkmcnt(0)
MA v,80,a,240,a,0
TR v,180,248,32768
MA v,80,a,244,a,20
TR v,182,248,34816
MA v,80,a,248,a,40
MA v,80,a,252,a,60
TR v,184,248,36864
MA v,84,a,252,a,64
TR v,186,248,38912
MA v,84,a,248,a,44
TR v,188,248,40960
MA v,84,a,244,a,24
TR v,190,248,43008
MA v,84,a,240,a,4
buffer_store_dwordx4 v[132:135], v249, s[52:55], s72 offen offset:3072 sc1
MA v,88,a,240,a,8
TR v,192,248,45056
MA v,88,a,244,a,28
TR v,194,248,47104
MA v,88,a,248,a,48
s_waitcnt vmcnt(10)
v_cmp_ne_u64 vcc, s[96:97], v[222:223]
MA v,88,a,252,a,68
s_cbranch_vccnz SPIN0%=
GOT0%=:
MA v,92,a,252,a,72
buffer_load_dwordx4 v[196:199], v249, s[52:55], s73 offen offset:0 sc1
MA v,92,a,248,a,52
v_xor_b32 v248, 0x14000, v248
MA v,92,a,244,a,32
MA v,92,a,240,a,12
MA v,96,a,240,a,16
s_add_u32 m0, s64, 32768
s_add_u32 s68, s13, 0
MA v,96,a,244,a,36
buffer_store_dwordx4 v[136:139], v250, s[52:55], s72 offen offset:0 sc1
MA v,96,a,248,a,56
MA v,96,a,252,a,76
s_waitcnt lgkmcnt(0)
s_barrier
MA v,100,v,180,a,0
TR a,240,247,32768
MA v,100,v,184,a,20
TR a,242,247,34816
MA v,100,v,188,a,40
buffer_load_dwordx4 v243, s[48:51], s68 offen offset:0 lds
MA v,100,v,192,a,60
TR a,244,247,36864
MA v,104,v,192,a,64
TR a,246,247,38912
MA v,104,v,188,a,44
TR a,248,247,40960
MA v,104,v,184,a,24
TR a,250,247,43008
MA v,104,v,180,a,4
buffer_load_dwordx4 v244, s[48:51], s68 offen offset:1024 lds
MA v,108,v,180,a,8
TR a,252,247,45056
MA v,108,v,184,a,28
TR a,254,247,47104
MA v,108,v,188,a,48
v_xor_b32 v247, 0x4000, v247
MA v,108,v,192,a,68
MA v,112,v,192,a,72
buffer_load_dwordx4 v245, s[48:51], s68 offen offset:2048 lds
MA v,112,v,188,a,52
MA v,112,v,184,a,32
buffer_load_dwordx4 v[200:203], v249, s[52:55], s73 offen offset:1024 sc1
MA v,112,v,180,a,12
MA v,116,v,180,a,16
buffer_load_dwordx4 v[204:207], v249, s[52:55], s73 offen offset:2048 sc1
MA v,116,v,184,a,36
buffer_load_dwordx4 v246, s[48:51], s68 offen offset:3072 lds
MA v,116,v,188,a,56
s_add_u32 m0, s64, 49152
s_add_u32 s69, s13, 131072
MA v,116,v,192,a,76
s_waitcnt lgkmcnt(0)
MA v,80,a,240,v,0
TR v,180,248,32768
MA v,80,a,244,v,20
TR v,182,248,34816
MA v,80,a,248,v,40
buffer_load_dwordx4 v243, s[48:51], s69 offen offset:0 lds
MA v,80,a,252,v,60
TR v,184,248,36864
MA v,84,a,252,v,64
TR v,186,248,38912
buffer_load_dwordx4 v[208:211], v249, s[52:55], s73 offen offset:3072 sc1
MA v,84,a,248,v,44
TR v,188,248,40960
MA v,84,a,244,v,24
TR v,190,248,43008
MA v,84,a,240,v,4
buffer_load_dwordx4 v244, s[48:51], s69 offen offset:1024 lds
MA v,88,a,240,v,8
TR v,192,248,45056
MA v,88,a,244,v,28
TR v,194,248,47104
buffer_load_dwordx4 v[212:215], v250, s[52:55], s73 offen offset:0 sc1
MA v,88,a,248,v,48
s_mul_i32 s73, s13, 160
s_add_u32 s73, s73, s22
MA v,88,a,252,v,68
s_lshr_b32 s75, s13, 4
s_add_u32 s75, s75, s23
MA v,92,a,252,v,72
buffer_load_dwordx4 v245, s[48:51], s69 offen offset:2048 lds
MA v,92,a,248,v,52
v_xor_b32 v248, 0x4000, v248
MA v,92,a,244,v,32
MA v,92,a,240,v,12
MA v,96,a,240,v,16
MA v,96,a,244,v,36
buffer_load_dwordx4 v246, s[48:51], s69 offen offset:3072 lds
MA v,96,a,248,v,56
MA v,96,a,252,v,76
s_waitcnt lgkmcnt(0)
MA v,100,v,180,v,0
TR a,240,247,32768
MA v,100,v,184,v,20
TR a,242,247,34816
MA v,100,v,188,v,40
MA v,100,v,192,v,60
TR a,244,247,36864
MA v,104,v,192,v,64
TR a,246,247,38912
MA v,104,v,188,v,44
TR a,248,247,40960
MA v,104,v,184,v,24
TR a,250,247,43008
MA v,104,v,180,v,4
MA v,108,v,180,v,8
TR a,252,247,45056
MA v,108,v,184,v,28
TR a,254,247,47104
MA v,108,v,188,v,48
buffer_load_dwordx2 v[222:223], off, s[60:63], s75 sc1
MA v,108,v,192,v,68
v_xor_b32 v247, 0x4000, v247
MA v,112,v,192,v,72
MA v,112,v,188,v,52
MA v,112,v,184,v,32
MA v,112,v,180,v,12
MA v,116,v,180,v,16
MA v,116,v,184,v,36
MA v,116,v,188,v,56
MA v,116,v,192,v,76
s_waitcnt lgkmcnt(0)
MA v,80,a,240,a,80
TR v,180,248,32768
MA v,80,a,244,a,100
TR v,182,248,34816
MA v,80,a,248,a,120
MA v,80,a,252,a,140
TR v,184,248,36864
MA v,84,a,252,a,144
TR v,186,248,38912
MA v,84,a,248,a,124
TR v,188,248,40960
MA v,84,a,244,a,104
TR v,190,248,43008
s_waitcnt vmcnt(13)
MA v,84,a,240,a,84
buffer_store_dwordx2 v[224:225], v254, s[60:63], s74 offen sc1
MA v,88,a,240,a,88
TR v,192,248,45056
CV 120,80,81
MA v,88,a,244,a,108
TR v,194,248,47104
CV 121,82,83
MA v,88,a,248,a,128
v_xor_b32 v248, 0x4000, v248
CV 124,84,85
MA v,88,a,252,a,148
CV 125,86,87
s_mul_i32 s72, s11, 1280
MA v,92,a,252,a,152
s_add_u32 s72, s72, s20
MA v,92,a,248,a,132
MA v,92,a,244,a,112
CV 128,88,89
CV 129,90,91
MA v,92,a,240,a,92
MA v,96,a,240,a,96
s_add_u32 m0, s64, 98304
s_add_u32 s70, s13, 262144
MA v,96,a,244,a,116
MA v,96,a,248,a,136
s_waitcnt vmcnt(4)
CV 132,92,93
MA v,96,a,252,a,156
s_waitcnt lgkmcnt(0)
s_barrier
MZ v,140,v,218,v,196
buffer_load_dwordx4 v243, s[48:51], s70 offen offset:0 lds
MZ v,160,v,216,v,196
CV 133,94,95
MZ v,144,v,218,v,200
CV 136,96,97
CV 137,98,99
MZ v,164,v,216,v,200
MZ v,148,v,218,v,204
MZ v,168,v,216,v,204
buffer_load_dwordx4 v244, s[48:51], s70 offen offset:1024 lds
MZ v,152,v,218,v,208
MZ v,172,v,216,v,208
MZ v,156,v,218,v,212
MZ v,176,v,216,v,212
MA v,100,v,180,a,80
TR a,240,247,0
MA v,100,v,184,a,100
TR a,242,247,2048
MA v,100,v,188,a,120
buffer_load_dwordx4 v245, s[48:51], s70 offen offset:2048 lds
MA v,100,v,192,a,140
TR a,244,247,4096
MA v,104,v,192,a,144
TR a,246,247,6144
MA v,104,v,188,a,124
TR a,248,247,8192
MA v,104,v,184,a,104
TR a,250,247,10240
CV 122,100,101
MA v,104,v,180,a,84
buffer_load_dwordx4 v246, s[48:51], s70 offen offset:3072 lds
MA v,108,v,180,a,88
TR a,252,247,12288
s_add_u32 m0, s64, 114688
MA v,108,v,184,a,108
TR a,254,247,14336
s_add_u32 s67, s13, 393216
MA v,108,v,188,a,128
v_xor_b32 v247, 0x4000, v247
CV 123,102,103
MA v,108,v,192,a,148
CV 126,104,105
CV 127,106,107
MA v,112,v,192,a,152
buffer_load_dwordx4 v243, s[48:51], s67 offen offset:0 lds
MA v,112,v,188,a,132
MA v,112,v,184,a,112
CV 130,108,109
CV 131,110,111
MA v,112,v,180,a,92
MA v,116,v,180,a,96
MA v,116,v,184,a,116
buffer_load_dwordx4 v244, s[48:51], s67 offen offset:1024 lds
MA v,116,v,188,a,136
CV 134,112,113
CV 135,114,115
MA v,116,v,192,a,156
s_waitcnt lgkmcnt(0)
MZ v,80,a,240,a,160
TR v,180,248,0
s_lshl1_add_u32 s74, s11, s21
MA v,80,a,244,a,180
TR v,182,248,2048
MA v,80,a,248,a,200
buffer_load_dwordx4 v245, s[48:51], s67 offen offset:2048 lds
)ASM"
R"ASM(MA v,80,a,252,a,220
TR v,184,248,4096
CV 138,116,117
MZ v,84,a,252,a,224
TR v,186,248,6144
CV 139,118,119
MA v,84,a,248,a,204
TR v,188,248,8192
MA v,84,a,244,a,184
TR v,190,248,10240
MA v,84,a,240,a,164
buffer_load_dwordx4 v246, s[48:51], s67 offen offset:3072 lds
MZ v,88,a,240,a,168
TR v,192,248,12288
MA v,88,a,244,a,188
TR v,194,248,14336
MA v,88,a,248,a,208
s_waitcnt vmcnt(9)
v_cmp_ne_u64 vcc, s[96:97], v[222:223]
MA v,88,a,252,a,228
s_cbranch_vccnz SPIN1%=
GOT1%=:
MZ v,92,a,252,a,232
buffer_store_dwordx4 v[120:123], v249, s[52:55], s72 offen offset:0 sc1
MA v,92,a,248,a,212
v_xor_b32 v248, 0x4000, v248
MA v,92,a,244,a,192
MA v,92,a,240,a,172
MZ v,96,a,240,a,176
MA v,96,a,244,a,196
buffer_load_dwordx4 v[196:199], v249, s[52:55], s73 offen offset:0 sc1
MA v,96,a,248,a,216
MA v,96,a,252,a,236
s_waitcnt lgkmcnt(0)
MZ v,100,v,180,a,160
TR a,240,247,0
MA v,100,v,184,a,180
TR a,242,247,2048
MA v,100,v,188,a,200
buffer_store_dwordx4 v[124:127], v249, s[52:55], s72 offen offset:1024 sc1
MA v,100,v,192,a,220
TR a,244,247,4096
MZ v,104,v,192,a,224
TR a,246,247,6144
MA v,104,v,188,a,204
TR a,248,247,8192
MA v,104,v,184,a,184
TR a,250,247,10240
MA v,104,v,180,a,164
buffer_load_dwordx4 v[200:203], v249, s[52:55], s73 offen offset:1024 sc1
MZ v,108,v,180,a,168
TR a,252,247,12288
MA v,108,v,184,a,188
TR a,254,247,14336
MA v,108,v,188,a,208
v_xor_b32 v247, 0x14000, v247
MA v,108,v,192,a,228
MZ v,112,v,192,a,232
buffer_load_dwordx4 v[204:207], v249, s[52:55], s73 offen offset:2048 sc1
MA v,112,v,188,a,212
MA v,112,v,184,a,192
MA v,112,v,180,a,172
MZ v,116,v,180,a,176
MA v,116,v,184,a,196
buffer_load_dwordx4 v[208:211], v249, s[52:55], s73 offen offset:3072 sc1
MA v,116,v,188,a,216
MA v,116,v,192,a,236
s_waitcnt lgkmcnt(0)
MA v,80,a,240,a,0
TR v,180,248,0
MA v,80,a,244,a,20
TR v,182,248,2048
MA v,80,a,248,a,40
buffer_store_dwordx4 v[128:131], v249, s[52:55], s72 offen offset:2048 sc1
MA v,80,a,252,a,60
TR v,184,248,4096
MA v,84,a,252,a,64
TR v,186,248,6144
MA v,84,a,248,a,44
TR v,188,248,8192
MA v,84,a,244,a,24
TR v,190,248,10240
MA v,84,a,240,a,4
buffer_load_dwordx4 v[212:215], v250, s[52:55], s73 offen offset:0 sc1
MA v,88,a,240,a,8
TR v,192,248,12288
s_mul_i32 s73, s12, 160
MA v,88,a,244,a,28
TR v,194,248,14336
s_add_u32 s73, s73, s22
MA v,88,a,248,a,48
s_lshr_b32 s75, s12, 4
s_add_u32 s75, s75, s23
MA v,88,a,252,a,68
v_xor_b32 v248, 0x14000, v248
MA v,92,a,252,a,72
buffer_store_dwordx4 v[132:135], v249, s[52:55], s72 offen offset:3072 sc1
MA v,92,a,248,a,52
MA v,92,a,244,a,32
MA v,92,a,240,a,12
MA v,96,a,240,a,16
s_add_u32 m0, s64, 65536
s_add_u32 s68, s14, 0
MA v,96,a,244,a,36
buffer_store_dwordx4 v[136:139], v250, s[52:55], s72 offen offset:0 sc1
MA v,96,a,248,a,56
MA v,96,a,252,a,76
s_waitcnt lgkmcnt(0)
s_barrier
MA v,100,v,180,a,0
TR a,240,247,0
MA v,100,v,184,a,20
TR a,242,247,2048
MA v,100,v,188,a,40
buffer_load_dwordx4 v243, s[48:51], s68 offen offset:0 lds
MA v,100,v,192,a,60
TR a,244,247,4096
MA v,104,v,192,a,64
TR a,246,247,6144
MA v,104,v,188,a,44
TR a,248,247,8192
MA v,104,v,184,a,24
TR a,250,247,10240
MA v,104,v,180,a,4
buffer_load_dwordx4 v244, s[48:51], s68 offen offset:1024 lds
MA v,108,v,180,a,8
TR a,252,247,12288
MA v,108,v,184,a,28
TR a,254,247,14336
MA v,108,v,188,a,48
v_xor_b32 v247, 0x4000, v247
MA v,108,v,192,a,68
buffer_load_dwordx2 v[222:223], off, s[60:63], s75 sc1
MA v,112,v,192,a,72
buffer_load_dwordx4 v245, s[48:51], s68 offen offset:2048 lds
MA v,112,v,188,a,52
MA v,112,v,184,a,32
MA v,112,v,180,a,12
MA v,116,v,180,a,16
MA v,116,v,184,a,36
buffer_load_dwordx4 v246, s[48:51], s68 offen offset:3072 lds
MA v,116,v,188,a,56
s_add_u32 m0, s64, 81920
s_add_u32 s69, s14, 131072
MA v,116,v,192,a,76
s_waitcnt lgkmcnt(0)
MA v,80,a,240,v,0
TR v,180,248,0
MA v,80,a,244,v,20
TR v,182,248,2048
MA v,80,a,248,v,40
buffer_load_dwordx4 v243, s[48:51], s69 offen offset:0 lds
MA v,80,a,252,v,60
TR v,184,248,4096
MA v,84,a,252,v,64
TR v,186,248,6144
MA v,84,a,248,v,44
TR v,188,248,8192
MA v,84,a,244,v,24
TR v,190,248,10240
MA v,84,a,240,v,4
buffer_load_dwordx4 v244, s[48:51], s69 offen offset:1024 lds
MA v,88,a,240,v,8
TR v,192,248,12288
MA v,88,a,244,v,28
TR v,194,248,14336
MA v,88,a,248,v,48
v_xor_b32 v248, 0x4000, v248
MA v,88,a,252,v,68
MA v,92,a,252,v,72
buffer_load_dwordx4 v245, s[48:51], s69 offen offset:2048 lds
MA v,92,a,248,v,52
MA v,92,a,244,v,32
MA v,92,a,240,v,12
MA v,96,a,240,v,16
MA v,96,a,244,v,36
buffer_load_dwordx4 v246, s[48:51], s69 offen offset:3072 lds
MA v,96,a,248,v,56
s_waitcnt vmcnt(11)
MA v,96,a,252,v,76
MA v,140,v,218,v,196
MA v,160,v,216,v,196
MA v,144,v,218,v,200
MA v,164,v,216,v,200
MA v,148,v,218,v,204
MA v,168,v,216,v,204
MA v,152,v,218,v,208
MA v,172,v,216,v,208
MA v,156,v,218,v,212
MA v,176,v,216,v,212
s_waitcnt lgkmcnt(0)
MA v,100,v,180,v,0
TR a,240,247,0
MA v,100,v,184,v,20
TR a,242,247,2048
MA v,100,v,188,v,40
MA v,100,v,192,v,60
TR a,244,247,4096
MA v,104,v,192,v,64
TR a,246,247,6144
MA v,104,v,188,v,44
TR a,248,247,8192
MA v,104,v,184,v,24
TR a,250,247,10240
MA v,104,v,180,v,4
MA v,108,v,180,v,8
TR a,252,247,12288
MA v,108,v,184,v,28
TR a,254,247,14336
MA v,108,v,188,v,48
v_xor_b32 v247, 0x4000, v247
MA v,108,v,192,v,68
MA v,112,v,192,v,72
MA v,112,v,188,v,52
MA v,112,v,184,v,32
MA v,112,v,180,v,12
MA v,116,v,180,v,16
s_waitcnt vmcnt(9)
MA v,116,v,184,v,36
buffer_store_dwordx2 v[224:225], v254, s[60:63], s74 offen sc1
MA v,116,v,188,v,56
MA v,116,v,192,v,76
s_waitcnt lgkmcnt(0)
MA v,80,a,240,a,80
TR v,180,248,0
MA v,80,a,244,a,100
TR v,182,248,2048
MA v,80,a,248,a,120
MA v,80,a,252,a,140
TR v,184,248,4096
MA v,84,a,252,a,144
TR v,186,248,6144
MA v,84,a,248,a,124
TR v,188,248,8192
MA v,84,a,244,a,104
TR v,190,248,10240
CV 120,80,81
MA v,84,a,240,a,84
CV 121,82,83
MA v,88,a,240,a,88
TR v,192,248,12288
s_mul_i32 s72, s12, 1280
MA v,88,a,244,a,108
TR v,194,248,14336
s_add_u32 s72, s72, s20
MA v,88,a,248,a,128
s_waitcnt vmcnt(7)
v_cmp_ne_u64 vcc, s[96:97], v[222:223]
MA v,88,a,252,a,148
s_cbranch_vccnz SPIN2%=
GOT2%=:
MA v,92,a,252,a,152
buffer_load_dwordx4 v[196:199], v249, s[52:55], s73 offen offset:0 sc1
MA v,92,a,248,a,132
v_xor_b32 v248, 0x4000, v248
CV 124,84,85
MA v,92,a,244,a,112
CV 125,86,87
CV 128,88,89
MA v,92,a,240,a,92
CV 129,90,91
MA v,96,a,240,a,96
s_add_u32 m0, s64, 0
s_add_u32 s70, s14, 262144
MA v,96,a,244,a,116
buffer_load_dwordx4 v[200:203], v249, s[52:55], s73 offen offset:1024 sc1
MA v,96,a,248,a,136
CV 132,92,93
CV 133,94,95
MA v,96,a,252,a,156
s_waitcnt lgkmcnt(0)
s_barrier
MA v,100,v,180,a,80
TR a,240,247,32768
MA v,100,v,184,a,100
TR a,242,247,34816
MA v,100,v,188,a,120
buffer_load_dwordx4 v243, s[48:51], s70 offen offset:0 lds
MA v,100,v,192,a,140
TR a,244,247,36864
CV 136,96,97
MA v,104,v,192,a,144
TR a,246,247,38912
CV 137,98,99
MA v,104,v,188,a,124
TR a,248,247,40960
MA v,104,v,184,a,104
TR a,250,247,43008
CV 122,100,101
MA v,104,v,180,a,84
buffer_load_dwordx4 v244, s[48:51], s70 offen offset:1024 lds
MA v,108,v,180,a,88
TR a,252,247,45056
CV 123,102,103
MA v,108,v,184,a,108
TR a,254,247,47104
MA v,108,v,188,a,128
v_xor_b32 v247, 0x4000, v247
CV 126,104,105
MA v,108,v,192,a,148
CV 127,106,107
MA v,112,v,192,a,152
buffer_load_dwordx4 v245, s[48:51], s70 offen offset:2048 lds
MA v,112,v,188,a,132
MA v,112,v,184,a,112
CV 130,108,109
CV 131,110,111
MA v,112,v,180,a,92
MA v,116,v,180,a,96
buffer_load_dwordx4 v[204:207], v249, s[52:55], s73 offen offset:2048 sc1
MA v,116,v,184,a,116
buffer_load_dwordx4 v246, s[48:51], s70 offen offset:3072 lds
MA v,116,v,188,a,136
s_add_u32 m0, s64, 16384
s_add_u32 s67, s14, 393216
MA v,116,v,192,a,156
s_waitcnt lgkmcnt(0)
CV 134,112,113
MZ v,80,a,240,a,160
TR v,180,248,32768
CV 135,114,115
MA v,80,a,244,a,180
TR v,182,248,34816
s_lshl1_add_u32 s74, s12, s21
MA v,80,a,248,a,200
buffer_load_dwordx4 v243, s[48:51], s67 offen offset:0 lds
MA v,80,a,252,a,220
TR v,184,248,36864
CV 138,116,117
MZ v,84,a,252,a,224
TR v,186,248,38912
buffer_load_dwordx4 v[208:211], v249, s[52:55], s73 offen offset:3072 sc1
MA v,84,a,248,a,204
TR v,188,248,40960
CV 139,118,119
MA v,84,a,244,a,184
TR v,190,248,43008
MA v,84,a,240,a,164
buffer_load_dwordx4 v244, s[48:51], s67 offen offset:1024 lds
MZ v,88,a,240,a,168
TR v,192,248,45056
MA v,88,a,244,a,188
TR v,194,248,47104
buffer_load_dwordx4 v[212:215], v250, s[52:55], s73 offen offset:0 sc1
MA v,88,a,248,a,208
v_xor_b32 v248, 0x4000, v248
MA v,88,a,252,a,228
MZ v,92,a,252,a,232
buffer_load_dwordx4 v245, s[48:51], s67 offen offset:2048 lds
MA v,92,a,248,a,212
MA v,92,a,244,a,192
MA v,92,a,240,a,172
MZ v,96,a,240,a,176
MA v,96,a,244,a,196
buffer_load_dwordx4 v246, s[48:51], s67 offen offset:3072 lds
MA v,96,a,248,a,216
MA v,96,a,252,a,236
s_waitcnt lgkmcnt(0)
MZ v,100,v,180,a,160
TR a,240,247,32768
MA v,100,v,184,a,180
TR a,242,247,34816
MA v,100,v,188,a,200
buffer_store_dwordx4 v[120:123], v249, s[52:55], s72 offen offset:0 sc1
MA v,100,v,192,a,220
TR a,244,247,36864
MZ v,104,v,192,a,224
TR a,246,247,38912
MA v,104,v,188,a,204
TR a,248,247,40960
MA v,104,v,184,a,184
TR a,250,247,43008
MA v,104,v,180,a,164
buffer_store_dwordx4 v[124:127], v249, s[52:55], s72 offen offset:1024 sc1
MZ v,108,v,180,a,168
TR a,252,247,45056
MA v,108,v,184,a,188
TR a,254,247,47104
MA v,108,v,188,a,208
v_xor_b32 v247, 0x14000, v247
MA v,108,v,192,a,228
MZ v,112,v,192,a,232
buffer_store_dwordx4 v[128:131], v249, s[52:55], s72 offen offset:2048 sc1
MA v,112,v,188,a,212
MA v,112,v,184,a,192
MA v,112,v,180,a,172
MZ v,116,v,180,a,176
MA v,116,v,184,a,196
MA v,116,v,188,a,216
MA v,116,v,192,a,236
s_waitcnt lgkmcnt(0)
MA v,80,a,240,a,0
TR v,180,248,32768
MA v,80,a,244,a,20
TR v,182,248,34816
MA v,80,a,248,a,40
MA v,80,a,252,a,60
TR v,184,248,36864
MA v,84,a,252,a,64
TR v,186,248,38912
MA v,84,a,248,a,44
TR v,188,248,40960
MA v,84,a,244,a,24
TR v,190,248,43008
MA v,84,a,240,a,4
buffer_store_dwordx4 v[132:135], v249, s[52:55], s72 offen offset:3072 sc1
MA v,88,a,240,a,8
TR v,192,248,45056
s_mul_i32 s73, s11, 160
MA v,88,a,244,a,28
TR v,194,248,47104
s_add_u32 s73, s73, s22
MA v,88,a,248,a,48
s_lshr_b32 s75, s11, 4
s_add_u32 s75, s75, s23
MA v,88,a,252,a,68
v_xor_b32 v248, 0x14000, v248
MA v,92,a,252,a,72
MA v,92,a,248,a,52
MA v,92,a,244,a,32
MA v,92,a,240,a,12
MA v,96,a,240,a,16
s_add_u32 m0, s64, 32768
s_add_u32 s68, s15, 0
MA v,96,a,244,a,36
MA v,96,a,248,a,56
s_waitcnt vmcnt(6)
MA v,96,a,252,a,76
s_waitcnt lgkmcnt(0)
s_barrier
MA v,140,v,218,v,196
buffer_load_dwordx4 v243, s[48:51], s68 offen offset:0 lds
MA v,160,v,216,v,196
MA v,144,v,218,v,200
MA v,164,v,216,v,200
MA v,148,v,218,v,204
MA v,168,v,216,v,204
buffer_load_dwordx4 v244, s[48:51], s68 offen offset:1024 lds
MA v,152,v,218,v,208
MA v,172,v,216,v,208
MA v,156,v,218,v,212
MA v,176,v,216,v,212
buffer_store_dwordx4 v[136:139], v250, s[52:55], s72 offen offset:0 sc1
MA v,100,v,180,a,0
TR a,240,247,32768
MA v,100,v,184,a,20
TR a,242,247,34816
MA v,100,v,188,a,40
buffer_load_dwordx4 v245, s[48:51], s68 offen offset:2048 lds
MA v,100,v,192,a,60
TR a,244,247,36864
MA v,104,v,192,a,64
TR a,246,247,38912
MA v,104,v,188,a,44
TR a,248,247,40960
MA v,104,v,184,a,24
TR a,250,247,43008
MA v,104,v,180,a,4
buffer_load_dwordx4 v246, s[48:51], s68 offen offset:3072 lds
MA v,108,v,180,a,8
TR a,252,247,45056
s_add_u32 m0, s64, 49152
MA v,108,v,184,a,28
TR a,254,247,47104
s_add_u32 s69, s15, 131072
MA v,108,v,188,a,48
v_xor_b32 v247, 0x4000, v247
MA v,108,v,192,a,68
buffer_load_dwordx2 v[222:223], off, s[60:63], s75 sc1
MA v,112,v,192,a,72
buffer_load_dwordx4 v243, s[48:51], s69 offen offset:0 lds
MA v,112,v,188,a,52
MA v,112,v,184,a,32
MA v,112,v,180,a,12
MA v,116,v,180,a,16
MA v,116,v,184,a,36
buffer_load_dwordx4 v244, s[48:51], s69 offen offset:1024 lds
MA v,116,v,188,a,56
MA v,116,v,192,a,76
s_waitcnt lgkmcnt(0)
MA v,80,a,240,v,0
TR v,180,248,32768
MA v,80,a,244,v,20
TR v,182,248,34816
MA v,80,a,248,v,40
buffer_load_dwordx4 v245, s[48:51], s69 offen offset:2048 lds
MA v,80,a,252,v,60
TR v,184,248,36864
MA v,84,a,252,v,64
TR v,186,248,38912
MA v,84,a,248,v,44
TR v,188,248,40960
MA v,84,a,244,v,24
TR v,190,248,43008
MA v,84,a,240,v,4
buffer_load_dwordx4 v246, s[48:51], s69 offen offset:3072 lds
)ASM"
R"ASM(MA v,88,a,240,v,8
TR v,192,248,45056
MA v,88,a,244,v,28
TR v,194,248,47104
MA v,88,a,248,v,48
v_xor_b32 v248, 0x4000, v248
MA v,88,a,252,v,68
MA v,92,a,252,v,72
MA v,92,a,248,v,52
MA v,92,a,244,v,32
MA v,92,a,240,v,12
MA v,96,a,240,v,16
MA v,96,a,244,v,36
MA v,96,a,248,v,56
MA v,96,a,252,v,76
s_waitcnt lgkmcnt(0)
MA v,100,v,180,v,0
TR a,240,247,32768
MA v,100,v,184,v,20
TR a,242,247,34816
MA v,100,v,188,v,40
MA v,100,v,192,v,60
TR a,244,247,36864
MA v,104,v,192,v,64
TR a,246,247,38912
MA v,104,v,188,v,44
TR a,248,247,40960
MA v,104,v,184,v,24
TR a,250,247,43008
MA v,104,v,180,v,4
MA v,108,v,180,v,8
TR a,252,247,45056
MA v,108,v,184,v,28
TR a,254,247,47104
MA v,108,v,188,v,48
v_xor_b32 v247, 0x4000, v247
MA v,108,v,192,v,68
MA v,112,v,192,v,72
MA v,112,v,188,v,52
MA v,112,v,184,v,32
MA v,112,v,180,v,12
MA v,116,v,180,v,16
s_waitcnt vmcnt(7)
MA v,116,v,184,v,36
buffer_store_dwordx2 v[224:225], v254, s[60:63], s74 offen sc1
MA v,116,v,188,v,56
MA v,116,v,192,v,76
s_waitcnt lgkmcnt(0)
MA v,80,a,240,a,80
TR v,180,248,32768
MA v,80,a,244,a,100
TR v,182,248,34816
MA v,80,a,248,a,120
MA v,80,a,252,a,140
TR v,184,248,36864
MA v,84,a,252,a,144
TR v,186,248,38912
MA v,84,a,248,a,124
TR v,188,248,40960
MA v,84,a,244,a,104
TR v,190,248,43008
CV 120,80,81
MA v,84,a,240,a,84
CV 121,82,83
MA v,88,a,240,a,88
TR v,192,248,45056
s_mul_i32 s72, s13, 1280
MA v,88,a,244,a,108
TR v,194,248,47104
s_add_u32 s72, s72, s20
MA v,88,a,248,a,128
s_waitcnt vmcnt(5)
v_cmp_ne_u64 vcc, s[96:97], v[222:223]
MA v,88,a,252,a,148
s_cbranch_vccnz SPIN3%=
GOT3%=:
MA v,92,a,252,a,152
buffer_load_dwordx4 v[196:199], v249, s[52:55], s73 offen offset:0 sc1
MA v,92,a,248,a,132
v_xor_b32 v248, 0x4000, v248
CV 124,84,85
MA v,92,a,244,a,112
CV 125,86,87
CV 128,88,89
MA v,92,a,240,a,92
CV 129,90,91
MA v,96,a,240,a,96
s_add_u32 m0, s64, 98304
s_add_u32 s70, s15, 262144
MA v,96,a,244,a,116
buffer_load_dwordx4 v[200:203], v249, s[52:55], s73 offen offset:1024 sc1
MA v,96,a,248,a,136
CV 132,92,93
CV 133,94,95
MA v,96,a,252,a,156
s_waitcnt lgkmcnt(0)
s_barrier
MA v,100,v,180,a,80
TR a,240,247,0
MA v,100,v,184,a,100
TR a,242,247,2048
MA v,100,v,188,a,120
buffer_load_dwordx4 v243, s[48:51], s70 offen offset:0 lds
MA v,100,v,192,a,140
TR a,244,247,4096
CV 136,96,97
MA v,104,v,192,a,144
TR a,246,247,6144
CV 137,98,99
MA v,104,v,188,a,124
TR a,248,247,8192
MA v,104,v,184,a,104
TR a,250,247,10240
CV 122,100,101
MA v,104,v,180,a,84
buffer_load_dwordx4 v244, s[48:51], s70 offen offset:1024 lds
MA v,108,v,180,a,88
TR a,252,247,12288
CV 123,102,103
MA v,108,v,184,a,108
TR a,254,247,14336
MA v,108,v,188,a,128
v_xor_b32 v247, 0x4000, v247
CV 126,104,105
MA v,108,v,192,a,148
CV 127,106,107
MA v,112,v,192,a,152
buffer_load_dwordx4 v245, s[48:51], s70 offen offset:2048 lds
MA v,112,v,188,a,132
MA v,112,v,184,a,112
CV 130,108,109
CV 131,110,111
MA v,112,v,180,a,92
MA v,116,v,180,a,96
buffer_load_dwordx4 v[204:207], v249, s[52:55], s73 offen offset:2048 sc1
MA v,116,v,184,a,116
buffer_load_dwordx4 v246, s[48:51], s70 offen offset:3072 lds
MA v,116,v,188,a,136
s_add_u32 m0, s64, 114688
s_add_u32 s67, s15, 393216
MA v,116,v,192,a,156
s_waitcnt lgkmcnt(0)
CV 134,112,113
MZ v,80,a,240,a,160
TR v,180,248,0
CV 135,114,115
MA v,80,a,244,a,180
TR v,182,248,2048
s_lshl1_add_u32 s74, s13, s21
MA v,80,a,248,a,200
buffer_load_dwordx4 v243, s[48:51], s67 offen offset:0 lds
MA v,80,a,252,a,220
TR v,184,248,4096
CV 138,116,117
MZ v,84,a,252,a,224
TR v,186,248,6144
buffer_load_dwordx4 v[208:211], v249, s[52:55], s73 offen offset:3072 sc1
MA v,84,a,248,a,204
TR v,188,248,8192
CV 139,118,119
MA v,84,a,244,a,184
TR v,190,248,10240
MA v,84,a,240,a,164
buffer_load_dwordx4 v244, s[48:51], s67 offen offset:1024 lds
MZ v,88,a,240,a,168
TR v,192,248,12288
MA v,88,a,244,a,188
TR v,194,248,14336
buffer_load_dwordx4 v[212:215], v250, s[52:55], s73 offen offset:0 sc1
MA v,88,a,248,a,208
v_xor_b32 v248, 0x4000, v248
MA v,88,a,252,a,228
MZ v,92,a,252,a,232
buffer_load_dwordx4 v245, s[48:51], s67 offen offset:2048 lds
MA v,92,a,248,a,212
MA v,92,a,244,a,192
MA v,92,a,240,a,172
MZ v,96,a,240,a,176
MA v,96,a,244,a,196
buffer_load_dwordx4 v246, s[48:51], s67 offen offset:3072 lds
MA v,96,a,248,a,216
MA v,96,a,252,a,236
s_waitcnt lgkmcnt(0)
MZ v,100,v,180,a,160
TR a,240,247,0
MA v,100,v,184,a,180
TR a,242,247,2048
MA v,100,v,188,a,200
buffer_store_dwordx4 v[120:123], v249, s[52:55], s72 offen offset:0 sc1
MA v,100,v,192,a,220
TR a,244,247,4096
MZ v,104,v,192,a,224
TR a,246,247,6144
MA v,104,v,188,a,204
TR a,248,247,8192
MA v,104,v,184,a,184
TR a,250,247,10240
MA v,104,v,180,a,164
buffer_store_dwordx4 v[124:127], v249, s[52:55], s72 offen offset:1024 sc1
MZ v,108,v,180,a,168
TR a,252,247,12288
MA v,108,v,184,a,188
TR a,254,247,14336
MA v,108,v,188,a,208
v_xor_b32 v247, 0x14000, v247
MA v,108,v,192,a,228
MZ v,112,v,192,a,232
buffer_store_dwordx4 v[128:131], v249, s[52:55], s72 offen offset:2048 sc1
MA v,112,v,188,a,212
MA v,112,v,184,a,192
MA v,112,v,180,a,172
MZ v,116,v,180,a,176
MA v,116,v,184,a,196
MA v,116,v,188,a,216
MA v,116,v,192,a,236
s_waitcnt lgkmcnt(0)
MA v,80,a,240,a,0
TR v,180,248,0
MA v,80,a,244,a,20
TR v,182,248,2048
MA v,80,a,248,a,40
MA v,80,a,252,a,60
TR v,184,248,4096
MA v,84,a,252,a,64
TR v,186,248,6144
MA v,84,a,248,a,44
TR v,188,248,8192
MA v,84,a,244,a,24
TR v,190,248,10240
MA v,84,a,240,a,4
buffer_store_dwordx4 v[132:135], v249, s[52:55], s72 offen offset:3072 sc1
MA v,88,a,240,a,8
TR v,192,248,12288
s_mul_i32 s73, s10, 160
MA v,88,a,244,a,28
TR v,194,248,14336
s_add_u32 s73, s73, s22
MA v,88,a,248,a,48
s_lshr_b32 s75, s10, 4
s_add_u32 s75, s75, s23
MA v,88,a,252,a,68
v_xor_b32 v248, 0x14000, v248
MA v,92,a,252,a,72
MA v,92,a,248,a,52
MA v,92,a,244,a,32
MA v,92,a,240,a,12
MA v,96,a,240,a,16
buffer_store_dwordx4 v[136:139], v250, s[52:55], s72 offen offset:0 sc1
MA v,96,a,244,a,36
MA v,96,a,248,a,56
s_waitcnt vmcnt(7)
MA v,96,a,252,a,76
s_waitcnt lgkmcnt(0)
s_barrier
MA v,140,v,218,v,196
MA v,160,v,216,v,196
MA v,144,v,218,v,200
MA v,164,v,216,v,200
MA v,148,v,218,v,204
MA v,168,v,216,v,204
MA v,152,v,218,v,208
MA v,172,v,216,v,208
MA v,156,v,218,v,212
MA v,176,v,216,v,212
MA v,100,v,180,a,0
TR a,240,247,0
MA v,100,v,184,a,20
TR a,242,247,2048
MA v,100,v,188,a,40
MA v,100,v,192,a,60
TR a,244,247,4096
MA v,104,v,192,a,64
TR a,246,247,6144
MA v,104,v,188,a,44
TR a,248,247,8192
MA v,104,v,184,a,24
TR a,250,247,10240
MA v,104,v,180,a,4
MA v,108,v,180,a,8
TR a,252,247,12288
MA v,108,v,184,a,28
TR a,254,247,14336
MA v,108,v,188,a,48
buffer_load_dwordx2 v[222:223], off, s[60:63], s75 sc1
MA v,108,v,192,a,68
v_xor_b32 v247, 0x4000, v247
MA v,112,v,192,a,72
MA v,112,v,188,a,52
MA v,112,v,184,a,32
MA v,112,v,180,a,12
MA v,116,v,180,a,16
MA v,116,v,184,a,36
MA v,116,v,188,a,56
MA v,116,v,192,a,76
s_waitcnt lgkmcnt(0)
MA v,80,a,240,v,0
TR v,180,248,0
MA v,80,a,244,v,20
TR v,182,248,2048
MA v,80,a,248,v,40
MA v,80,a,252,v,60
TR v,184,248,4096
MA v,84,a,252,v,64
TR v,186,248,6144
MA v,84,a,248,v,44
TR v,188,248,8192
MA v,84,a,244,v,24
TR v,190,248,10240
MA v,84,a,240,v,4
MA v,88,a,240,v,8
TR v,192,248,12288
MA v,88,a,244,v,28
TR v,194,248,14336
MA v,88,a,248,v,48
v_xor_b32 v248, 0x4000, v248
MA v,88,a,252,v,68
MA v,92,a,252,v,72
MA v,92,a,248,v,52
MA v,92,a,244,v,32
MA v,92,a,240,v,12
MA v,96,a,240,v,16
MA v,96,a,244,v,36
MA v,96,a,248,v,56
MA v,96,a,252,v,76
s_waitcnt lgkmcnt(0)
MA v,100,v,180,v,0
TR a,240,247,0
MA v,100,v,184,v,20
TR a,242,247,2048
MA v,100,v,188,v,40
MA v,100,v,192,v,60
TR a,244,247,4096
MA v,104,v,192,v,64
TR a,246,247,6144
MA v,104,v,188,v,44
TR a,248,247,8192
MA v,104,v,184,v,24
TR a,250,247,10240
MA v,104,v,180,v,4
MA v,108,v,180,v,8
TR a,252,247,12288
MA v,108,v,184,v,28
TR a,254,247,14336
MA v,108,v,188,v,48
v_xor_b32 v247, 0x4000, v247
MA v,108,v,192,v,68
MA v,112,v,192,v,72
MA v,112,v,188,v,52
MA v,112,v,184,v,32
MA v,112,v,180,v,12
MA v,116,v,180,v,16
s_waitcnt vmcnt(1)
MA v,116,v,184,v,36
buffer_store_dwordx2 v[224:225], v254, s[60:63], s74 offen sc1
MA v,116,v,188,v,56
MA v,116,v,192,v,76
s_waitcnt lgkmcnt(0)
MA v,80,a,240,a,80
TR v,180,248,0
MA v,80,a,244,a,100
TR v,182,248,2048
MA v,80,a,248,a,120
MA v,80,a,252,a,140
TR v,184,248,4096
MA v,84,a,252,a,144
TR v,186,248,6144
MA v,84,a,248,a,124
TR v,188,248,8192
MA v,84,a,244,a,104
TR v,190,248,10240
CV 120,80,81
MA v,84,a,240,a,84
CV 121,82,83
MA v,88,a,240,a,88
TR v,192,248,12288
s_mul_i32 s72, s14, 1280
MA v,88,a,244,a,108
TR v,194,248,14336
s_add_u32 s72, s72, s20
MA v,88,a,248,a,128
s_waitcnt vmcnt(1)
v_cmp_ne_u64 vcc, s[96:97], v[222:223]
MA v,88,a,252,a,148
s_cbranch_vccnz SPIN4%=
GOT4%=:
MA v,92,a,252,a,152
buffer_load_dwordx4 v[196:199], v249, s[52:55], s73 offen offset:0 sc1
MA v,92,a,248,a,132
v_xor_b32 v248, 0x4000, v248
CV 124,84,85
MA v,92,a,244,a,112
CV 125,86,87
CV 128,88,89
MA v,92,a,240,a,92
CV 129,90,91
MA v,96,a,240,a,96
MA v,96,a,244,a,116
buffer_load_dwordx4 v[200:203], v249, s[52:55], s73 offen offset:1024 sc1
MA v,96,a,248,a,136
CV 132,92,93
CV 133,94,95
MA v,96,a,252,a,156
s_waitcnt lgkmcnt(0)
s_barrier
MA v,100,v,180,a,80
TR a,240,247,32768
MA v,100,v,184,a,100
TR a,242,247,34816
MA v,100,v,188,a,120
buffer_load_dwordx4 v[204:207], v249, s[52:55], s73 offen offset:2048 sc1
MA v,100,v,192,a,140
TR a,244,247,36864
CV 136,96,97
MA v,104,v,192,a,144
TR a,246,247,38912
CV 137,98,99
MA v,104,v,188,a,124
TR a,248,247,40960
MA v,104,v,184,a,104
TR a,250,247,43008
CV 122,100,101
MA v,104,v,180,a,84
buffer_load_dwordx4 v[208:211], v249, s[52:55], s73 offen offset:3072 sc1
MA v,108,v,180,a,88
TR a,252,247,45056
CV 123,102,103
MA v,108,v,184,a,108
TR a,254,247,47104
MA v,108,v,188,a,128
v_xor_b32 v247, 0x4000, v247
CV 126,104,105
MA v,108,v,192,a,148
CV 127,106,107
MA v,112,v,192,a,152
buffer_load_dwordx4 v[212:215], v250, s[52:55], s73 offen offset:0 sc1
MA v,112,v,188,a,132
MA v,112,v,184,a,112
CV 130,108,109
CV 131,110,111
MA v,112,v,180,a,92
MA v,116,v,180,a,96
MA v,116,v,184,a,116
MA v,116,v,188,a,136
CV 134,112,113
CV 135,114,115
MA v,116,v,192,a,156
s_waitcnt lgkmcnt(0)
MA v,140,a,240,a,160
TR v,180,248,32768
s_lshl1_add_u32 s74, s14, s21
MA v,140,a,244,a,180
TR v,182,248,34816
MA v,140,a,248,a,200
CV 138,116,117
CV 139,118,119
MA v,140,a,252,a,220
TR v,184,248,36864
MA v,144,a,252,a,224
TR v,186,248,38912
MA v,144,a,248,a,204
TR v,188,248,40960
MA v,144,a,244,a,184
TR v,190,248,43008
MA v,144,a,240,a,164
MA v,148,a,240,a,168
TR v,192,248,45056
s_mul_i32 s73, s9, 160
MA v,148,a,244,a,188
TR v,194,248,47104
s_add_u32 s73, s73, s22
MA v,148,a,248,a,208
buffer_store_dwordx4 v[120:123], v249, s[52:55], s72 offen offset:0 sc1
MA v,148,a,252,a,228
s_lshr_b32 s75, s9, 4
s_add_u32 s75, s75, s23
MA v,152,a,252,a,232
v_xor_b32 v248, 0x4000, v248
MA v,152,a,248,a,212
MA v,152,a,244,a,192
MA v,152,a,240,a,172
MA v,156,a,240,a,176
MA v,156,a,244,a,196
MA v,156,a,248,a,216
MA v,156,a,252,a,236
s_waitcnt lgkmcnt(0)
MA v,160,v,180,a,160
TR a,240,247,32768
MA v,160,v,184,a,180
TR a,242,247,34816
MA v,160,v,188,a,200
buffer_store_dwordx4 v[124:127], v249, s[52:55], s72 offen offset:1024 sc1
MA v,160,v,192,a,220
TR a,244,247,36864
MA v,164,v,192,a,224
TR a,246,247,38912
MA v,164,v,188,a,204
TR a,248,247,40960
MA v,164,v,184,a,184
TR a,250,247,43008
MA v,164,v,180,a,164
MA v,168,v,180,a,168
TR a,252,247,45056
MA v,168,v,184,a,188
TR a,254,247,47104
MA v,168,v,188,a,208
buffer_load_dwordx2 v[222:223], off, s[60:63], s75 sc1
MA v,168,v,192,a,228
v_xor_b32 v247, 0x14000, v247
MA v,172,v,192,a,232
MA v,172,v,188,a,212
MA v,172,v,184,a,192
MA v,172,v,180,a,172
buffer_store_dwordx4 v[128:131], v249, s[52:55], s72 offen offset:2048 sc1
MA v,176,v,180,a,176
MA v,176,v,184,a,196
MA v,176,v,188,a,216
MA v,176,v,192,a,236
s_waitcnt lgkmcnt(0)
MA v,140,a,240,a,0
TR v,180,248,32768
MA v,140,a,244,a,20
TR v,182,248,34816
MA v,140,a,248,a,40
MA v,140,a,252,a,60
TR v,184,248,36864
MA v,144,a,252,a,64
TR v,186,248,38912
MA v,144,a,248,a,44
TR v,188,248,40960
MA v,144,a,244,a,24
TR v,190,248,43008
MA v,144,a,240,a,4
buffer_store_dwordx4 v[132:135], v249, s[52:55], s72 offen offset:3072 sc1
MA v,148,a,240,a,8
)ASM"
R"ASM(TR v,192,248,45056
MA v,148,a,244,a,28
TR v,194,248,47104
MA v,148,a,248,a,48
v_xor_b32 v248, 0x14000, v248
MA v,148,a,252,a,68
MA v,152,a,252,a,72
MA v,152,a,248,a,52
MA v,152,a,244,a,32
MA v,152,a,240,a,12
MA v,156,a,240,a,16
buffer_store_dwordx4 v[136:139], v250, s[52:55], s72 offen offset:0 sc1
MA v,156,a,244,a,36
MA v,156,a,248,a,56
s_waitcnt vmcnt(6)
MA v,156,a,252,a,76
s_waitcnt lgkmcnt(0)
s_barrier
MA v,140,v,218,v,196
MA v,160,v,216,v,196
MA v,144,v,218,v,200
MA v,164,v,216,v,200
MA v,148,v,218,v,204
MA v,168,v,216,v,204
MA v,152,v,218,v,208
MA v,172,v,216,v,208
MA v,156,v,218,v,212
MA v,176,v,216,v,212
MA v,160,v,180,a,0
TR a,240,247,32768
MA v,160,v,184,a,20
TR a,242,247,34816
MA v,160,v,188,a,40
MA v,160,v,192,a,60
TR a,244,247,36864
MA v,164,v,192,a,64
TR a,246,247,38912
MA v,164,v,188,a,44
TR a,248,247,40960
MA v,164,v,184,a,24
TR a,250,247,43008
MA v,164,v,180,a,4
MA v,168,v,180,a,8
TR a,252,247,45056
MA v,168,v,184,a,28
TR a,254,247,47104
MA v,168,v,188,a,48
v_xor_b32 v247, 0x4000, v247
MA v,168,v,192,a,68
MA v,172,v,192,a,72
MA v,172,v,188,a,52
MA v,172,v,184,a,32
MA v,172,v,180,a,12
MA v,176,v,180,a,16
MA v,176,v,184,a,36
MA v,176,v,188,a,56
MA v,176,v,192,a,76
s_waitcnt lgkmcnt(0)
MA v,140,a,240,v,0
TR v,180,248,32768
MA v,140,a,244,v,20
TR v,182,248,34816
MA v,140,a,248,v,40
MA v,140,a,252,v,60
TR v,184,248,36864
MA v,144,a,252,v,64
TR v,186,248,38912
MA v,144,a,248,v,44
TR v,188,248,40960
MA v,144,a,244,v,24
TR v,190,248,43008
MA v,144,a,240,v,4
MA v,148,a,240,v,8
TR v,192,248,45056
MA v,148,a,244,v,28
TR v,194,248,47104
MA v,148,a,248,v,48
s_waitcnt vmcnt(3)
v_cmp_ne_u64 vcc, s[96:97], v[222:223]
MA v,148,a,252,v,68
s_cbranch_vccnz SPIN5%=
GOT5%=:
MA v,152,a,252,v,72
buffer_load_dwordx4 v[196:199], v249, s[52:55], s73 offen offset:0 sc1
MA v,152,a,248,v,52
v_xor_b32 v248, 0x4000, v248
MA v,152,a,244,v,32
MA v,152,a,240,v,12
MA v,156,a,240,v,16
MA v,156,a,244,v,36
buffer_load_dwordx4 v[200:203], v249, s[52:55], s73 offen offset:1024 sc1
MA v,156,a,248,v,56
MA v,156,a,252,v,76
s_waitcnt lgkmcnt(0)
MA v,160,v,180,v,0
TR a,240,247,32768
MA v,160,v,184,v,20
TR a,242,247,34816
s_waitcnt vmcnt(2)
MA v,160,v,188,v,40
buffer_store_dwordx2 v[224:225], v254, s[60:63], s74 offen sc1
MA v,160,v,192,v,60
TR a,244,247,36864
MA v,164,v,192,v,64
TR a,246,247,38912
MA v,164,v,188,v,44
TR a,248,247,40960
MA v,164,v,184,v,24
TR a,250,247,43008
MA v,164,v,180,v,4
buffer_load_dwordx4 v[204:207], v249, s[52:55], s73 offen offset:2048 sc1
MA v,168,v,180,v,8
TR a,252,247,45056
MA v,168,v,184,v,28
TR a,254,247,47104
MA v,168,v,188,v,48
MA v,168,v,192,v,68
MA v,172,v,192,v,72
buffer_load_dwordx4 v[208:211], v249, s[52:55], s73 offen offset:3072 sc1
MA v,172,v,188,v,52
MA v,172,v,184,v,32
MA v,172,v,180,v,12
MA v,176,v,180,v,16
MA v,176,v,184,v,36
buffer_load_dwordx4 v[212:215], v250, s[52:55], s73 offen offset:0 sc1
MA v,176,v,188,v,56
MA v,176,v,192,v,76
s_waitcnt lgkmcnt(0)
MA v,140,a,240,a,80
TR v,180,248,32768
MA v,140,a,244,a,100
TR v,182,248,34816
MA v,140,a,248,a,120
MA v,140,a,252,a,140
TR v,184,248,36864
MA v,144,a,252,a,144
TR v,186,248,38912
MA v,144,a,248,a,124
TR v,188,248,40960
MA v,144,a,244,a,104
TR v,190,248,43008
MA v,144,a,240,a,84
MA v,148,a,240,a,88
TR v,192,248,45056
MA v,148,a,244,a,108
TR v,194,248,47104
MA v,148,a,248,a,128
s_mul_i32 s73, s8, 160
s_add_u32 s73, s73, s22
MA v,148,a,252,a,148
s_lshr_b32 s75, s8, 4
s_add_u32 s75, s75, s23
MA v,152,a,252,a,152
MA v,152,a,248,a,132
MA v,152,a,244,a,112
MA v,152,a,240,a,92
MA v,156,a,240,a,96
MA v,156,a,244,a,116
MA v,156,a,248,a,136
buffer_load_dwordx2 v[222:223], off, s[60:63], s75 sc1
MA v,156,a,252,a,156
s_waitcnt lgkmcnt(0)
MA v,160,v,180,a,80
MA v,160,v,184,a,100
MA v,160,v,188,a,120
MA v,160,v,192,a,140
MA v,164,v,192,a,144
MA v,164,v,188,a,124
MA v,164,v,184,a,104
MA v,164,v,180,a,84
MA v,168,v,180,a,88
MA v,168,v,184,a,108
MA v,168,v,188,a,128
MA v,168,v,192,a,148
MA v,172,v,192,a,152
MA v,172,v,188,a,132
MA v,172,v,184,a,112
MA v,172,v,180,a,92
MA v,176,v,180,a,96
MA v,176,v,184,a,116
MA v,176,v,188,a,136
s_waitcnt vmcnt(0)
v_cmp_ne_u64 vcc, s[96:97], v[222:223]
MA v,176,v,192,a,156
s_cbranch_vccnz SPIN6%=
GOT6%=:
MA v,140,v,218,v,196
buffer_load_dwordx4 v[80:83], v249, s[52:55], s73 offen offset:0 sc1
MA v,160,v,216,v,196
buffer_load_dwordx4 v[84:87], v249, s[52:55], s73 offen offset:1024 sc1
MA v,144,v,218,v,200
buffer_load_dwordx4 v[88:91], v249, s[52:55], s73 offen offset:2048 sc1
MA v,164,v,216,v,200
buffer_load_dwordx4 v[92:95], v249, s[52:55], s73 offen offset:3072 sc1
MA v,148,v,218,v,204
buffer_load_dwordx4 v[96:99], v250, s[52:55], s73 offen offset:0 sc1
MA v,168,v,216,v,204
MA v,152,v,218,v,208
MA v,172,v,216,v,208
MA v,156,v,218,v,212
MA v,176,v,216,v,212
s_waitcnt vmcnt(4)
MA v,140,v,218,v,80
MA v,160,v,216,v,80
s_waitcnt vmcnt(3)
MA v,144,v,218,v,84
MA v,164,v,216,v,84
s_waitcnt vmcnt(2)
MA v,148,v,218,v,88
CV 120,140,141
CV 121,142,143
CV 122,160,161
CV 123,162,163
s_mov_b32 s31, 0
v_permlane16_swap_b32 v120, v122
v_permlane16_swap_b32 v121, v123
buffer_store_dwordx4 v[120:123], v253, s[56:59], s31 offen sc1
MA v,168,v,216,v,88
s_waitcnt vmcnt(2)
MA v,152,v,218,v,92
CV 124,144,145
CV 125,146,147
CV 126,164,165
CV 127,166,167
s_mov_b32 s31, 32768
v_permlane16_swap_b32 v124, v126
v_permlane16_swap_b32 v125, v127
buffer_store_dwordx4 v[124:127], v253, s[56:59], s31 offen sc1
MA v,172,v,216,v,92
s_waitcnt vmcnt(2)
MA v,156,v,218,v,96
CV 128,148,149
CV 129,150,151
CV 130,168,169
CV 131,170,171
s_mov_b32 s31, 65536
v_permlane16_swap_b32 v128, v130
v_permlane16_swap_b32 v129, v131
buffer_store_dwordx4 v[128:131], v253, s[56:59], s31 offen sc1
MA v,176,v,216,v,96
CV 132,152,153
CV 133,154,155
CV 134,172,173
CV 135,174,175
s_mov_b32 s31, 98304
v_permlane16_swap_b32 v132, v134
v_permlane16_swap_b32 v133, v135
buffer_store_dwordx4 v[132:135], v253, s[56:59], s31 offen sc1
CV 136,156,157
CV 137,158,159
CV 138,176,177
CV 139,178,179
s_mov_b32 s31, 131072
v_permlane16_swap_b32 v136, v138
v_permlane16_swap_b32 v137, v139
buffer_store_dwordx4 v[136:139], v253, s[56:59], s31 offen sc1
s_cmp_eq_u32 s28, 0
s_cbranch_scc0 NOEP%=
buffer_store_dwordx2 v[224:225], v254, s[60:63], 0 offen offset:2048 sc1
NOEP%=:
s_endpgm
PACE0_1%=:
s_sleep 1
s_memrealtime s[98:99]
s_waitcnt lgkmcnt(0)
s_sub_i32 s98, s98, s76
s_cmp_lt_i32 s98, 0
s_cbranch_scc1 PACE0_1%=
s_branch PACED0_1%=
PACE0_2%=:
s_sleep 1
s_memrealtime s[98:99]
s_waitcnt lgkmcnt(0)
s_sub_i32 s98, s98, s76
s_cmp_lt_i32 s98, 0
s_cbranch_scc1 PACE0_2%=
s_branch PACED0_2%=
PACE0_3%=:
s_sleep 1
s_memrealtime s[98:99]
s_waitcnt lgkmcnt(0)
s_sub_i32 s98, s98, s76
s_cmp_lt_i32 s98, 0
s_cbranch_scc1 PACE0_3%=
s_branch PACED0_3%=
PACE0_4%=:
s_sleep 1
s_memrealtime s[98:99]
s_waitcnt lgkmcnt(0)
s_sub_i32 s98, s98, s76
s_cmp_lt_i32 s98, 0
s_cbranch_scc1 PACE0_4%=
s_branch PACED0_4%=
PACE0_5%=:
s_sleep 1
s_memrealtime s[98:99]
s_waitcnt lgkmcnt(0)
s_sub_i32 s98, s98, s76
s_cmp_lt_i32 s98, 0
s_cbranch_scc1 PACE0_5%=
s_branch PACED0_5%=
PACE0_6%=:
s_sleep 1
s_memrealtime s[98:99]
s_waitcnt lgkmcnt(0)
s_sub_i32 s98, s98, s76
s_cmp_lt_i32 s98, 0
s_cbranch_scc1 PACE0_6%=
s_branch PACED0_6%=
PACE0_7%=:
s_sleep 1
s_memrealtime s[98:99]
s_waitcnt lgkmcnt(0)
s_sub_i32 s98, s98, s76
s_cmp_lt_i32 s98, 0
s_cbranch_scc1 PACE0_7%=
s_branch PACED0_7%=
PACE0_8%=:
s_sleep 1
s_memrealtime s[98:99]
s_waitcnt lgkmcnt(0)
s_sub_i32 s98, s98, s76
s_cmp_lt_i32 s98, 0
s_cbranch_scc1 PACE0_8%=
s_branch PACED0_8%=
PACE0_9%=:
s_sleep 1
s_memrealtime s[98:99]
s_waitcnt lgkmcnt(0)
s_sub_i32 s98, s98, s76
s_cmp_lt_i32 s98, 0
s_cbranch_scc1 PACE0_9%=
s_branch PACED0_9%=
PACE0_10%=:
s_sleep 1
s_memrealtime s[98:99]
s_waitcnt lgkmcnt(0)
s_sub_i32 s98, s98, s76
s_cmp_lt_i32 s98, 0
s_cbranch_scc1 PACE0_10%=
s_branch PACED0_10%=
PACE0_11%=:
s_sleep 1
s_memrealtime s[98:99]
s_waitcnt lgkmcnt(0)
s_sub_i32 s98, s98, s76
s_cmp_lt_i32 s98, 0
s_cbranch_scc1 PACE0_11%=
s_branch PACED0_11%=
PACE0_12%=:
s_sleep 1
s_memrealtime s[98:99]
s_waitcnt lgkmcnt(0)
s_sub_i32 s98, s98, s76
s_cmp_lt_i32 s98, 0
s_cbranch_scc1 PACE0_12%=
s_branch PACED0_12%=
PACE0_13%=:
s_sleep 1
s_memrealtime s[98:99]
s_waitcnt lgkmcnt(0)
s_sub_i32 s98, s98, s76
s_cmp_lt_i32 s98, 0
s_cbranch_scc1 PACE0_13%=
s_branch PACED0_13%=
PACE0_14%=:
s_sleep 1
s_memrealtime s[98:99]
s_waitcnt lgkmcnt(0)
s_sub_i32 s98, s98, s76
s_cmp_lt_i32 s98, 0
s_cbranch_scc1 PACE0_14%=
s_branch PACED0_14%=
PACE1_1%=:
s_sleep 1
s_memrealtime s[98:99]
s_waitcnt lgkmcnt(0)
s_sub_i32 s98, s98, s76
s_cmp_lt_i32 s98, 0
s_cbranch_scc1 PACE1_1%=
s_branch PACED1_1%=
PACE1_2%=:
s_sleep 1
s_memrealtime s[98:99]
s_waitcnt lgkmcnt(0)
s_sub_i32 s98, s98, s76
s_cmp_lt_i32 s98, 0
s_cbranch_scc1 PACE1_2%=
s_branch PACED1_2%=
PACE1_3%=:
s_sleep 1
s_memrealtime s[98:99]
s_waitcnt lgkmcnt(0)
s_sub_i32 s98, s98, s76
s_cmp_lt_i32 s98, 0
s_cbranch_scc1 PACE1_3%=
s_branch PACED1_3%=
PACE1_4%=:
s_sleep 1
s_memrealtime s[98:99]
s_waitcnt lgkmcnt(0)
s_sub_i32 s98, s98, s76
s_cmp_lt_i32 s98, 0
s_cbranch_scc1 PACE1_4%=
s_branch PACED1_4%=
PACE1_5%=:
s_sleep 1
s_memrealtime s[98:99]
s_waitcnt lgkmcnt(0)
s_sub_i32 s98, s98, s76
s_cmp_lt_i32 s98, 0
s_cbranch_scc1 PACE1_5%=
s_branch PACED1_5%=
PACE1_6%=:
s_sleep 1
s_memrealtime s[98:99]
s_waitcnt lgkmcnt(0)
s_sub_i32 s98, s98, s76
s_cmp_lt_i32 s98, 0
s_cbranch_scc1 PACE1_6%=
s_branch PACED1_6%=
PACE1_7%=:
s_sleep 1
s_memrealtime s[98:99]
s_waitcnt lgkmcnt(0)
s_sub_i32 s98, s98, s76
s_cmp_lt_i32 s98, 0
s_cbranch_scc1 PACE1_7%=
s_branch PACED1_7%=
PACE1_8%=:
s_sleep 1
s_memrealtime s[98:99]
s_waitcnt lgkmcnt(0)
s_sub_i32 s98, s98, s76
s_cmp_lt_i32 s98, 0
s_cbranch_scc1 PACE1_8%=
s_branch PACED1_8%=
PACE1_9%=:
s_sleep 1
s_memrealtime s[98:99]
s_waitcnt lgkmcnt(0)
s_sub_i32 s98, s98, s76
s_cmp_lt_i32 s98, 0
s_cbranch_scc1 PACE1_9%=
s_branch PACED1_9%=
PACE1_10%=:
s_sleep 1
s_memrealtime s[98:99]
s_waitcnt lgkmcnt(0)
s_sub_i32 s98, s98, s76
s_cmp_lt_i32 s98, 0
s_cbranch_scc1 PACE1_10%=
s_branch PACED1_10%=
PACE1_11%=:
s_sleep 1
s_memrealtime s[98:99]
s_waitcnt lgkmcnt(0)
s_sub_i32 s98, s98, s76
s_cmp_lt_i32 s98, 0
s_cbranch_scc1 PACE1_11%=
s_branch PACED1_11%=
PACE1_12%=:
s_sleep 1
s_memrealtime s[98:99]
s_waitcnt lgkmcnt(0)
s_sub_i32 s98, s98, s76
s_cmp_lt_i32 s98, 0
s_cbranch_scc1 PACE1_12%=
s_branch PACED1_12%=
PACE1_13%=:
s_sleep 1
s_memrealtime s[98:99]
s_waitcnt lgkmcnt(0)
s_sub_i32 s98, s98, s76
s_cmp_lt_i32 s98, 0
s_cbranch_scc1 PACE1_13%=
s_branch PACED1_13%=
PACE1_14%=:
s_sleep 1
s_memrealtime s[98:99]
s_waitcnt lgkmcnt(0)
s_sub_i32 s98, s98, s76
s_cmp_lt_i32 s98, 0
s_cbranch_scc1 PACE1_14%=
s_branch PACED1_14%=
SPIN0%=:
s_mov_b32 s77, 0x40000
SPL0%=:
buffer_load_dwordx2 v[222:223], off, s[60:63], s75 sc1
s_sub_u32 s77, s77, 1
s_cbranch_scc1 GOT0%=
s_waitcnt vmcnt(0)
v_cmp_ne_u64 vcc, s[96:97], v[222:223]
s_cbranch_vccz GOT0%=
s_sleep 2
s_branch SPL0%=
SPIN1%=:
s_mov_b32 s77, 0x40000
SPL1%=:
buffer_load_dwordx2 v[222:223], off, s[60:63], s75 sc1
s_sub_u32 s77, s77, 1
s_cbranch_scc1 GOT1%=
s_waitcnt vmcnt(0)
v_cmp_ne_u64 vcc, s[96:97], v[222:223]
s_cbranch_vccz GOT1%=
s_sleep 2
s_branch SPL1%=
SPIN2%=:
s_mov_b32 s77, 0x40000
SPL2%=:
buffer_load_dwordx2 v[222:223], off, s[60:63], s75 sc1
s_sub_u32 s77, s77, 1
s_cbranch_scc1 GOT2%=
s_waitcnt vmcnt(0)
v_cmp_ne_u64 vcc, s[96:97], v[222:223]
s_cbranch_vccz GOT2%=
s_sleep 2
s_branch SPL2%=
SPIN3%=:
s_mov_b32 s77, 0x40000
SPL3%=:
buffer_load_dwordx2 v[222:223], off, s[60:63], s75 sc1
s_sub_u32 s77, s77, 1
s_cbranch_scc1 GOT3%=
s_waitcnt vmcnt(0)
v_cmp_ne_u64 vcc, s[96:97], v[222:223]
s_cbranch_vccz GOT3%=
s_sleep 2
s_branch SPL3%=
SPIN4%=:
s_mov_b32 s77, 0x40000
SPL4%=:
buffer_load_dwordx2 v[222:223], off, s[60:63], s75 sc1
s_sub_u32 s77, s77, 1
s_cbranch_scc1 GOT4%=
s_waitcnt vmcnt(0)
v_cmp_ne_u64 vcc, s[96:97], v[222:223]
s_cbranch_vccz GOT4%=
s_sleep 2
s_branch SPL4%=
SPIN5%=:
s_mov_b32 s77, 0x40000
SPL5%=:
buffer_load_dwordx2 v[222:223], off, s[60:63], s75 sc1
s_sub_u32 s77, s77, 1
s_cbranch_scc1 GOT5%=
s_waitcnt vmcnt(0)
v_cmp_ne_u64 vcc, s[96:97], v[222:223]
s_cbranch_vccz GOT5%=
s_sleep 2
s_branch SPL5%=
)ASM"
R"ASM(SPIN6%=:
s_mov_b32 s77, 0x40000
SPL6%=:
buffer_load_dwordx2 v[222:223], off, s[60:63], s75 sc1
s_sub_u32 s77, s77, 1
s_cbranch_scc1 GOT6%=
s_waitcnt vmcnt(0)
v_cmp_ne_u64 vcc, s[96:97], v[222:223]
s_cbranch_vccz GOT6%=
s_sleep 2
s_branch SPL6%=
SLOWCALL0%=:
s_mov_b32 s80, 0
s_getpc_b64 s[78:79]
s_branch SLOW%=
v_rcp_f32 v232, v192
v_rcp_f32 v233, v196
v_rcp_f32 v234, v220
v_rcp_f32 v235, v224
v_rcp_f32 v236, v228
TR v,208,253,49152
TR v,210,253,49408
TR v,212,253,51200
TR v,214,253,51456
R4 a,240,251,0
R4 a,244,251,512
R4 a,248,251,2048
R4 a,252,251,2560
R4 v,160,251,4096
R4 v,164,251,4608
R4 v,168,251,6144
R4 v,172,251,6656
R4 v,176,251,8192
R4 v,180,251,8704
s_waitcnt lgkmcnt(0)
s_branch SLOWRET0%=
SLOWCALL1%=:
s_mov_b32 s80, 262144
s_getpc_b64 s[78:79]
s_branch SLOW%=
v_rcp_f32 v232, v192
v_rcp_f32 v233, v196
v_rcp_f32 v234, v220
v_rcp_f32 v235, v224
v_rcp_f32 v236, v228
TR a,240,247,0
TR a,242,247,2048
TR a,244,247,4096
TR a,246,247,6144
TR a,248,247,8192
TR a,250,247,10240
TR a,252,247,12288
TR a,254,247,14336
s_waitcnt lgkmcnt(0)
s_branch SLOWRET1%=
SLOW%=:
s_waitcnt vmcnt(0) lgkmcnt(0)
s_nop 7
v_mbcnt_lo_u32_b32 v123, -1, 0
v_mbcnt_hi_u32_b32 v123, -1, v123
s_lshl_b32 s31, s26, 17
s_add_u32 s31, s31, 1024
v_and_b32 v120, 15, v123
v_lshrrev_b32 v121, 5, v123
v_bfe_u32 v122, v123, 4, 1
v_lshrrev_b32 v121, 4, v123
v_lshlrev_b32 v121, 4, v121
v_lshl_add_u32 v232, v120, 8, v121
v_add_u32 v232, s31, v232
v_lshrrev_b32 v121, 4, v123
v_lshlrev_b32 v121, 10, v121
v_lshl_add_u32 v233, v120, 1, v121
v_add_u32 v233, s31, v233
v_add_u32 v234, 4096, v233
v_and_b32 v120, 3, v123
v_cmp_eq_u32 vcc, 0, v120
v_mov_b32 v121, 0x3f803f80
s_nop 0
v_cndmask_b32 v216, 0, v121, vcc
v_cndmask_b32 v217, 0, v121, vcc
v_cndmask_b32 v218, 0, v121, vcc
v_cndmask_b32 v219, 0, v121, vcc
v_mov_b32 v140, 0xff800000
v_mov_b32 v144, 0xff800000
v_mov_b32 v148, 0xff800000
v_mov_b32 v152, 0xff800000
v_mov_b32 v156, 0xff800000
s_mov_b32 s31, s80
s_mov_b32 s77, 32
SL1%=:
buffer_load_dwordx4 v[200:203], v232, s[40:43], s31 offen offset:0
buffer_load_dwordx4 v[204:207], v232, s[40:43], s31 offen offset:64
buffer_load_dwordx4 v[208:211], v232, s[40:43], s31 offen offset:128
buffer_load_dwordx4 v[212:215], v232, s[40:43], s31 offen offset:192
s_waitcnt vmcnt(0)
MZ v,80,v,200,v,0
MA v,80,v,204,v,20
MA v,80,v,208,v,40
MA v,80,v,212,v,60
MZ v,84,v,200,v,4
MA v,84,v,204,v,24
MA v,84,v,208,v,44
MA v,84,v,212,v,64
MZ v,88,v,200,v,8
MA v,88,v,204,v,28
MA v,88,v,208,v,48
MA v,88,v,212,v,68
MZ v,92,v,200,v,12
MA v,92,v,204,v,32
MA v,92,v,208,v,52
MA v,92,v,212,v,72
MZ v,96,v,200,v,16
MA v,96,v,204,v,36
MA v,96,v,208,v,56
MA v,96,v,212,v,76
s_nop 7
s_nop 1
v_max3_f32 v140, v140, v80, v81
v_max3_f32 v140, v140, v82, v83
v_max3_f32 v144, v144, v84, v85
v_max3_f32 v144, v144, v86, v87
v_max3_f32 v148, v148, v88, v89
v_max3_f32 v148, v148, v90, v91
v_max3_f32 v152, v152, v92, v93
v_max3_f32 v152, v152, v94, v95
v_max3_f32 v156, v156, v96, v97
v_max3_f32 v156, v156, v98, v99
s_add_u32 s31, s31, 4096
s_sub_u32 s77, s77, 1
s_cmp_lg_u32 s77, 0
s_cbranch_scc1 SL1%=
v_mov_b32 v141, v140
s_nop 1
v_permlane32_swap_b32 v140, v141
v_max_f32 v140, v140, v141
v_mov_b32 v141, v140
s_nop 1
v_permlane16_swap_b32 v140, v141
v_max_f32 v140, v140, v141
v_sub_f32 v140, 0, v140
v_mov_b32 v141, v140
v_mov_b32 v142, v140
v_mov_b32 v143, v140
v_mov_b32 v192, 0
v_mov_b32 v193, 0
v_mov_b32 v194, 0
v_mov_b32 v195, 0
v_mov_b32 v145, v144
s_nop 1
v_permlane32_swap_b32 v144, v145
v_max_f32 v144, v144, v145
v_mov_b32 v145, v144
s_nop 1
v_permlane16_swap_b32 v144, v145
v_max_f32 v144, v144, v145
v_sub_f32 v144, 0, v144
v_mov_b32 v145, v144
v_mov_b32 v146, v144
v_mov_b32 v147, v144
v_mov_b32 v196, 0
v_mov_b32 v197, 0
v_mov_b32 v198, 0
v_mov_b32 v199, 0
v_mov_b32 v149, v148
s_nop 1
v_permlane32_swap_b32 v148, v149
v_max_f32 v148, v148, v149
v_mov_b32 v149, v148
s_nop 1
v_permlane16_swap_b32 v148, v149
v_max_f32 v148, v148, v149
v_sub_f32 v148, 0, v148
v_mov_b32 v149, v148
v_mov_b32 v150, v148
v_mov_b32 v151, v148
v_mov_b32 v220, 0
v_mov_b32 v221, 0
v_mov_b32 v222, 0
v_mov_b32 v223, 0
v_mov_b32 v153, v152
s_nop 1
v_permlane32_swap_b32 v152, v153
v_max_f32 v152, v152, v153
v_mov_b32 v153, v152
s_nop 1
v_permlane16_swap_b32 v152, v153
v_max_f32 v152, v152, v153
v_sub_f32 v152, 0, v152
v_mov_b32 v153, v152
v_mov_b32 v154, v152
v_mov_b32 v155, v152
v_mov_b32 v224, 0
v_mov_b32 v225, 0
v_mov_b32 v226, 0
v_mov_b32 v227, 0
v_mov_b32 v157, v156
s_nop 1
v_permlane32_swap_b32 v156, v157
v_max_f32 v156, v156, v157
v_mov_b32 v157, v156
s_nop 1
v_permlane16_swap_b32 v156, v157
v_max_f32 v156, v156, v157
v_sub_f32 v156, 0, v156
v_mov_b32 v157, v156
v_mov_b32 v158, v156
v_mov_b32 v159, v156
v_mov_b32 v228, 0
v_mov_b32 v229, 0
v_mov_b32 v230, 0
v_mov_b32 v231, 0
v_accvgpr_write_b32 a0, 0
v_accvgpr_write_b32 a1, 0
v_accvgpr_write_b32 a2, 0
v_accvgpr_write_b32 a3, 0
v_accvgpr_write_b32 a4, 0
v_accvgpr_write_b32 a5, 0
v_accvgpr_write_b32 a6, 0
v_accvgpr_write_b32 a7, 0
v_accvgpr_write_b32 a8, 0
v_accvgpr_write_b32 a9, 0
v_accvgpr_write_b32 a10, 0
v_accvgpr_write_b32 a11, 0
v_accvgpr_write_b32 a12, 0
v_accvgpr_write_b32 a13, 0
v_accvgpr_write_b32 a14, 0
v_accvgpr_write_b32 a15, 0
v_accvgpr_write_b32 a16, 0
v_accvgpr_write_b32 a17, 0
v_accvgpr_write_b32 a18, 0
v_accvgpr_write_b32 a19, 0
v_accvgpr_write_b32 a20, 0
v_accvgpr_write_b32 a21, 0
v_accvgpr_write_b32 a22, 0
v_accvgpr_write_b32 a23, 0
v_accvgpr_write_b32 a24, 0
v_accvgpr_write_b32 a25, 0
v_accvgpr_write_b32 a26, 0
v_accvgpr_write_b32 a27, 0
v_accvgpr_write_b32 a28, 0
v_accvgpr_write_b32 a29, 0
v_accvgpr_write_b32 a30, 0
v_accvgpr_write_b32 a31, 0
v_accvgpr_write_b32 a32, 0
v_accvgpr_write_b32 a33, 0
v_accvgpr_write_b32 a34, 0
v_accvgpr_write_b32 a35, 0
v_accvgpr_write_b32 a36, 0
v_accvgpr_write_b32 a37, 0
v_accvgpr_write_b32 a38, 0
v_accvgpr_write_b32 a39, 0
v_accvgpr_write_b32 a40, 0
v_accvgpr_write_b32 a41, 0
v_accvgpr_write_b32 a42, 0
v_accvgpr_write_b32 a43, 0
v_accvgpr_write_b32 a44, 0
v_accvgpr_write_b32 a45, 0
v_accvgpr_write_b32 a46, 0
v_accvgpr_write_b32 a47, 0
v_accvgpr_write_b32 a48, 0
v_accvgpr_write_b32 a49, 0
v_accvgpr_write_b32 a50, 0
v_accvgpr_write_b32 a51, 0
v_accvgpr_write_b32 a52, 0
v_accvgpr_write_b32 a53, 0
v_accvgpr_write_b32 a54, 0
v_accvgpr_write_b32 a55, 0
v_accvgpr_write_b32 a56, 0
v_accvgpr_write_b32 a57, 0
v_accvgpr_write_b32 a58, 0
v_accvgpr_write_b32 a59, 0
v_accvgpr_write_b32 a60, 0
v_accvgpr_write_b32 a61, 0
v_accvgpr_write_b32 a62, 0
v_accvgpr_write_b32 a63, 0
v_accvgpr_write_b32 a64, 0
v_accvgpr_write_b32 a65, 0
v_accvgpr_write_b32 a66, 0
v_accvgpr_write_b32 a67, 0
v_accvgpr_write_b32 a68, 0
v_accvgpr_write_b32 a69, 0
v_accvgpr_write_b32 a70, 0
v_accvgpr_write_b32 a71, 0
v_accvgpr_write_b32 a72, 0
v_accvgpr_write_b32 a73, 0
v_accvgpr_write_b32 a74, 0
v_accvgpr_write_b32 a75, 0
v_accvgpr_write_b32 a76, 0
v_accvgpr_write_b32 a77, 0
v_accvgpr_write_b32 a78, 0
v_accvgpr_write_b32 a79, 0
v_accvgpr_write_b32 a80, 0
v_accvgpr_write_b32 a81, 0
v_accvgpr_write_b32 a82, 0
v_accvgpr_write_b32 a83, 0
v_accvgpr_write_b32 a84, 0
v_accvgpr_write_b32 a85, 0
v_accvgpr_write_b32 a86, 0
v_accvgpr_write_b32 a87, 0
v_accvgpr_write_b32 a88, 0
v_accvgpr_write_b32 a89, 0
v_accvgpr_write_b32 a90, 0
v_accvgpr_write_b32 a91, 0
v_accvgpr_write_b32 a92, 0
v_accvgpr_write_b32 a93, 0
v_accvgpr_write_b32 a94, 0
v_accvgpr_write_b32 a95, 0
v_accvgpr_write_b32 a96, 0
v_accvgpr_write_b32 a97, 0
v_accvgpr_write_b32 a98, 0
v_accvgpr_write_b32 a99, 0
v_accvgpr_write_b32 a100, 0
v_accvgpr_write_b32 a101, 0
v_accvgpr_write_b32 a102, 0
v_accvgpr_write_b32 a103, 0
v_accvgpr_write_b32 a104, 0
v_accvgpr_write_b32 a105, 0
v_accvgpr_write_b32 a106, 0
v_accvgpr_write_b32 a107, 0
v_accvgpr_write_b32 a108, 0
v_accvgpr_write_b32 a109, 0
v_accvgpr_write_b32 a110, 0
v_accvgpr_write_b32 a111, 0
v_accvgpr_write_b32 a112, 0
v_accvgpr_write_b32 a113, 0
v_accvgpr_write_b32 a114, 0
v_accvgpr_write_b32 a115, 0
v_accvgpr_write_b32 a116, 0
v_accvgpr_write_b32 a117, 0
v_accvgpr_write_b32 a118, 0
v_accvgpr_write_b32 a119, 0
v_accvgpr_write_b32 a120, 0
v_accvgpr_write_b32 a121, 0
v_accvgpr_write_b32 a122, 0
v_accvgpr_write_b32 a123, 0
v_accvgpr_write_b32 a124, 0
v_accvgpr_write_b32 a125, 0
v_accvgpr_write_b32 a126, 0
v_accvgpr_write_b32 a127, 0
v_accvgpr_write_b32 a128, 0
v_accvgpr_write_b32 a129, 0
v_accvgpr_write_b32 a130, 0
v_accvgpr_write_b32 a131, 0
v_accvgpr_write_b32 a132, 0
v_accvgpr_write_b32 a133, 0
v_accvgpr_write_b32 a134, 0
v_accvgpr_write_b32 a135, 0
v_accvgpr_write_b32 a136, 0
v_accvgpr_write_b32 a137, 0
v_accvgpr_write_b32 a138, 0
v_accvgpr_write_b32 a139, 0
v_accvgpr_write_b32 a140, 0
v_accvgpr_write_b32 a141, 0
v_accvgpr_write_b32 a142, 0
v_accvgpr_write_b32 a143, 0
v_accvgpr_write_b32 a144, 0
v_accvgpr_write_b32 a145, 0
v_accvgpr_write_b32 a146, 0
v_accvgpr_write_b32 a147, 0
v_accvgpr_write_b32 a148, 0
v_accvgpr_write_b32 a149, 0
v_accvgpr_write_b32 a150, 0
v_accvgpr_write_b32 a151, 0
v_accvgpr_write_b32 a152, 0
v_accvgpr_write_b32 a153, 0
v_accvgpr_write_b32 a154, 0
v_accvgpr_write_b32 a155, 0
v_accvgpr_write_b32 a156, 0
v_accvgpr_write_b32 a157, 0
v_accvgpr_write_b32 a158, 0
v_accvgpr_write_b32 a159, 0
s_mov_b32 s31, s80
s_mov_b32 s77, 16
SL2%=:
s_add_u32 s72, s31, 4096
buffer_load_dwordx4 v[200:203], v232, s[40:43], s31 offen offset:0
buffer_load_dwordx4 v[204:207], v232, s[40:43], s31 offen offset:64
buffer_load_dwordx4 v[208:211], v232, s[40:43], s31 offen offset:128
buffer_load_dwordx4 v[212:215], v232, s[40:43], s31 offen offset:192
buffer_load_dwordx4 a[240:243], v232, s[40:43], s72 offen offset:0
buffer_load_dwordx4 a[244:247], v232, s[40:43], s72 offen offset:64
buffer_load_dwordx4 a[248:251], v232, s[40:43], s72 offen offset:128
buffer_load_dwordx4 a[252:255], v232, s[40:43], s72 offen offset:192
buffer_load_ushort v80, v233, s[44:47], s31 offen offset:0
buffer_load_ushort v81, v233, s[44:47], s31 offen offset:256
buffer_load_ushort v82, v233, s[44:47], s31 offen offset:512
buffer_load_ushort v83, v233, s[44:47], s31 offen offset:768
buffer_load_ushort v84, v234, s[44:47], s31 offen offset:0
buffer_load_ushort v85, v234, s[44:47], s31 offen offset:256
buffer_load_ushort v86, v234, s[44:47], s31 offen offset:512
buffer_load_ushort v87, v234, s[44:47], s31 offen offset:768
buffer_load_ushort v88, v233, s[44:47], s31 offen offset:32
buffer_load_ushort v89, v233, s[44:47], s31 offen offset:288
buffer_load_ushort v90, v233, s[44:47], s31 offen offset:544
buffer_load_ushort v91, v233, s[44:47], s31 offen offset:800
buffer_load_ushort v92, v234, s[44:47], s31 offen offset:32
buffer_load_ushort v93, v234, s[44:47], s31 offen offset:288
buffer_load_ushort v94, v234, s[44:47], s31 offen offset:544
buffer_load_ushort v95, v234, s[44:47], s31 offen offset:800
buffer_load_ushort v96, v233, s[44:47], s31 offen offset:64
buffer_load_ushort v97, v233, s[44:47], s31 offen offset:320
buffer_load_ushort v98, v233, s[44:47], s31 offen offset:576
buffer_load_ushort v99, v233, s[44:47], s31 offen offset:832
buffer_load_ushort v100, v234, s[44:47], s31 offen offset:64
buffer_load_ushort v101, v234, s[44:47], s31 offen offset:320
buffer_load_ushort v102, v234, s[44:47], s31 offen offset:576
buffer_load_ushort v103, v234, s[44:47], s31 offen offset:832
buffer_load_ushort v104, v233, s[44:47], s31 offen offset:96
buffer_load_ushort v105, v233, s[44:47], s31 offen offset:352
buffer_load_ushort v106, v233, s[44:47], s31 offen offset:608
buffer_load_ushort v107, v233, s[44:47], s31 offen offset:864
buffer_load_ushort v108, v234, s[44:47], s31 offen offset:96
buffer_load_ushort v109, v234, s[44:47], s31 offen offset:352
buffer_load_ushort v110, v234, s[44:47], s31 offen offset:608
buffer_load_ushort v111, v234, s[44:47], s31 offen offset:864
s_waitcnt vmcnt(0)
v_lshl_or_b32 v160, v81, 16, v80
v_lshl_or_b32 v161, v83, 16, v82
v_lshl_or_b32 v162, v85, 16, v84
v_lshl_or_b32 v163, v87, 16, v86
v_lshl_or_b32 v164, v89, 16, v88
v_lshl_or_b32 v165, v91, 16, v90
v_lshl_or_b32 v166, v93, 16, v92
v_lshl_or_b32 v167, v95, 16, v94
v_lshl_or_b32 v168, v97, 16, v96
)ASM"
R"ASM(v_lshl_or_b32 v169, v99, 16, v98
v_lshl_or_b32 v170, v101, 16, v100
v_lshl_or_b32 v171, v103, 16, v102
v_lshl_or_b32 v172, v105, 16, v104
v_lshl_or_b32 v173, v107, 16, v106
v_lshl_or_b32 v174, v109, 16, v108
v_lshl_or_b32 v175, v111, 16, v110
buffer_load_ushort v80, v233, s[44:47], s31 offen offset:128
buffer_load_ushort v81, v233, s[44:47], s31 offen offset:384
buffer_load_ushort v82, v233, s[44:47], s31 offen offset:640
buffer_load_ushort v83, v233, s[44:47], s31 offen offset:896
buffer_load_ushort v84, v234, s[44:47], s31 offen offset:128
buffer_load_ushort v85, v234, s[44:47], s31 offen offset:384
buffer_load_ushort v86, v234, s[44:47], s31 offen offset:640
buffer_load_ushort v87, v234, s[44:47], s31 offen offset:896
buffer_load_ushort v88, v233, s[44:47], s31 offen offset:160
buffer_load_ushort v89, v233, s[44:47], s31 offen offset:416
buffer_load_ushort v90, v233, s[44:47], s31 offen offset:672
buffer_load_ushort v91, v233, s[44:47], s31 offen offset:928
buffer_load_ushort v92, v234, s[44:47], s31 offen offset:160
buffer_load_ushort v93, v234, s[44:47], s31 offen offset:416
buffer_load_ushort v94, v234, s[44:47], s31 offen offset:672
buffer_load_ushort v95, v234, s[44:47], s31 offen offset:928
buffer_load_ushort v96, v233, s[44:47], s31 offen offset:192
buffer_load_ushort v97, v233, s[44:47], s31 offen offset:448
buffer_load_ushort v98, v233, s[44:47], s31 offen offset:704
buffer_load_ushort v99, v233, s[44:47], s31 offen offset:960
buffer_load_ushort v100, v234, s[44:47], s31 offen offset:192
buffer_load_ushort v101, v234, s[44:47], s31 offen offset:448
buffer_load_ushort v102, v234, s[44:47], s31 offen offset:704
buffer_load_ushort v103, v234, s[44:47], s31 offen offset:960
buffer_load_ushort v104, v233, s[44:47], s31 offen offset:224
buffer_load_ushort v105, v233, s[44:47], s31 offen offset:480
buffer_load_ushort v106, v233, s[44:47], s31 offen offset:736
buffer_load_ushort v107, v233, s[44:47], s31 offen offset:992
buffer_load_ushort v108, v234, s[44:47], s31 offen offset:224
buffer_load_ushort v109, v234, s[44:47], s31 offen offset:480
buffer_load_ushort v110, v234, s[44:47], s31 offen offset:736
buffer_load_ushort v111, v234, s[44:47], s31 offen offset:992
s_waitcnt vmcnt(0)
v_lshl_or_b32 v176, v81, 16, v80
v_lshl_or_b32 v177, v83, 16, v82
v_lshl_or_b32 v178, v85, 16, v84
v_lshl_or_b32 v179, v87, 16, v86
v_lshl_or_b32 v180, v89, 16, v88
v_lshl_or_b32 v181, v91, 16, v90
v_lshl_or_b32 v182, v93, 16, v92
v_lshl_or_b32 v183, v95, 16, v94
v_lshl_or_b32 v184, v97, 16, v96
v_lshl_or_b32 v185, v99, 16, v98
v_lshl_or_b32 v186, v101, 16, v100
v_lshl_or_b32 v187, v103, 16, v102
v_lshl_or_b32 v188, v105, 16, v104
v_lshl_or_b32 v189, v107, 16, v106
v_lshl_or_b32 v190, v109, 16, v108
v_lshl_or_b32 v191, v111, 16, v110
s_nop 1
MC v,80,v,200,v,0,140
MA v,80,v,204,v,20
MA v,80,v,208,v,40
MA v,80,v,212,v,60
MC v,84,a,240,v,0,140
MA v,84,a,244,v,20
MA v,84,a,248,v,40
MA v,84,a,252,v,60
MC v,88,v,200,v,4,144
MA v,88,v,204,v,24
MA v,88,v,208,v,44
MA v,88,v,212,v,64
MC v,92,a,240,v,4,144
MA v,92,a,244,v,24
MA v,92,a,248,v,44
MA v,92,a,252,v,64
MC v,96,v,200,v,8,148
MA v,96,v,204,v,28
MA v,96,v,208,v,48
MA v,96,v,212,v,68
MC v,100,a,240,v,8,148
MA v,100,a,244,v,28
MA v,100,a,248,v,48
MA v,100,a,252,v,68
MC v,104,v,200,v,12,152
MA v,104,v,204,v,32
MA v,104,v,208,v,52
MA v,104,v,212,v,72
MC v,108,a,240,v,12,152
MA v,108,a,244,v,32
MA v,108,a,248,v,52
MA v,108,a,252,v,72
MC v,112,v,200,v,16,156
MA v,112,v,204,v,36
MA v,112,v,208,v,56
MA v,112,v,212,v,76
MC v,116,a,240,v,16,156
MA v,116,a,244,v,36
MA v,116,a,248,v,56
MA v,116,a,252,v,76
s_nop 7
s_nop 1
v_exp_f32 v80, v80
v_exp_f32 v81, v81
v_exp_f32 v82, v82
v_exp_f32 v83, v83
v_exp_f32 v84, v84
v_exp_f32 v85, v85
v_exp_f32 v86, v86
v_exp_f32 v87, v87
v_exp_f32 v88, v88
v_exp_f32 v89, v89
v_exp_f32 v90, v90
v_exp_f32 v91, v91
v_exp_f32 v92, v92
v_exp_f32 v93, v93
v_exp_f32 v94, v94
v_exp_f32 v95, v95
v_exp_f32 v96, v96
v_exp_f32 v97, v97
v_exp_f32 v98, v98
v_exp_f32 v99, v99
v_exp_f32 v100, v100
v_exp_f32 v101, v101
v_exp_f32 v102, v102
v_exp_f32 v103, v103
v_exp_f32 v104, v104
v_exp_f32 v105, v105
v_exp_f32 v106, v106
v_exp_f32 v107, v107
v_exp_f32 v108, v108
v_exp_f32 v109, v109
v_exp_f32 v110, v110
v_exp_f32 v111, v111
v_exp_f32 v112, v112
v_exp_f32 v113, v113
v_exp_f32 v114, v114
v_exp_f32 v115, v115
v_exp_f32 v116, v116
v_exp_f32 v117, v117
v_exp_f32 v118, v118
v_exp_f32 v119, v119
s_nop 0
CV 120,80,81
CV 121,82,83
CV 122,84,85
CV 123,86,87
CV 124,88,89
CV 125,90,91
CV 126,92,93
CV 127,94,95
CV 128,96,97
CV 129,98,99
CV 130,100,101
CV 131,102,103
CV 132,104,105
CV 133,106,107
CV 134,108,109
CV 135,110,111
CV 136,112,113
CV 137,114,115
CV 138,116,117
CV 139,118,119
s_nop 1
MA a,0,v,160,v,120
MA a,20,v,164,v,120
MA a,40,v,168,v,120
MA a,60,v,172,v,120
MA a,80,v,176,v,120
MA a,100,v,180,v,120
MA a,120,v,184,v,120
MA a,140,v,188,v,120
MA v,192,v,216,v,120
MA a,4,v,160,v,124
MA a,24,v,164,v,124
MA a,44,v,168,v,124
MA a,64,v,172,v,124
MA a,84,v,176,v,124
MA a,104,v,180,v,124
MA a,124,v,184,v,124
MA a,144,v,188,v,124
MA v,196,v,216,v,124
MA a,8,v,160,v,128
MA a,28,v,164,v,128
MA a,48,v,168,v,128
MA a,68,v,172,v,128
MA a,88,v,176,v,128
MA a,108,v,180,v,128
MA a,128,v,184,v,128
MA a,148,v,188,v,128
MA v,220,v,216,v,128
MA a,12,v,160,v,132
MA a,32,v,164,v,132
MA a,52,v,168,v,132
MA a,72,v,172,v,132
MA a,92,v,176,v,132
MA a,112,v,180,v,132
MA a,132,v,184,v,132
MA a,152,v,188,v,132
MA v,224,v,216,v,132
MA a,16,v,160,v,136
MA a,36,v,164,v,136
MA a,56,v,168,v,136
MA a,76,v,172,v,136
MA a,96,v,176,v,136
MA a,116,v,180,v,136
MA a,136,v,184,v,136
MA a,156,v,188,v,136
MA v,228,v,216,v,136
s_nop 7
s_nop 1
s_add_u32 s31, s31, 8192
s_sub_u32 s77, s77, 1
s_cmp_lg_u32 s77, 0
s_cbranch_scc1 SL2%=
s_add_u32 s78, s78, 4
s_addc_u32 s79, s79, 0
s_setpc_b64 s[78:79]
CODEEND%=:
.purgem MZ
.purgem MA
.purgem MC
.purgem R4
.purgem TR
.purgem CV
.purgem AR
.purgem AW
)ASM"
    :: "s"(__builtin_amdgcn_kernarg_segment_ptr()), "s"(wg), "s"(wave), "s"((unsigned)(size_t)lds)
    : "v0","v1","v2","v3","v4","v5","v6","v7","v8","v9","v10","v11","v12","v13","v14","v15","v16","v17","v18","v19","v20","v21","v22","v23","v24","v25","v26","v27","v28","v29","v30","v31","v32","v33","v34","v35","v36","v37","v38","v39","v40","v41","v42","v43","v44","v45","v46","v47","v48","v49","v50","v51","v52","v53","v54","v55","v56","v57","v58","v59","v60","v61","v62","v63","v64","v65","v66","v67","v68","v69","v70","v71","v72","v73","v74","v75","v76","v77","v78","v79","v80","v81","v82","v83","v84","v85","v86","v87","v88","v89","v90","v91","v92","v93","v94","v95","v96","v97","v98","v99","v100","v101","v102","v103","v104","v105","v106","v107","v108","v109","v110","v111","v112","v113","v114","v115","v116","v117","v118","v119","v120","v121","v122","v123","v124","v125","v126","v127","v128","v129","v130","v131","v132","v133","v134","v135","v136","v137","v138","v139","v140","v141","v142","v143","v144","v145","v146","v147","v148","v149","v150","v151","v152","v153","v154","v155","v156","v157","v158","v159","v160","v161","v162","v163","v164","v165","v166","v167","v168","v169","v170","v171","v172","v173","v174","v175","v176","v177","v178","v179","v180","v181","v182","v183","v184","v185","v186","v187","v188","v189","v190","v191","v192","v193","v194","v195","v196","v197","v198","v199","v200","v201","v202","v203","v204","v205","v206","v207","v208","v209","v210","v211","v212","v213","v214","v215","v216","v217","v218","v219","v220","v221","v222","v223","v224","v225","v226","v227","v228","v229","v230","v231","v232","v233","v234","v235","v236","v237","v238","v239","v240","v241","v242","v243","v244","v245","v246","v247","v248","v249","v250","v251","v252","v253","v254","v255","a0","a1","a2","a3","a4","a5","a6","a7","a8","a9","a10","a11","a12","a13","a14","a15","a16","a17","a18","a19","a20","a21","a22","a23","a24","a25","a26","a27","a28","a29","a30","a31","a32","a33","a34","a35","a36","a37","a38","a39","a40","a41","a42","a43","a44","a45","a46","a47","a48","a49","a50","a51","a52","a53","a54","a55","a56","a57","a58","a59","a60","a61","a62","a63","a64","a65","a66","a67","a68","a69","a70","a71","a72","a73","a74","a75","a76","a77","a78","a79","a80","a81","a82","a83","a84","a85","a86","a87","a88","a89","a90","a91","a92","a93","a94","a95","a96","a97","a98","a99","a100","a101","a102","a103","a104","a105","a106","a107","a108","a109","a110","a111","a112","a113","a114","a115","a116","a117","a118","a119","a120","a121","a122","a123","a124","a125","a126","a127","a128","a129","a130","a131","a132","a133","a134","a135","a136","a137","a138","a139","a140","a141","a142","a143","a144","a145","a146","a147","a148","a149","a150","a151","a152","a153","a154","a155","a156","a157","a158","a159","a160","a161","a162","a163","a164","a165","a166","a167","a168","a169","a170","a171","a172","a173","a174","a175","a176","a177","a178","a179","a180","a181","a182","a183","a184","a185","a186","a187","a188","a189","a190","a191","a192","a193","a194","a195","a196","a197","a198","a199","a200","a201","a202","a203","a204","a205","a206","a207","a208","a209","a210","a211","a212","a213","a214","a215","a216","a217","a218","a219","a220","a221","a222","a223","a224","a225","a226","a227","a228","a229","a230","a231","a232","a233","a234","a235","a236","a237","a238","a239","a240","a241","a242","a243","a244","a245","a246","a247","a248","a249","a250","a251","a252","a253","a254","a255","s8","s9","s10","s11","s12","s13","s14","s15","s16","s17","s18","s19","s20","s21","s22","s23","s24","s25","s26","s27","s28","s29","s30","s31","s32","s33","s34","s35","s36","s37","s38","s39","s40","s41","s42","s43","s44","s45","s46","s47","s48","s49","s50","s51","s52","s53","s54","s55","s56","s57","s58","s59","s60","s61","s62","s63","s64","s65","s66","s67","s68","s69","s70","s71","s72","s73","s74","s75","s76","s77","s78","s79","s80","s81","s82","s83","s84","s85","s86","s87","s88","s89","s90","s91","s92","s93","s94","s95","s96","s97","s98","s99","vcc","scc","memory");
}

extern "C" void kernel_launch(void* const* d_in, const int* in_sizes, int n_in, void* d_out, int out_size, void* d_ws, size_t ws_size,
                              hipStream_t stream) {
  fused_mha_pair_kernel<<<256, 256, 0, stream>>>(d_in[0], d_in[1], d_in[2], d_in[3], d_in[4], d_in[5], d_in[6], d_in[7], d_in[8], d_in[9], d_out, d_ws);
}
```
